```python
import torch

def _softmax(x, axis=-1):
    return torch.softmax(x, dim=axis)
def _gelu(x):
    return torch.nn.functional.gelu(x, approximate="tanh")
def _silu(x):
    return torch.nn.functional.silu(x)
def _sigmoid(x):
    return torch.sigmoid(x)
def _layernorm(x, axis=-1, eps=1e-5):
    m = x.mean(dim=axis, keepdim=True)
    v = x.var(dim=axis, keepdim=True, unbiased=False)
    return (x - m) / torch.sqrt(v + eps)
def _scatter_add(tgt, idx, src, axis=0):
    return tgt.clone().index_add_(axis, idx, src)
def _segment_sum(src, seg, n):
    out = torch.zeros((n,) + src.shape[1:], dtype=src.dtype)
    return out.index_add_(0, seg, src)
def _pad(x, pw):
    flat = tuple(v for pair in reversed(pw) for v in pair)
    return torch.nn.functional.pad(x, flat)

kernel_name = 'gated_mix__sliding_local_dilated__glu_gated_glu__prompt'

def setup_inputs():

    _w = (lambda a: (lambda t: t.to(torch.bfloat16) if t.is_floating_point() else t)(torch.as_tensor(a)))
    def _A_setup():
        B, S, D = 1, 2048, 448
        return {
            "x": _w((lambda *s: torch.randn(*s, dtype=torch.bfloat16) * 0.05)(B, S, D)),
            "wqkv": _w((lambda *s: torch.randn(*s, dtype=torch.bfloat16) * 0.8)(D, 3 * D)),
            "wo": _w((lambda *s: torch.randn(*s, dtype=torch.bfloat16) * 0.05)(D, D)),
        }
    _A_in = _A_setup()

    def _B_setup():
        B, S, D, F = 1, 2048, 448, 1792
        return {
            "x": _w((lambda *s: torch.randn(*s, dtype=torch.bfloat16) * 0.05)(B, S, D)),
            "w_gate": _w((lambda *s: torch.randn(*s, dtype=torch.bfloat16) * 1.6)(D, F)),
            "w_up": _w((lambda *s: torch.randn(*s, dtype=torch.bfloat16) * 0.1)(D, F)),
            "w_down": _w((lambda *s: torch.randn(*s, dtype=torch.bfloat16) * 0.1)(F, D)),
        }
    _B_in = _B_setup()

    return {
        "A_x": _A_in["x"],
        "A_wqkv": _A_in["wqkv"],
        "A_wo": _A_in["wo"],
        "B_x": _B_in["x"],
        "B_w_gate": _B_in["w_gate"],
        "B_w_up": _B_in["w_up"],
        "B_w_down": _B_in["w_down"],
        "gate_w": _w((lambda *s: torch.randn(*s, dtype=torch.bfloat16) * 0.05)(448) * 16.0),
    }

def reference(A_x, A_wqkv, A_wo, B_x, B_w_gate, B_w_up, B_w_down, gate_w):
    def _A_ref(x, wqkv, wo):
        B, S, D = x.shape
        H = 16; Dh = D // H
        W, dil = 8, 2
        qkv = (lambda x, *d: x.permute(*d))((x @ wqkv).reshape(B, S, 3, H, Dh), 2, 0, 3, 1, 4)
        q, k, v = qkv[0], qkv[1], qkv[2]
        scale = Dh ** -0.5
        pos = torch.arange(S)
        off = torch.arange(W) * dil
        idx = pos[:, None] - off[None, :]
        mask = idx >= 0
        idx_c = torch.where(mask, idx, 0).reshape(-1)
        kg = k[:, :, idx_c, :].reshape(B, H, S, W, Dh)
        vg = v[:, :, idx_c, :].reshape(B, H, S, W, Dh)
        logits = torch.einsum("bhsd,bhswd->bhsw", q, kg) * scale
        logits = torch.where(mask[None, None, :, :], logits, -1e9)
        attn = _softmax(logits, axis=-1)
        o = torch.einsum("bhsw,bhswd->bhsd", attn, vg)
        return (lambda x, *d: x.permute(*d))(o, 0, 2, 1, 3).reshape(B, S, D) @ wo

    def _B_ref(x, w_gate, w_up, w_down):
        g = torch.einsum("bsd,df->bsf", x, w_gate)
        u = torch.einsum("bsd,df->bsf", x, w_up)
        h = _sigmoid(g) * u
        return torch.einsum("bsf,fd->bsd", h, w_down)

    x = A_x
    g = _sigmoid((x * gate_w).sum(-1, keepdims=True))
    a = _A_ref(x, A_wqkv, A_wo)
    b = _B_ref(x, B_w_gate, B_w_up, B_w_down)
    return g * a + (1 - g) * b
```

```cpp
#include <hip/hip_runtime.h>
#include <hip/hip_bf16.h>
typedef unsigned short u16;
__device__ __forceinline__ float bf2f(u16 v){ return __uint_as_float(((unsigned)v)<<16); }
__device__ __forceinline__ u16 f2bf(float f){ __hip_bfloat16 b = __float2bfloat16(f); return *reinterpret_cast<u16*>(&b); }
constexpr int S=2048, D=448, F=1792, H=16, DH=28;

__global__ void gemm_naive(const u16* A, const u16* B, u16* C, int M, int N, int K){
  int n = blockIdx.x*blockDim.x+threadIdx.x; int m = blockIdx.y;
  if(n>=N) return;
  float acc=0.f;
  for(int k=0;k<K;++k) acc += bf2f(A[m*K+k])*bf2f(B[(long)k*N+n]);
  C[(long)m*N+n]=f2bf(acc);
}
__global__ void attn_naive(const u16* qkv, u16* o){
  int s = blockIdx.x; int h = threadIdx.x; if(h>=H) return;
  float lg[8]; float mx=-1e30f;
  const float scale = rsqrtf((float)DH);
  #pragma unroll
  for(int w=0;w<8;++w){ int p=s-2*w; float a=-1e9f; if(p>=0){ a=0.f; for(int d=0;d<DH;++d) a+=bf2f(qkv[s*3*D+h*DH+d])*bf2f(qkv[p*3*D+D+h*DH+d]); a*=scale;} lg[w]=a; mx=fmaxf(mx,a);}
  float sum=0.f;
  #pragma unroll
  for(int w=0;w<8;++w){ lg[w]=__expf(lg[w]-mx); sum+=lg[w]; }
  float inv=1.f/sum;
  for(int d=0;d<DH;++d){ float a=0.f;
    #pragma unroll
    for(int w=0;w<8;++w){ int p=s-2*w; if(p<0)p=0; a+=lg[w]*inv*bf2f(qkv[p*3*D+2*D+h*DH+d]); }
    o[s*D+h*DH+d]=f2bf(a);}
}
__global__ void glu_naive(const u16* g, const u16* u, u16* h, int n){
  int i=blockIdx.x*blockDim.x+threadIdx.x; if(i>=n) return;
  float gv=bf2f(g[i]); h[i]=f2bf(bf2f(u[i])/(1.f+__expf(-gv)));
}
__global__ void mix_naive(const u16* x, const u16* gw, const u16* a, const u16* b, u16* out){
  int s=blockIdx.x; __shared__ float sg;
  if(threadIdx.x==0){ float t=0.f; for(int d=0;d<D;++d) t+=bf2f(x[s*D+d])*bf2f(gw[d]); sg=1.f/(1.f+__expf(-t)); }
  __syncthreads();
  for(int d=threadIdx.x;d<D;d+=blockDim.x) out[s*D+d]=f2bf(sg*bf2f(a[s*D+d])+(1.f-sg)*bf2f(b[s*D+d]));
}
extern "C" void kernel_launch(void* const* d_in, const int* in_sizes, int n_in,
                              void* d_out, int out_size, void* d_ws, size_t ws_size,
                              hipStream_t stream) {
  const u16* x=(const u16*)d_in[0]; const u16* wqkv=(const u16*)d_in[1]; const u16* wo=(const u16*)d_in[2];
  const u16* wg=(const u16*)d_in[4]; const u16* wu=(const u16*)d_in[5]; const u16* wd=(const u16*)d_in[6]; const u16* gw=(const u16*)d_in[7];
  u16* ws=(u16*)d_ws;
  u16* qkv=ws; u16* o=qkv+S*3*D; u16* g=o+S*D; u16* u=g+S*F; u16* h=u+S*F; u16* a=h+S*F; u16* b=a+S*D;
  gemm_naive<<<dim3((3*D+255)/256,S),256,0,stream>>>(x,wqkv,qkv,S,3*D,D);
  gemm_naive<<<dim3((F+255)/256,S),256,0,stream>>>(x,wg,g,S,F,D);
  gemm_naive<<<dim3((F+255)/256,S),256,0,stream>>>(x,wu,u,S,F,D);
  attn_naive<<<S,64,0,stream>>>(qkv,o);
  glu_naive<<<(S*F+255)/256,256,0,stream>>>(g,u,h,S*F);
  gemm_naive<<<dim3((D+255)/256,S),256,0,stream>>>(o,wo,a,S,D,D);
  gemm_naive<<<dim3((D+255)/256,S),256,0,stream>>>(h,wd,b,S,D,F);
  mix_naive<<<S,256,0,stream>>>(x,gw,a,b,(u16*)d_out);
}
```

```cpp
#include <hip/hip_runtime.h>
#include <hip/hip_bf16.h>

typedef unsigned long long u64;
typedef unsigned u32;
typedef __bf16 bf16x8 __attribute__((ext_vector_type(8)));
typedef __bf16 bf16x2 __attribute__((ext_vector_type(2)));
typedef short s16x4 __attribute__((ext_vector_type(4)));
typedef u32 u32x4 __attribute__((ext_vector_type(4)));
typedef u32 u32x2 __attribute__((ext_vector_type(2)));
typedef float f32x16 __attribute__((ext_vector_type(16)));
typedef float f32x4 __attribute__((ext_vector_type(4)));
typedef __amdgpu_buffer_rsrc_t rsrc_t;
#define LAS __attribute__((address_space(3)))
#define GAS __attribute__((address_space(1)))
#define DI __device__ __forceinline__
#define SCHED() __builtin_amdgcn_sched_barrier(0)

constexpr u32 WS_E = 0;
constexpr u32 WS_FLAG = 4096;
constexpr u32 WS_OMG = WS_FLAG + 3 * 64 * 256;
constexpr u32 WS_K = 65536;
constexpr u32 KV_T = 28672;
constexpr u32 WS_V = WS_K + 64 * KV_T;
constexpr u32 WS_A2 = WS_V + 64 * KV_T;
constexpr u32 A2_T = 143360;
constexpr u32 WS_END = WS_A2 + 64 * A2_T;
constexpr u32 OOB = 0xFFFFFF00u;
constexpr int SC1 = 16;
constexpr int SLOT = 8192;

DI rsrc_t mk(const void* p, u32 bytes) { return __builtin_amdgcn_make_buffer_rsrc((void*)p, (short)0, (int)bytes, 0x00020000); }
DI void dma(u32 m0v, u32 voff, rsrc_t rs, u32 soff) {
  asm volatile("s_mov_b32 m0, %0\n\ts_nop 0\n\tbuffer_load_dwordx4 %1, %2, %3 offen lds" ::"s"(m0v), "v"(voff), "s"(rs), "s"(soff) : "memory");
}
DI u32x4 ld128_sc1(u32 voff, rsrc_t rs, u32 soff) {
  u32x4 v;
  asm volatile("buffer_load_dwordx4 %0, %1, %2, %3 offen sc1" : "=v"(v) : "v"(voff), "s"(rs), "s"(soff) : "memory");
  return v;
}
DI u32x2 ld64_sc1(u32 voff, rsrc_t rs, u32 soff) {
  u32x2 v;
  asm volatile("buffer_load_dwordx2 %0, %1, %2, %3 offen sc1" : "=v"(v) : "v"(voff), "s"(rs), "s"(soff) : "memory");
  return v;
}
DI u32 ld32_sc1(u32 voff, rsrc_t rs, u32 soff) {
  u32 v;
  asm volatile("buffer_load_dword %0, %1, %2, %3 offen sc1" : "=v"(v) : "v"(voff), "s"(rs), "s"(soff) : "memory");
  return v;
}
template <int N> DI void wait_vm() { asm volatile("s_waitcnt vmcnt(%0)" ::"n"(N > 63 ? 63 : N) : "memory"); }
template <int N, class T> DI void wait_vm_for(T& v) { asm volatile("s_waitcnt vmcnt(%1)" : "+v"(v) : "n"(N > 63 ? 63 : N) : "memory"); }
template <int N> DI void sync_vm() { asm volatile("s_waitcnt vmcnt(%0) lgkmcnt(0)\n\ts_barrier" ::"n"(N > 63 ? 63 : N) : "memory"); }

DI u32x4 trfrag(LAS char* p) {
  s16x4 lo = __builtin_amdgcn_ds_read_tr16_b64_v4i16((LAS s16x4*)p);
  s16x4 hi = __builtin_amdgcn_ds_read_tr16_b64_v4i16((LAS s16x4*)(p + 512));
  u32x2 a = __builtin_bit_cast(u32x2, lo), b = __builtin_bit_cast(u32x2, hi);
  return u32x4{a.x, a.y, b.x, b.y};
}
DI f32x16 mfma(u32x4 a, u32x4 b, f32x16 c) {
  return __builtin_amdgcn_mfma_f32_32x32x16_bf16(__builtin_bit_cast(bf16x8, a), __builtin_bit_cast(bf16x8, b), c, 0, 0, 0);
}
DI u32 pk(float a, float b) { bf16x2 c; c[0] = (__bf16)a; c[1] = (__bf16)b; return __builtin_bit_cast(u32, c); }
DI u32x4 pk8(const f32x16& v, int s) { return u32x4{pk(v[8 * s], v[8 * s + 1]), pk(v[8 * s + 2], v[8 * s + 3]), pk(v[8 * s + 4], v[8 * s + 5]), pk(v[8 * s + 6], v[8 * s + 7])}; }
DI float dot2(u32 a, u32 b, float c) { return __builtin_amdgcn_fdot2_f32_bf16(__builtin_bit_cast(bf16x2, a), __builtin_bit_cast(bf16x2, b), c, false); }
DI float glu(float g, float u) { return u * __builtin_amdgcn_rcpf(1.f + __builtin_amdgcn_exp2f(g * -1.4426950408889634f)); }
DI float xhalf_max(float v) { auto r = __builtin_amdgcn_permlane32_swap(__float_as_uint(v), __float_as_uint(v), false, false); return fmaxf(__uint_as_float(r[0]), __uint_as_float(r[1])); }
DI float xhalf_sum(float v) { auto r = __builtin_amdgcn_permlane32_swap(__float_as_uint(v), __float_as_uint(v), false, false); return __uint_as_float(r[0]) + __uint_as_float(r[1]); }

DI void spin(const char* ws, u32 off, bool need, u64 tag) {
  const GAS u64* p = (const GAS u64*)(ws + off);
#pragma unroll 1
  for (int i = 0; i < (1 << 16); ++i) {
    u64 v = __hip_atomic_load(p, __ATOMIC_RELAXED, __HIP_MEMORY_SCOPE_AGENT);
    if (__all(!need || v == tag)) break;
    __builtin_amdgcn_s_sleep(1);
  }
  asm volatile("" ::: "memory");
}
DI bool match(u32x2 v, bool need, u64 tag) { return __all(!need || (v.x == (u32)tag && v.y == (u32)(tag >> 32))); }

#define STAMP(i)

constexpr int G_KVST = 28, G_KVPUB = 36, G_H1STA = 30, G_H1STB = 32, G_H1PUB = 40, G_POLL1 = 48, G_CHK1 = 52;
constexpr int G_H2ST = 57, G_H2PUB = 63, G_POLL2 = 66, G_CHK2 = 69, G_POLL3 = 76, G_CHK3 = 80;
constexpr int PRO_OPS = 21;
constexpr bool is_sync(int g) { return g < 56 ? (g % 4 == 3) : (g < 90 && (g - 56) % 5 == 4); }
constexpr int need_seq(int g) { int n = g + 1; return n < 28 ? 5 * (n / 4) + 4 : (n < 56 ? 36 + 2 * ((n - 28) / 4) : 49 + (n - 56) + 4); }
constexpr int dma_lo(int g) {
  if (g < 27 && g % 4 == 3) return 20 + 5 * (g / 4);
  if (g == 28) return 50;
  if (g > 28 && g < 56 && g % 4 == 3) return 55 + 2 * ((g - 28) / 4);
  if (g >= 56 && is_sync(g) && (g - 56) / 5 < 3) return 69 + 5 * ((g - 56) / 5);
  return 0;
}
constexpr int n_dma(int g) { return dma_lo(g) == 0 ? 0 : ((g > 28 && g < 56) ? 2 : 5); }
constexpr int n_flag(int g) { return (g == G_KVPUB) + (g == G_H1PUB) + (g == G_H2PUB); }
constexpr int n_st(int g) { return 2 * (g == G_KVST) + (g == G_H1STA) + (g == G_H1STB) + 2 * (g == G_H2ST); }
constexpr int n_poll(int g) { return (g == G_POLL1) + (g == G_POLL2) + (g == G_POLL3); }
constexpr int n_ld(int g) { return 14 * (g == G_CHK1) + 14 * (g == G_CHK2) + 8 * (g == G_CHK3); }
constexpr int ops_in(int g) { return n_flag(g) + n_st(g) + n_poll(g) + n_ld(g) + n_dma(g); }
constexpr int base(int g) { int t = PRO_OPS; for (int j = 0; j < g; ++j) t += ops_in(j); return t; }
constexpr int pos_st_last(int g) { return base(g) + n_flag(g) + n_st(g) - 1; }
constexpr int pos_poll(int g) { return base(g) + n_flag(g) + n_st(g); }
constexpr int pos_ld(int g, int i) { return base(g) + n_flag(g) + n_st(g) + n_poll(g) + i; }
constexpr int pos_dma(int n) {
  if (n < 20) return PRO_OPS - 20 + n;
  for (int g = 0; g < 91; ++g)
    if (n_dma(g) && n >= dma_lo(g) && n < dma_lo(g) + n_dma(g)) return base(g) + n_flag(g) + n_st(g) + n_poll(g) + n_ld(g) + (n - dma_lo(g));
  return -1000;
}
constexpr int pos_a2(int i) { return i < 14 ? pos_ld(G_CHK1, i) : (i < 28 ? pos_ld(G_CHK2, i - 14) : pos_ld(G_CHK3, i - 28 + 1)); }

template <int I, int N, class Fn> DI void static_for(Fn&& f) {
  if constexpr (I < N) { f(std::integral_constant<int, I>{}); static_for<I + 1, N>(f); }
}

struct Ctx {
  LAS char* sm; u32 m0w;
  rsrc_t rx, rqkv, rwg, rwu, rwd, rwo, rws;
  const char* ws;
  u32 vx, vkv, vg, vu, vw2;
};

template <int N> DI void issueA(const Ctx& c) {
  constexpr u32 dst = (N % 20) * SLOT;
  if constexpr (N < 35) {
    constexpr int kc = N / 5, j = N % 5;
    if constexpr (j < 2) dma(c.m0w + dst, c.vx, c.rx, j * 896 + kc * 128);
    else if constexpr (j == 2) dma(c.m0w + dst, c.vkv, c.rqkv, kc * 64 * 2688);
    else if constexpr (j == 3) dma(c.m0w + dst, c.vg, c.rwg, kc * 64 * 3584);
    else dma(c.m0w + dst, c.vu, c.rwu, kc * 64 * 3584);
  } else if constexpr (N < 49) {
    constexpr int kc = (N - 35) / 2;
    if constexpr ((N - 35) % 2 == 0) dma(c.m0w + dst, c.vg, c.rwg, kc * 64 * 3584 + 1792);
    else dma(c.m0w + dst, c.vu, c.rwu, kc * 64 * 3584 + 1792);
  } else if constexpr (N < 77) dma(c.m0w + dst, c.vw2, c.rwd, (N - 49) * 64 * 896);
  else dma(c.m0w + dst, c.vw2, c.rwo, (N - 77) * 64 * 896);
}

template <bool ISV>
DI void type_a(const Ctx& c, int w, int lane, int mt, int role, int R2, int n2, void* out
) {
  const int r = lane & 31, hi = lane >> 5, blk = (lane >> 4) & 1, q = (lane & 15) >> 2, p = lane & 3;
  const int sp = ((p & 1) << 1) | (p >> 1);
  const int rg = w & 3, cg = w >> 2, T = mt * 4 + rg;
  LAS char* sm = c.sm;
  u32x2 ev = ld64_sc1(0, c.rws, WS_E);
  static_for<0, 20>([&](auto N) { issueA<N>(c); });
  const int R = 32 * (rg & 1) + r, xm = (R >> 1) & 7;
  int xa[4];
#pragma unroll
  for (int s = 0; s < 4; ++s) xa[s] = (rg >> 1) * SLOT + R * 128 + (((2 * s + hi) ^ xm) << 4);
  const int rowb = (8 * hi + q) * 128, q2 = q & 2;
  const int akv = rowb + (((2 * cg + blk) ^ q2) << 5) + (ISV ? p : sp) * 8;
  int agu[2];
#pragma unroll
  for (int i = 0; i < 2; ++i) agu[i] = blk * SLOT + rowb + (((2 * cg + i) ^ q2 ^ blk) << 5) + sp * 8;
  const int rt = w & 1, kq = w >> 1;
  int aw2[2];
#pragma unroll
  for (int i = 0; i < 2; ++i) aw2[i] = kq * 2048 + rowb + (((2 * i + blk) ^ q2) << 5) + p * 8;
  const int T2 = (R2 >> 1) * 4 + 2 * rt + (R2 & 1);

  u32x4 xf[28], wf[2][3], af[35], hp[2];
  f32x16 akvacc = {}, g1[2] = {}, g2[2] = {}, ah[2] = {}, ao[2] = {};
  u32x2 pollv; u32 omg; u64 tag;
  const u32 lane16 = lane * 16;
  const u32 flag_off = lane == 0 ? WS_FLAG + T * 256 + (role * 2 + cg) * 8 : OOB;
  const u32 poll_off = WS_FLAG + T2 * 256 + r * 8;
  const bool need_h = lane < 28 && (lane & 1) == (kq >> 1), need_o = lane >= 28 && lane < 32;
  const u32 a2_off = WS_A2 + T2 * A2_T + kq * 1024 + lane16;
  const u32 h_off = WS_A2 + T * A2_T + (role * 4 + 2 * cg) * 1024 + lane16;

  auto load_frags = [&](auto G) {
    constexpr int g = G, b = g & 1;
    if constexpr (g < 28) {
      constexpr int kc = g / 4, s = g % 4, sl = (5 * kc) % 20;
      xf[g] = *(LAS u32x4*)(sm + xa[s] + sl * SLOT);
      wf[b][0] = trfrag(sm + akv + (sl + 2) * SLOT + s * 2048);
      wf[b][1] = trfrag(sm + agu[0] + (sl + 3) * SLOT + s * 2048);
      wf[b][2] = trfrag(sm + agu[1] + (sl + 3) * SLOT + s * 2048);
    } else if constexpr (g < 56) {
      constexpr int kc = (g - 28) / 4, s = g % 4, sl = (35 + 2 * kc) % 20;
      const int wrap = sl == 19 ? blk * (-20 * SLOT) : 0;
      wf[b][1] = trfrag(sm + agu[0] + wrap + sl * SLOT + s * 2048);
      wf[b][2] = trfrag(sm + agu[1] + wrap + sl * SLOT + s * 2048);
    } else if constexpr (g < 91) {
      constexpr int sl = (49 + g - 56) % 20;
      wf[b][1] = trfrag(sm + aw2[0] + sl * SLOT);
      wf[b][2] = trfrag(sm + aw2[1] + sl * SLOT);
    }
  };
  auto publish = [&](int cls) {
    __builtin_amdgcn_raw_buffer_store_b64(u32x2{(u32)tag, (u32)(tag >> 32)}, c.rws, flag_off, cls * 64 * 256, SC1);
  };

  STAMP(1);
  sync_vm<15>();
  STAMP(2);
  asm volatile("" : "+v"(ev));
  {
    u64 e = ((u64)__builtin_amdgcn_readfirstlane(ev.y) << 32) | (u32)__builtin_amdgcn_readfirstlane(ev.x);
    tag = e * 6364136223846793005ull + 1442695040888963407ull;
  }
  load_frags(std::integral_constant<int, 0>{});
  SCHED();
  static_for<0, 91>([&](auto G) {
    constexpr int g = G, b = g & 1;
    if constexpr (g == 28) STAMP(3);
    if constexpr (g == 56) STAMP(4);
    if constexpr (g == 70) STAMP(6);
    if constexpr (g == 84) STAMP(8);
    if constexpr (is_sync(g)) sync_vm<base(g) - pos_dma(need_seq(g)) - 1>();
    load_frags(std::integral_constant<int, g + 1>{});
    if constexpr (g == G_KVPUB) { wait_vm<base(g) - pos_st_last(G_KVST) - 1>(); publish(0); }
    if constexpr (g == G_H1PUB) { wait_vm<base(g) - pos_st_last(G_H1STB) - 1>(); publish(1); }
    if constexpr (g == G_H2PUB) { wait_vm<base(g) - pos_st_last(G_H2ST) - 1>(); publish(2); }
    if constexpr (g == G_KVST) {
#pragma unroll
      for (int s = 0; s < 2; ++s) {
        const u32 off = ISV ? WS_V + T * KV_T + (s * 2 + hi) * 7168 + ((role - 7) * 64 + cg * 32 + r) * 16
                            : WS_K + T * KV_T + (role * 8 + cg * 4 + 2 * s) * 512 + lane16;
        __builtin_amdgcn_raw_buffer_store_b128(pk8(akvacc, s), c.rws, off, 0, SC1);
      }
    }
    if constexpr (g == G_H1STA) __builtin_amdgcn_raw_buffer_store_b128(hp[0], c.rws, h_off, 0, SC1);
    if constexpr (g == G_H1STB) __builtin_amdgcn_raw_buffer_store_b128(hp[1], c.rws, h_off, 1024, SC1);
    if constexpr (g == G_H2ST) {
      __builtin_amdgcn_raw_buffer_store_b128(hp[0], c.rws, h_off, 56 * 1024, SC1);
      __builtin_amdgcn_raw_buffer_store_b128(hp[1], c.rws, h_off, 57 * 1024, SC1);
    }
    asm volatile("" ::: "memory");
    if constexpr (g == G_POLL1) pollv = ld64_sc1(poll_off, c.rws, 1 * 64 * 256);
    if constexpr (g == G_POLL2 || g == G_POLL3) pollv = ld64_sc1(poll_off, c.rws, 2 * 64 * 256);
    if constexpr (g == G_CHK1) {
      wait_vm_for<pos_ld(g, 0) - pos_poll(G_POLL1) - 1>(pollv);
      if (!match(pollv, need_h, tag)) spin(c.ws, poll_off + 1 * 64 * 256, need_h, tag);
      static_for<0, 14>([&](auto I) { af[I] = ld128_sc1(a2_off, c.rws, I * 4096); });
    }
    if constexpr (g == G_CHK2) {
      STAMP(5);
      wait_vm_for<pos_ld(g, 0) - pos_poll(G_POLL2) - 1>(pollv);
      if (!match(pollv, need_h, tag)) spin(c.ws, poll_off + 2 * 64 * 256, need_h, tag);
      static_for<14, 28>([&](auto I) { af[I] = ld128_sc1(a2_off, c.rws, I * 4096); });
    }
    if constexpr (g == G_CHK3) {
      STAMP(7);
      wait_vm_for<pos_ld(g, 0) - pos_poll(G_POLL3) - 1>(pollv);
      if (!match(pollv, need_o, tag)) spin(c.ws, poll_off + 2 * 64 * 256, need_o, tag);
      omg = ld32_sc1(r * 4, c.rws, WS_OMG + T2 * 128);
      static_for<28, 35>([&](auto I) { af[I] = ld128_sc1(a2_off, c.rws, I * 4096); });
    }
    static_for<0, n_dma(g)>([&](auto I) { issueA<dma_lo(g) + I>(c); });
    if constexpr (g < 28) {
      akvacc = ISV ? mfma(xf[g], wf[b][0], akvacc) : mfma(wf[b][0], xf[g], akvacc);
      g1[0] = mfma(wf[b][1], xf[g], g1[0]);
      g1[1] = mfma(wf[b][2], xf[g], g1[1]);
    } else if constexpr (g < 56) {
      g2[0] = mfma(wf[b][1], xf[g - 28], g2[0]);
      g2[1] = mfma(wf[b][2], xf[g - 28], g2[1]);
      if constexpr (g < 32) {
        constexpr int t = (g - 28) / 2, j = ((g - 28) % 2) * 4;
        hp[t][j / 2] = pk(glu(g1[t][j], g1[t][8 + j]), glu(g1[t][j + 1], g1[t][9 + j]));
        hp[t][j / 2 + 1] = pk(glu(g1[t][j + 2], g1[t][10 + j]), glu(g1[t][j + 3], g1[t][11 + j]));
      }
    } else {
      constexpr int i = g - 56;
      if constexpr (g == 56) {
#pragma unroll
        for (int t = 0; t < 2; ++t)
#pragma unroll
          for (int j = 0; j < 8; j += 2) hp[t][j / 2] = pk(glu(g2[t][j], g2[t][8 + j]), glu(g2[t][j + 1], g2[t][9 + j]));
        asm volatile("" : "+v"(hp[0]), "+v"(hp[1]));
      }
      wait_vm_for<base(g + 1) - pos_a2(i) - 1>(af[i]);
      if constexpr (i < 28) {
        ah[0] = mfma(wf[b][1], af[i], ah[0]);
        ah[1] = mfma(wf[b][2], af[i], ah[1]);
      } else {
        ao[0] = mfma(wf[b][1], af[i], ao[0]);
        ao[1] = mfma(wf[b][2], af[i], ao[1]);
      }
    }
    SCHED();
  });
  STAMP(9);
  asm volatile("" : "+v"(omg));
  const float om = __uint_as_float(omg);
  LAS char* red = sm + 4 * SLOT;
#pragma unroll
  for (int t = 0; t < 2; ++t)
#pragma unroll
    for (int a = 0; a < 4; ++a) {
      f32x4 v;
#pragma unroll
      for (int e = 0; e < 4; ++e) v[e] = ah[t][4 * a + e] * om + ao[t][4 * a + e];
      *(LAS f32x4*)(red + kq * 16384 + (2 * r + rt) * 256 + (((8 * t + 2 * a + hi) ^ (r & 15)) << 4)) = v;
    }
  asm volatile("s_waitcnt lgkmcnt(0)\n\ts_barrier" ::: "memory");
  {
    const int row = w * 8 + (lane >> 3), c8 = lane & 7, m = (row >> 1) & 15;
    f32x4 s0 = {}, s1 = {};
#pragma unroll
    for (int k = 0; k < 4; ++k) {
      s0 += *(LAS f32x4*)(red + k * 16384 + row * 256 + (((2 * c8) ^ m) << 4));
      s1 += *(LAS f32x4*)(red + k * 16384 + row * 256 + (((2 * c8 + 1) ^ m) << 4));
    }
    u32x4 o = {pk(s0[0], s0[1]), pk(s0[2], s0[3]), pk(s1[0], s1[1]), pk(s1[2], s1[3])};
    *(u32x4*)((char*)out + (size_t)(R2 * 64 + row) * 896 + n2 * 128 + c8 * 16) = o;
  }
}

template <int N> DI void issueB(const Ctx& c) {
  constexpr int kc = N / 6, j = N % 6;
  constexpr u32 dst = (N % 18) * SLOT;
  if constexpr (j < 2) dma(c.m0w + dst, c.vx, c.rx, j * 896 + kc * 128);
  else dma(c.m0w + dst, c.vkv, c.rqkv, kc * 64 * 2688 + (j - 2) * 128);
}

DI void type_b(const Ctx& c, int w, int lane, int mt, int hf, const void* gw
) {
  const int r = lane & 31, hi = lane >> 5, blk = (lane >> 4) & 1, q = (lane & 15) >> 2, p = lane & 3;
  const int sp = ((p & 1) << 1) | (p >> 1);
  const int rg = w & 3, cg = w >> 2, T = mt * 4 + rg;
  LAS char* sm = c.sm;
  u32x2 ev = ld64_sc1(0, c.rws, WS_E);
  if (w == 0) dma(c.m0w + 18 * SLOT, lane * 16, mk(gw, 896), 0);
  static_for<0, 18>([&](auto N) { issueB<N>(c); });
  const int R = 32 * (rg & 1) + r, xm = (R >> 1) & 7;
  int xa[4], aq[4];
#pragma unroll
  for (int s = 0; s < 4; ++s) xa[s] = (rg >> 1) * SLOT + R * 128 + (((2 * s + hi) ^ xm) << 4);
  const int h0 = 8 * hf + 4 * cg;
#pragma unroll
  for (int i = 0; i < 4; ++i) {
    const int col = 28 * (h0 + i) - 4 * (i & 1) - 192 * hf + 16 * blk + 4 * sp;
    aq[i] = (2 + (col >> 6)) * SLOT + (8 * hi + q) * 128 + (((col & 63) * 2) ^ ((q & 2) << 5));
  }
  u32x4 xf[2], gf[2], wf[2][4];
  f32x16 qa[4] = {};
  float z = 0.f;
  auto load_frags = [&](auto G) {
    constexpr int g = G, b = g & 1;
    if constexpr (g < 28) {
      constexpr int kc = g / 4, s = g % 4, sl = (6 * kc) % 18;
      xf[b] = *(LAS u32x4*)(sm + xa[s] + sl * SLOT);
      gf[b] = *(LAS u32x4*)(sm + 18 * SLOT + g * 32 + hi * 16);
#pragma unroll
      for (int i = 0; i < 4; ++i) wf[b][i] = trfrag(sm + aq[i] + sl * SLOT + s * 2048);
    }
  };
  STAMP(1);
  sync_vm<12>();
  STAMP(2);
  asm volatile("" : "+v"(ev));
  u64 tag;
  {
    u64 e = ((u64)__builtin_amdgcn_readfirstlane(ev.y) << 32) | (u32)__builtin_amdgcn_readfirstlane(ev.x);
    tag = e * 6364136223846793005ull + 1442695040888963407ull;
  }
  load_frags(std::integral_constant<int, 0>{});
  SCHED();
  static_for<0, 28>([&](auto G) {
    constexpr int g = G, b = g & 1;
    if constexpr (g % 4 == 3 && g < 27) sync_vm<(g < 23 ? 6 : 0)>();
    load_frags(std::integral_constant<int, g + 1>{});
    if constexpr (g % 4 == 3 && g < 16) static_for<0, 6>([&](auto I) { issueB<18 + 6 * (g / 4) + I>(c); });
#pragma unroll
    for (int i = 0; i < 4; ++i) qa[i] = mfma(wf[b][i], xf[b], qa[i]);
#pragma unroll
    for (int j = 0; j < 4; ++j) z = dot2(xf[b][j], gf[b][j], z);
    asm volatile("" : "+v"(z));
    SCHED();
  });
  STAMP(3);
  const bool first = (mt == 0) && !(rg & 1);
  const int Tp = first ? T : ((rg & 1) ? T - 1 : T - 3);
  spin(c.ws, WS_FLAG + (hi ? Tp : T) * 256 + r * 8, r < 28, tag);
  STAMP(4);
  u32x4 kf[4][2], kp[4][2], vf[4][2], vp[4];
  const int sr = (r & 19) | ((r & 4) << 1) | ((r & 8) >> 1);
#pragma unroll
  for (int i = 0; i < 4; ++i) {
    const int wh = 28 * (h0 + i) - 4 * (i & 1);
#pragma unroll
    for (int s = 0; s < 2; ++s) {
      kf[i][s] = __builtin_amdgcn_raw_buffer_load_b128(c.rws, lane * 16, WS_K + T * KV_T + wh * 64 + s * 1024, SC1);
      kp[i][s] = __builtin_amdgcn_raw_buffer_load_b128(c.rws, r >= 24 ? lane * 16 : OOB, WS_K + Tp * KV_T + wh * 64 + s * 1024, SC1);
    }
#pragma unroll
    for (int s = 0; s < 2; ++s) vf[i][s] = __builtin_amdgcn_raw_buffer_load_b128(c.rws, hi * 7168 + sr * 16, WS_V + T * KV_T + s * 14336 + wh * 16, SC1);
    vp[i] = __builtin_amdgcn_raw_buffer_load_b128(c.rws, hi * 7168 + sr * 16, WS_V + Tp * KV_T + 14336 + wh * 16, SC1);
  }
  SCHED();
  z = xhalf_sum(z);
  const float gate = __builtin_amdgcn_rcpf(1.f + __builtin_amdgcn_exp2f(z * -1.4426950408889634f));
  const float omg = __builtin_amdgcn_rcpf(1.f + __builtin_amdgcn_exp2f(z * 1.4426950408889634f));
  __builtin_amdgcn_raw_buffer_store_b32(__float_as_uint(omg), c.rws, (hf == 0 && cg == 0 && hi == 0) ? r * 4 : OOB, WS_OMG + T * 128, SC1);
  int ab[4];
  u32 msk[5][2];
#pragma unroll
  for (int b = 0; b < 4; ++b) ab[b] = (r >> 3) - ((r & 7) < 4 * hi + b ? 1 : 0);
#pragma unroll
  for (int a = 0; a < 5; ++a)
#pragma unroll
    for (int e = 0; e < 2; ++e) msk[a][e] = (ab[2 * e] == a - 1 ? 0xFFFFu : 0u) | (ab[2 * e + 1] == a - 1 ? 0xFFFF0000u : 0u);
  const float csc = 0.18898223650461362f * 1.4426950408889634f;
  u32x4 op[4][2];
#pragma unroll
  for (int i = 0; i < 4; ++i) {
    u32x4 qf[2] = {pk8(qa[i], 0), pk8(qa[i], 1)};
    if (i & 1) { qf[0][0] = hi ? qf[0][0] : 0u; qf[0][1] = hi ? qf[0][1] : 0u; }
    else       { qf[1][2] = hi ? 0u : qf[1][2]; qf[1][3] = hi ? 0u : qf[1][3]; }
    f32x16 so = {}, sv = {};
    so = mfma(kf[i][0], qf[0], so); sv = mfma(kp[i][0], qf[0], sv);
    so = mfma(kf[i][1], qf[1], so); sv = mfma(kp[i][1], qf[1], sv);
    float v[4];
#pragma unroll
    for (int b = 0; b < 4; ++b) {
      float t = first ? -1e30f : sv[12 + b];
#pragma unroll
      for (int a = 0; a < 4; ++a) t = ab[b] == a ? so[4 * a + b] : t;
      v[b] = t;
    }
    const float mx = xhalf_max(fmaxf(fmaxf(v[0], v[1]), fmaxf(v[2], v[3])));
#pragma unroll
    for (int b = 0; b < 4; ++b) v[b] = __builtin_amdgcn_exp2f((v[b] - mx) * csc);
    const float sc = gate * __builtin_amdgcn_rcpf(xhalf_sum((v[0] + v[1]) + (v[2] + v[3])));
    const u32 p01 = pk(v[0] * sc, v[1] * sc), p23 = pk(v[2] * sc, v[3] * sc);
    f32x16 oa = {};
    oa = mfma(vp[i], u32x4{0u, 0u, p01 & msk[0][0], p23 & msk[0][1]}, oa);
    oa = mfma(vf[i][0], u32x4{p01 & msk[1][0], p23 & msk[1][1], p01 & msk[2][0], p23 & msk[2][1]}, oa);
    oa = mfma(vf[i][1], u32x4{p01 & msk[3][0], p23 & msk[3][1], p01 & msk[4][0], p23 & msk[4][1]}, oa);
    op[i][0] = pk8(oa, 0); op[i][1] = pk8(oa, 1);
    if (i & 1) {
#pragma unroll
      for (int e = 0; e < 2; ++e) { auto t = __builtin_amdgcn_permlane32_swap(op[i - 1][1][e], op[i][0][e], false, false); op[i - 1][1][e] = t[0]; op[i][0][e] = t[1]; }
      const u32 ob = WS_A2 + T * A2_T + (1792 + 28 * (h0 + i - 1)) * 64;
      __builtin_amdgcn_raw_buffer_store_b128(op[i - 1][0], c.rws, lane * 16, ob, SC1);
      __builtin_amdgcn_raw_buffer_store_b128(op[i - 1][1], c.rws, hi ? OOB : lane * 16, ob + 1024, SC1);
      __builtin_amdgcn_raw_buffer_store_b128(op[i][0], c.rws, lane * 16, ob + 1536, SC1);
      __builtin_amdgcn_raw_buffer_store_b128(op[i][1], c.rws, lane * 16, ob + 2560, SC1);
    }
  }
  STAMP(5);
  wait_vm<0>();
  __builtin_amdgcn_raw_buffer_store_b64(u32x2{(u32)tag, (u32)(tag >> 32)}, c.rws, lane == 0 ? (28 + 2 * hf + cg) * 8 : OOB, WS_FLAG + (128 + T) * 256, SC1);
  STAMP(6);
  if (mt == 0 && hf == 0) {
#pragma unroll 1
    for (int i = 0; i < 4; ++i) spin(c.ws, WS_FLAG + 128 * 256 + (w * 4 + i) * 512 + lane * 8, true, tag);
    __builtin_amdgcn_s_barrier();
    if (w == 0) __builtin_amdgcn_raw_buffer_store_b64(u32x2{(u32)tag, (u32)(tag >> 32)}, c.rws, lane == 0 ? 0 : OOB, WS_E, SC1);
  }
}

__global__ __launch_bounds__(512, 2) void fused_kernel(const void* __restrict__ x, const void* __restrict__ wqkv, const void* __restrict__ wo,
                                                       const void* __restrict__ wg, const void* __restrict__ wu, const void* __restrict__ wd,
                                                       const void* __restrict__ gw, void* __restrict__ out, void* __restrict__ ws) {
  __shared__ __attribute__((aligned(1024))) char smem[163840];
  const int lane = threadIdx.x & 63, w = __builtin_amdgcn_readfirstlane(threadIdx.x >> 6);
  const int b = blockIdx.x, xc = b & 7, ix = b >> 3;
  const int mt = (xc >> 1) * 4 + (ix & 3), rl = ix >> 2, rh = xc & 1;
  Ctx c;
  c.sm = (LAS char*)smem;
  c.m0w = (u32)(size_t)c.sm + w * 1024;
  c.rx = mk(x, 2048 * 896); c.rqkv = mk(wqkv, 448 * 2688); c.rwg = mk(wg, 448 * 3584); c.rwu = mk(wu, 448 * 3584);
  c.rwd = mk(wd, 1792 * 896); c.rwo = mk(wo, 448 * 896); c.rws = mk(ws, WS_END);
  c.ws = (const char*)ws;
  const int r8 = lane >> 3, c8 = lane & 7, R = 8 * w + r8;
  c.vx = (mt * 128 + 2 * R) * 896 + ((c8 ^ ((R >> 1) & 7)) << 4);
  const int sw = (c8 ^ ((r8 & 2) << 1)) << 4;
  if (rl < 7) {
    const int role = rh * 7 + rl;
    c.vkv = R * 2688 + 896 + role * 128 + sw;
    c.vg = R * 3584 + role * 128 + sw;
    c.vu = R * 3584 + role * 128 + (sw ^ 32);
    c.vw2 = R * 896 + rl * 128 + sw;
    if (rh) type_a<true>(c, w, lane, mt, role, xc * 4 + (ix & 3), rl, out);
    else type_a<false>(c, w, lane, mt, role, xc * 4 + (ix & 3), rl, out);
  } else {
    c.vkv = R * 2688 + rh * 384 + sw;
    type_b(c, w, lane, mt, rh, gw);
  }
}

extern "C" void kernel_launch(void* const* d_in, const int* in_sizes, int n_in, void* d_out, int out_size, void* d_ws, size_t ws_size,
                              hipStream_t stream) {
  fused_kernel<<<256, 512, 0, stream>>>(d_in[0], d_in[1], d_in[2], d_in[4], d_in[5], d_in[6], d_in[7], d_out, d_ws);
}
```

```cpp
#define PF_CODE
#define PF_DATA
#include <hip/hip_runtime.h>
#include <hip/hip_bf16.h>

typedef unsigned long long u64;
typedef unsigned u32;
typedef __bf16 bf16x8 __attribute__((ext_vector_type(8)));
typedef __bf16 bf16x2 __attribute__((ext_vector_type(2)));
typedef short s16x4 __attribute__((ext_vector_type(4)));
typedef u32 u32x4 __attribute__((ext_vector_type(4)));
typedef u32 u32x2 __attribute__((ext_vector_type(2)));
typedef float f32x16 __attribute__((ext_vector_type(16)));
typedef float f32x4 __attribute__((ext_vector_type(4)));
typedef __amdgpu_buffer_rsrc_t rsrc_t;
#define LAS __attribute__((address_space(3)))
#define GAS __attribute__((address_space(1)))
#define DI __device__ __forceinline__
#define SCHED() __builtin_amdgcn_sched_barrier(0)

constexpr u32 WS_E = 0;
constexpr u32 WS_FLAG = 4096;
constexpr u32 WS_OMG = WS_FLAG + 3 * 64 * 256;
constexpr u32 WS_K = 65536;
constexpr u32 KV_T = 28672;
constexpr u32 WS_V = WS_K + 64 * KV_T;
constexpr u32 WS_A2 = WS_V + 64 * KV_T;
constexpr u32 A2_T = 143360;
constexpr u32 WS_END = WS_A2 + 64 * A2_T;
constexpr u32 OOB = 0xFFFFFF00u;
constexpr int SC1 = 16;
constexpr int SLOT = 8192;

DI rsrc_t mk(const void* p, u32 bytes) { return __builtin_amdgcn_make_buffer_rsrc((void*)p, (short)0, (int)bytes, 0x00020000); }
DI void dma(u32 m0v, u32 voff, rsrc_t rs, u32 soff) {
  asm volatile("s_mov_b32 m0, %0\n\ts_nop 0\n\tbuffer_load_dwordx4 %1, %2, %3 offen lds" ::"s"(m0v), "v"(voff), "s"(rs), "s"(soff) : "memory");
}
DI u32x4 ld128_sc1(u32 voff, rsrc_t rs, u32 soff) {
  u32x4 v;
  asm volatile("buffer_load_dwordx4 %0, %1, %2, %3 offen sc1" : "=v"(v) : "v"(voff), "s"(rs), "s"(soff) : "memory");
  return v;
}
DI u32x2 ld64_sc1(u32 voff, rsrc_t rs, u32 soff) {
  u32x2 v;
  asm volatile("buffer_load_dwordx2 %0, %1, %2, %3 offen sc1" : "=v"(v) : "v"(voff), "s"(rs), "s"(soff) : "memory");
  return v;
}
DI u32 ld32_sc1(u32 voff, rsrc_t rs, u32 soff) {
  u32 v;
  asm volatile("buffer_load_dword %0, %1, %2, %3 offen sc1" : "=v"(v) : "v"(voff), "s"(rs), "s"(soff) : "memory");
  return v;
}
template <int N> DI void wait_vm() { asm volatile("s_waitcnt vmcnt(%0)" ::"n"(N > 63 ? 63 : N) : "memory"); }
template <int N, class T> DI void wait_vm_for(T& v) { asm volatile("s_waitcnt vmcnt(%1)" : "+v"(v) : "n"(N > 63 ? 63 : N) : "memory"); }
template <int N> DI void sync_vm() { asm volatile("s_waitcnt vmcnt(%0) lgkmcnt(0)\n\ts_barrier" ::"n"(N > 63 ? 63 : N) : "memory"); }

DI u32x4 trfrag(LAS char* p) {
  s16x4 lo = __builtin_amdgcn_ds_read_tr16_b64_v4i16((LAS s16x4*)p);
  s16x4 hi = __builtin_amdgcn_ds_read_tr16_b64_v4i16((LAS s16x4*)(p + 512));
  u32x2 a = __builtin_bit_cast(u32x2, lo), b = __builtin_bit_cast(u32x2, hi);
  return u32x4{a.x, a.y, b.x, b.y};
}
DI f32x16 mfma(u32x4 a, u32x4 b, f32x16 c) {
  return __builtin_amdgcn_mfma_f32_32x32x16_bf16(__builtin_bit_cast(bf16x8, a), __builtin_bit_cast(bf16x8, b), c, 0, 0, 0);
}
DI u32 pk(float a, float b) { bf16x2 c; c[0] = (__bf16)a; c[1] = (__bf16)b; return __builtin_bit_cast(u32, c); }
DI u32x4 pk8(const f32x16& v, int s) { return u32x4{pk(v[8 * s], v[8 * s + 1]), pk(v[8 * s + 2], v[8 * s + 3]), pk(v[8 * s + 4], v[8 * s + 5]), pk(v[8 * s + 6], v[8 * s + 7])}; }
DI float dot2(u32 a, u32 b, float c) { return __builtin_amdgcn_fdot2_f32_bf16(__builtin_bit_cast(bf16x2, a), __builtin_bit_cast(bf16x2, b), c, false); }
DI float glu(float g, float u) { return u * __builtin_amdgcn_rcpf(1.f + __builtin_amdgcn_exp2f(g * -1.4426950408889634f)); }
DI float xhalf_max(float v) { auto r = __builtin_amdgcn_permlane32_swap(__float_as_uint(v), __float_as_uint(v), false, false); return fmaxf(__uint_as_float(r[0]), __uint_as_float(r[1])); }
DI float xhalf_sum(float v) { auto r = __builtin_amdgcn_permlane32_swap(__float_as_uint(v), __float_as_uint(v), false, false); return __uint_as_float(r[0]) + __uint_as_float(r[1]); }

DI void spin(const char* ws, u32 off, bool need, u64 tag) {
  const GAS u64* p = (const GAS u64*)(ws + off);
#pragma unroll 1
  for (int i = 0; i < (1 << 16); ++i) {
    u64 v = __hip_atomic_load(p, __ATOMIC_RELAXED, __HIP_MEMORY_SCOPE_AGENT);
    if (__all(!need || v == tag)) break;
    __builtin_amdgcn_s_sleep(1);
  }
  asm volatile("" ::: "memory");
}
DI bool match(u32x2 v, bool need, u64 tag) { return __all(!need || (v.x == (u32)tag && v.y == (u32)(tag >> 32))); }

#define STAMP(i)

constexpr int G_KVST = 28, G_KVPUB = 36, G_H1STA = 30, G_H1STB = 32, G_H1PUB = 40, G_POLL1 = 48, G_CHK1 = 52;
constexpr int G_H2ST = 57, G_H2PUB = 63, G_POLL2 = 66, G_CHK2 = 69, G_POLL3 = 76, G_CHK3 = 80;
constexpr int PRO_OPS = 21;
#define CODE_BYTES 46080
constexpr bool is_sync(int g) { return g < 56 ? (g % 4 == 3) : (g < 90 && (g - 56) % 5 == 4); }
constexpr int need_seq(int g) { int n = g + 1; return n < 28 ? 5 * (n / 4) + 4 : (n < 56 ? 36 + 2 * ((n - 28) / 4) : 49 + (n - 56) + 4); }
constexpr int dma_lo(int g) {
  if (g < 27 && g % 4 == 3) return 20 + 5 * (g / 4);
  if (g == 28) return 50;
  if (g > 28 && g < 56 && g % 4 == 3) return 55 + 2 * ((g - 28) / 4);
  if (g >= 56 && is_sync(g) && (g - 56) / 5 < 3) return 69 + 5 * ((g - 56) / 5);
  return 0;
}
constexpr int n_dma(int g) { return dma_lo(g) == 0 ? 0 : ((g > 28 && g < 56) ? 2 : 5); }
constexpr int n_flag(int g) { return (g == G_KVPUB) + (g == G_H1PUB) + (g == G_H2PUB); }
constexpr int n_st(int g) { return 2 * (g == G_KVST) + (g == G_H1STA) + (g == G_H1STB) + 2 * (g == G_H2ST); }
constexpr int n_poll(int g) { return (g == G_POLL1) + (g == G_POLL2) + (g == G_POLL3); }
constexpr int n_ld(int g) { return 14 * (g == G_CHK1) + 14 * (g == G_CHK2) + 8 * (g == G_CHK3); }
constexpr int ops_in(int g) { return n_flag(g) + n_st(g) + n_poll(g) + n_ld(g) + n_dma(g); }
constexpr int base(int g) { int t = PRO_OPS; for (int j = 0; j < g; ++j) t += ops_in(j); return t; }
constexpr int pos_st_last(int g) { return base(g) + n_flag(g) + n_st(g) - 1; }
constexpr int pos_poll(int g) { return base(g) + n_flag(g) + n_st(g); }
constexpr int pos_ld(int g, int i) { return base(g) + n_flag(g) + n_st(g) + n_poll(g) + i; }
constexpr int pos_dma(int n) {
  if (n < 20) return PRO_OPS - 20 + n;
  for (int g = 0; g < 91; ++g)
    if (n_dma(g) && n >= dma_lo(g) && n < dma_lo(g) + n_dma(g)) return base(g) + n_flag(g) + n_st(g) + n_poll(g) + n_ld(g) + (n - dma_lo(g));
  return -1000;
}
constexpr int pos_a2(int i) { return i < 14 ? pos_ld(G_CHK1, i) : (i < 28 ? pos_ld(G_CHK2, i - 14) : pos_ld(G_CHK3, i - 28 + 1)); }

template <int I, int N, class Fn> DI void static_for(Fn&& f) {
  if constexpr (I < N) { f(std::integral_constant<int, I>{}); static_for<I + 1, N>(f); }
}

struct Ctx {
  LAS char* sm; u32 m0w;
  rsrc_t rx, rqkv, rwg, rwu, rwd, rwo, rws;
  const char* ws;
  u32 td; u32 vx, vkv, vg, vu, vw2;
};

template <int N> DI void issueA(const Ctx& c) {
  constexpr u32 dst = (N % 20) * SLOT;
  if constexpr (N < 35) {
    constexpr int kc = N / 5, j = N % 5;
    if constexpr (j < 2) dma(c.m0w + dst, c.vx, c.rx, j * 896 + kc * 128);
    else if constexpr (j == 2) dma(c.m0w + dst, c.vkv, c.rqkv, kc * 64 * 2688);
    else if constexpr (j == 3) dma(c.m0w + dst, c.vg, c.rwg, kc * 64 * 3584);
    else dma(c.m0w + dst, c.vu, c.rwu, kc * 64 * 3584);
  } else if constexpr (N < 49) {
    constexpr int kc = (N - 35) / 2;
    if constexpr ((N - 35) % 2 == 0) dma(c.m0w + dst, c.vg, c.rwg, kc * 64 * 3584 + 1792);
    else dma(c.m0w + dst, c.vu, c.rwu, kc * 64 * 3584 + 1792);
  } else if constexpr (N < 77) dma(c.m0w + dst, c.vw2, c.rwd, (N - 49) * 64 * 896);
  else dma(c.m0w + dst, c.vw2, c.rwo, (N - 77) * 64 * 896);
}

template <bool ISV>
DI void type_a(const Ctx& c, int w, int lane, int mt, int role, int R2, int n2, void* out
) {
  const int r = lane & 31, hi = lane >> 5, blk = (lane >> 4) & 1, q = (lane & 15) >> 2, p = lane & 3;
  const int sp = ((p & 1) << 1) | (p >> 1);
  const int rg = w & 3, cg = w >> 2, T = mt * 4 + rg;
  LAS char* sm = c.sm;
  u32x2 ev = ld64_sc1(0, c.rws, WS_E);
  static_for<0, 20>([&](auto N) { issueA<N>(c); });
  const int R = 32 * (rg & 1) + r, xm = (R >> 1) & 7;
  int xa[4];
#pragma unroll
  for (int s = 0; s < 4; ++s) xa[s] = (rg >> 1) * SLOT + R * 128 + (((2 * s + hi) ^ xm) << 4);
  const int rowb = (8 * hi + q) * 128, q2 = q & 2;
  const int akv = rowb + (((2 * cg + blk) ^ q2) << 5) + (ISV ? p : sp) * 8;
  int agu[2];
#pragma unroll
  for (int i = 0; i < 2; ++i) agu[i] = blk * SLOT + rowb + (((2 * cg + i) ^ q2 ^ blk) << 5) + sp * 8;
  const int rt = w & 1, kq = w >> 1;
  int aw2[2];
#pragma unroll
  for (int i = 0; i < 2; ++i) aw2[i] = kq * 2048 + rowb + (((2 * i + blk) ^ q2) << 5) + p * 8;
  const int T2 = (R2 >> 1) * 4 + 2 * rt + (R2 & 1);

  u32x4 xf[28], wf[2][3], af[35], hp[2];
  f32x16 akvacc = {}, g1[2] = {}, g2[2] = {}, ah[2] = {}, ao[2] = {};
  u32x2 pollv; u32 omg; u64 tag;
  const u32 lane16 = lane * 16;
  const u32 flag_off = lane == 0 ? WS_FLAG + T * 256 + (role * 2 + cg) * 8 : OOB;
  const u32 poll_off = WS_FLAG + T2 * 256 + r * 8;
  const bool need_h = lane < 28 && (lane & 1) == (kq >> 1), need_o = lane >= 28 && lane < 32;
  const u32 a2_off = WS_A2 + T2 * A2_T + kq * 1024 + lane16;
  const u32 h_off = WS_A2 + T * A2_T + (role * 4 + 2 * cg) * 1024 + lane16;

  auto load_frags = [&](auto G) {
    constexpr int g = G, b = g & 1;
    if constexpr (g < 28) {
      constexpr int kc = g / 4, s = g % 4, sl = (5 * kc) % 20;
      xf[g] = *(LAS u32x4*)(sm + xa[s] + sl * SLOT);
      wf[b][0] = trfrag(sm + akv + (sl + 2) * SLOT + s * 2048);
      wf[b][1] = trfrag(sm + agu[0] + (sl + 3) * SLOT + s * 2048);
      wf[b][2] = trfrag(sm + agu[1] + (sl + 3) * SLOT + s * 2048);
    } else if constexpr (g < 56) {
      constexpr int kc = (g - 28) / 4, s = g % 4, sl = (35 + 2 * kc) % 20;
      const int wrap = sl == 19 ? blk * (-20 * SLOT) : 0;
      wf[b][1] = trfrag(sm + agu[0] + wrap + sl * SLOT + s * 2048);
      wf[b][2] = trfrag(sm + agu[1] + wrap + sl * SLOT + s * 2048);
    } else if constexpr (g < 91) {
      constexpr int sl = (49 + g - 56) % 20;
      wf[b][1] = trfrag(sm + aw2[0] + sl * SLOT);
      wf[b][2] = trfrag(sm + aw2[1] + sl * SLOT);
    }
  };
  auto publish = [&](int cls) {
    __builtin_amdgcn_raw_buffer_store_b64(u32x2{(u32)tag, (u32)(tag >> 32)}, c.rws, flag_off, cls * 64 * 256, SC1);
  };

  STAMP(1);
  sync_vm<15>();
  STAMP(2);
  asm volatile("" : "+v"(ev));
  asm volatile("" :: "v"(c.td));
  {
    u64 e = ((u64)__builtin_amdgcn_readfirstlane(ev.y) << 32) | (u32)__builtin_amdgcn_readfirstlane(ev.x);
    tag = e * 6364136223846793005ull + 1442695040888963407ull;
  }
  load_frags(std::integral_constant<int, 0>{});
  SCHED();
  static_for<0, 91>([&](auto G) {
    constexpr int g = G, b = g & 1;
    if constexpr (g == 28) STAMP(3);
    if constexpr (g == 56) STAMP(4);
    if constexpr (g == 70) STAMP(6);
    if constexpr (g == 84) STAMP(8);
    if constexpr (is_sync(g)) sync_vm<base(g) - pos_dma(need_seq(g)) - 1>();
    load_frags(std::integral_constant<int, g + 1>{});
    if constexpr (g == G_KVPUB) { wait_vm<base(g) - pos_st_last(G_KVST) - 1>(); publish(0); }
    if constexpr (g == G_H1PUB) { wait_vm<base(g) - pos_st_last(G_H1STB) - 1>(); publish(1); }
    if constexpr (g == G_H2PUB) { wait_vm<base(g) - pos_st_last(G_H2ST) - 1>(); publish(2); }
    if constexpr (g == G_KVST) {
#pragma unroll
      for (int s = 0; s < 2; ++s) {
        const u32 off = ISV ? WS_V + T * KV_T + (s * 2 + hi) * 7168 + ((role - 7) * 64 + cg * 32 + r) * 16
                            : WS_K + T * KV_T + (role * 8 + cg * 4 + 2 * s) * 512 + lane16;
        __builtin_amdgcn_raw_buffer_store_b128(pk8(akvacc, s), c.rws, off, 0, SC1);
      }
    }
    if constexpr (g == G_H1STA) __builtin_amdgcn_raw_buffer_store_b128(hp[0], c.rws, h_off, 0, SC1);
    if constexpr (g == G_H1STB) __builtin_amdgcn_raw_buffer_store_b128(hp[1], c.rws, h_off, 1024, SC1);
    if constexpr (g == G_H2ST) {
      __builtin_amdgcn_raw_buffer_store_b128(hp[0], c.rws, h_off, 56 * 1024, SC1);
      __builtin_amdgcn_raw_buffer_store_b128(hp[1], c.rws, h_off, 57 * 1024, SC1);
    }
    asm volatile("" ::: "memory");
    if constexpr (g == G_POLL1) pollv = ld64_sc1(poll_off, c.rws, 1 * 64 * 256);
    if constexpr (g == G_POLL2 || g == G_POLL3) pollv = ld64_sc1(poll_off, c.rws, 2 * 64 * 256);
    if constexpr (g == G_CHK1) {
      wait_vm_for<pos_ld(g, 0) - pos_poll(G_POLL1) - 1>(pollv);
      if (!match(pollv, need_h, tag)) spin(c.ws, poll_off + 1 * 64 * 256, need_h, tag);
      static_for<0, 14>([&](auto I) { af[I] = ld128_sc1(a2_off, c.rws, I * 4096); });
    }
    if constexpr (g == G_CHK2) {
      STAMP(5);
      wait_vm_for<pos_ld(g, 0) - pos_poll(G_POLL2) - 1>(pollv);
      if (!match(pollv, need_h, tag)) spin(c.ws, poll_off + 2 * 64 * 256, need_h, tag);
      static_for<14, 28>([&](auto I) { af[I] = ld128_sc1(a2_off, c.rws, I * 4096); });
    }
    if constexpr (g == G_CHK3) {
      STAMP(7);
      wait_vm_for<pos_ld(g, 0) - pos_poll(G_POLL3) - 1>(pollv);
      if (!match(pollv, need_o, tag)) spin(c.ws, poll_off + 2 * 64 * 256, need_o, tag);
      omg = ld32_sc1(r * 4, c.rws, WS_OMG + T2 * 128);
      static_for<28, 35>([&](auto I) { af[I] = ld128_sc1(a2_off, c.rws, I * 4096); });
    }
    static_for<0, n_dma(g)>([&](auto I) { issueA<dma_lo(g) + I>(c); });
    if constexpr (g < 28) {
      akvacc = ISV ? mfma(xf[g], wf[b][0], akvacc) : mfma(wf[b][0], xf[g], akvacc);
      g1[0] = mfma(wf[b][1], xf[g], g1[0]);
      g1[1] = mfma(wf[b][2], xf[g], g1[1]);
    } else if constexpr (g < 56) {
      g2[0] = mfma(wf[b][1], xf[g - 28], g2[0]);
      g2[1] = mfma(wf[b][2], xf[g - 28], g2[1]);
      if constexpr (g < 32) {
        constexpr int t = (g - 28) / 2, j = ((g - 28) % 2) * 4;
        hp[t][j / 2] = pk(glu(g1[t][j], g1[t][8 + j]), glu(g1[t][j + 1], g1[t][9 + j]));
        hp[t][j / 2 + 1] = pk(glu(g1[t][j + 2], g1[t][10 + j]), glu(g1[t][j + 3], g1[t][11 + j]));
      }
    } else {
      constexpr int i = g - 56;
      if constexpr (g == 56) {
#pragma unroll
        for (int t = 0; t < 2; ++t)
#pragma unroll
          for (int j = 0; j < 8; j += 2) hp[t][j / 2] = pk(glu(g2[t][j], g2[t][8 + j]), glu(g2[t][j + 1], g2[t][9 + j]));
        asm volatile("" : "+v"(hp[0]), "+v"(hp[1]));
      }
      wait_vm_for<base(g + 1) - pos_a2(i) - 1>(af[i]);
      if constexpr (i < 28) {
        ah[0] = mfma(wf[b][1], af[i], ah[0]);
        ah[1] = mfma(wf[b][2], af[i], ah[1]);
      } else {
        ao[0] = mfma(wf[b][1], af[i], ao[0]);
        ao[1] = mfma(wf[b][2], af[i], ao[1]);
      }
    }
    SCHED();
  });
  STAMP(9);
  asm volatile("" : "+v"(omg));
  const float om = __uint_as_float(omg);
  LAS char* red = sm + 4 * SLOT;
#pragma unroll
  for (int t = 0; t < 2; ++t)
#pragma unroll
    for (int a = 0; a < 4; ++a) {
      f32x4 v;
#pragma unroll
      for (int e = 0; e < 4; ++e) v[e] = ah[t][4 * a + e] * om + ao[t][4 * a + e];
      *(LAS f32x4*)(red + kq * 16384 + (2 * r + rt) * 256 + (((8 * t + 2 * a + hi) ^ (r & 15)) << 4)) = v;
    }
  asm volatile("s_waitcnt lgkmcnt(0)\n\ts_barrier" ::: "memory");
  {
    const int row = w * 8 + (lane >> 3), c8 = lane & 7, m = (row >> 1) & 15;
    f32x4 s0 = {}, s1 = {};
#pragma unroll
    for (int k = 0; k < 4; ++k) {
      s0 += *(LAS f32x4*)(red + k * 16384 + row * 256 + (((2 * c8) ^ m) << 4));
      s1 += *(LAS f32x4*)(red + k * 16384 + row * 256 + (((2 * c8 + 1) ^ m) << 4));
    }
    u32x4 o = {pk(s0[0], s0[1]), pk(s0[2], s0[3]), pk(s1[0], s1[1]), pk(s1[2], s1[3])};
    *(u32x4*)((char*)out + (size_t)(R2 * 64 + row) * 896 + n2 * 128 + c8 * 16) = o;
  }
}

template <int N> DI void issueB(const Ctx& c) {
  constexpr int kc = N / 6, j = N % 6;
  constexpr u32 dst = (N % 18) * SLOT;
  if constexpr (j < 2) dma(c.m0w + dst, c.vx, c.rx, j * 896 + kc * 128);
  else dma(c.m0w + dst, c.vkv, c.rqkv, kc * 64 * 2688 + (j - 2) * 128);
}

DI void type_b(const Ctx& c, int w, int lane, int mt, int hf, const void* gw
) {
  const int r = lane & 31, hi = lane >> 5, blk = (lane >> 4) & 1, q = (lane & 15) >> 2, p = lane & 3;
  const int sp = ((p & 1) << 1) | (p >> 1);
  const int rg = w & 3, cg = w >> 2, T = mt * 4 + rg;
  LAS char* sm = c.sm;
  u32x2 ev = ld64_sc1(0, c.rws, WS_E);
  if (w == 0) dma(c.m0w + 18 * SLOT, lane * 16, mk(gw, 896), 0);
  static_for<0, 18>([&](auto N) { issueB<N>(c); });
  const int R = 32 * (rg & 1) + r, xm = (R >> 1) & 7;
  int xa[4], aq[4];
#pragma unroll
  for (int s = 0; s < 4; ++s) xa[s] = (rg >> 1) * SLOT + R * 128 + (((2 * s + hi) ^ xm) << 4);
  const int h0 = 8 * hf + 4 * cg;
#pragma unroll
  for (int i = 0; i < 4; ++i) {
    const int col = 28 * (h0 + i) - 4 * (i & 1) - 192 * hf + 16 * blk + 4 * sp;
    aq[i] = (2 + (col >> 6)) * SLOT + (8 * hi + q) * 128 + (((col & 63) * 2) ^ ((q & 2) << 5));
  }
  u32x4 xf[2], gf[2], wf[2][4];
  f32x16 qa[4] = {};
  float z = 0.f;
  auto load_frags = [&](auto G) {
    constexpr int g = G, b = g & 1;
    if constexpr (g < 28) {
      constexpr int kc = g / 4, s = g % 4, sl = (6 * kc) % 18;
      xf[b] = *(LAS u32x4*)(sm + xa[s] + sl * SLOT);
      gf[b] = *(LAS u32x4*)(sm + 18 * SLOT + g * 32 + hi * 16);
#pragma unroll
      for (int i = 0; i < 4; ++i) wf[b][i] = trfrag(sm + aq[i] + sl * SLOT + s * 2048);
    }
  };
  STAMP(1);
  sync_vm<12>();
  STAMP(2);
  asm volatile("" : "+v"(ev));
  asm volatile("" :: "v"(c.td));
  u64 tag;
  {
    u64 e = ((u64)__builtin_amdgcn_readfirstlane(ev.y) << 32) | (u32)__builtin_amdgcn_readfirstlane(ev.x);
    tag = e * 6364136223846793005ull + 1442695040888963407ull;
  }
  load_frags(std::integral_constant<int, 0>{});
  SCHED();
  static_for<0, 28>([&](auto G) {
    constexpr int g = G, b = g & 1;
    if constexpr (g % 4 == 3 && g < 27) sync_vm<(g < 23 ? 6 : 0)>();
    load_frags(std::integral_constant<int, g + 1>{});
    if constexpr (g % 4 == 3 && g < 16) static_for<0, 6>([&](auto I) { issueB<18 + 6 * (g / 4) + I>(c); });
#pragma unroll
    for (int i = 0; i < 4; ++i) qa[i] = mfma(wf[b][i], xf[b], qa[i]);
#pragma unroll
    for (int j = 0; j < 4; ++j) z = dot2(xf[b][j], gf[b][j], z);
    asm volatile("" : "+v"(z));
    SCHED();
  });
  STAMP(3);
  const bool first = (mt == 0) && !(rg & 1);
  const int Tp = first ? T : ((rg & 1) ? T - 1 : T - 3);
  spin(c.ws, WS_FLAG + (hi ? Tp : T) * 256 + r * 8, r < 28, tag);
  STAMP(4);
  u32x4 kf[4][2], kp[4][2], vf[4][2], vp[4];
  const int sr = (r & 19) | ((r & 4) << 1) | ((r & 8) >> 1);
#pragma unroll
  for (int i = 0; i < 4; ++i) {
    const int wh = 28 * (h0 + i) - 4 * (i & 1);
#pragma unroll
    for (int s = 0; s < 2; ++s) {
      kf[i][s] = __builtin_amdgcn_raw_buffer_load_b128(c.rws, lane * 16, WS_K + T * KV_T + wh * 64 + s * 1024, SC1);
      kp[i][s] = __builtin_amdgcn_raw_buffer_load_b128(c.rws, r >= 24 ? lane * 16 : OOB, WS_K + Tp * KV_T + wh * 64 + s * 1024, SC1);
    }
#pragma unroll
    for (int s = 0; s < 2; ++s) vf[i][s] = __builtin_amdgcn_raw_buffer_load_b128(c.rws, hi * 7168 + sr * 16, WS_V + T * KV_T + s * 14336 + wh * 16, SC1);
    vp[i] = __builtin_amdgcn_raw_buffer_load_b128(c.rws, hi * 7168 + sr * 16, WS_V + Tp * KV_T + 14336 + wh * 16, SC1);
  }
  SCHED();
  z = xhalf_sum(z);
  const float gate = __builtin_amdgcn_rcpf(1.f + __builtin_amdgcn_exp2f(z * -1.4426950408889634f));
  const float omg = __builtin_amdgcn_rcpf(1.f + __builtin_amdgcn_exp2f(z * 1.4426950408889634f));
  __builtin_amdgcn_raw_buffer_store_b32(__float_as_uint(omg), c.rws, (hf == 0 && cg == 0 && hi == 0) ? r * 4 : OOB, WS_OMG + T * 128, SC1);
  int ab[4];
  u32 msk[5][2];
#pragma unroll
  for (int b = 0; b < 4; ++b) ab[b] = (r >> 3) - ((r & 7) < 4 * hi + b ? 1 : 0);
#pragma unroll
  for (int a = 0; a < 5; ++a)
#pragma unroll
    for (int e = 0; e < 2; ++e) msk[a][e] = (ab[2 * e] == a - 1 ? 0xFFFFu : 0u) | (ab[2 * e + 1] == a - 1 ? 0xFFFF0000u : 0u);
  const float csc = 0.18898223650461362f * 1.4426950408889634f;
  u32x4 op[4][2];
#pragma unroll
  for (int i = 0; i < 4; ++i) {
    u32x4 qf[2] = {pk8(qa[i], 0), pk8(qa[i], 1)};
    if (i & 1) { qf[0][0] = hi ? qf[0][0] : 0u; qf[0][1] = hi ? qf[0][1] : 0u; }
    else       { qf[1][2] = hi ? 0u : qf[1][2]; qf[1][3] = hi ? 0u : qf[1][3]; }
    f32x16 so = {}, sv = {};
    so = mfma(kf[i][0], qf[0], so); sv = mfma(kp[i][0], qf[0], sv);
    so = mfma(kf[i][1], qf[1], so); sv = mfma(kp[i][1], qf[1], sv);
    float v[4];
#pragma unroll
    for (int b = 0; b < 4; ++b) {
      float t = first ? -1e30f : sv[12 + b];
#pragma unroll
      for (int a = 0; a < 4; ++a) t = ab[b] == a ? so[4 * a + b] : t;
      v[b] = t;
    }
    const float mx = xhalf_max(fmaxf(fmaxf(v[0], v[1]), fmaxf(v[2], v[3])));
#pragma unroll
    for (int b = 0; b < 4; ++b) v[b] = __builtin_amdgcn_exp2f((v[b] - mx) * csc);
    const float sc = gate * __builtin_amdgcn_rcpf(xhalf_sum((v[0] + v[1]) + (v[2] + v[3])));
    const u32 p01 = pk(v[0] * sc, v[1] * sc), p23 = pk(v[2] * sc, v[3] * sc);
    f32x16 oa = {};
    oa = mfma(vp[i], u32x4{0u, 0u, p01 & msk[0][0], p23 & msk[0][1]}, oa);
    oa = mfma(vf[i][0], u32x4{p01 & msk[1][0], p23 & msk[1][1], p01 & msk[2][0], p23 & msk[2][1]}, oa);
    oa = mfma(vf[i][1], u32x4{p01 & msk[3][0], p23 & msk[3][1], p01 & msk[4][0], p23 & msk[4][1]}, oa);
    op[i][0] = pk8(oa, 0); op[i][1] = pk8(oa, 1);
    if (i & 1) {
#pragma unroll
      for (int e = 0; e < 2; ++e) { auto t = __builtin_amdgcn_permlane32_swap(op[i - 1][1][e], op[i][0][e], false, false); op[i - 1][1][e] = t[0]; op[i][0][e] = t[1]; }
      const u32 ob = WS_A2 + T * A2_T + (1792 + 28 * (h0 + i - 1)) * 64;
      __builtin_amdgcn_raw_buffer_store_b128(op[i - 1][0], c.rws, lane * 16, ob, SC1);
      __builtin_amdgcn_raw_buffer_store_b128(op[i - 1][1], c.rws, hi ? OOB : lane * 16, ob + 1024, SC1);
      __builtin_amdgcn_raw_buffer_store_b128(op[i][0], c.rws, lane * 16, ob + 1536, SC1);
      __builtin_amdgcn_raw_buffer_store_b128(op[i][1], c.rws, lane * 16, ob + 2560, SC1);
    }
  }
  STAMP(5);
  wait_vm<0>();
  __builtin_amdgcn_raw_buffer_store_b64(u32x2{(u32)tag, (u32)(tag >> 32)}, c.rws, lane == 0 ? (28 + 2 * hf + cg) * 8 : OOB, WS_FLAG + (128 + T) * 256, SC1);
  STAMP(6);
  if (mt == 0 && hf == 0) {
#pragma unroll 1
    for (int i = 0; i < 4; ++i) spin(c.ws, WS_FLAG + 128 * 256 + (w * 4 + i) * 512 + lane * 8, true, tag);
    __builtin_amdgcn_s_barrier();
    if (w == 0) __builtin_amdgcn_raw_buffer_store_b64(u32x2{(u32)tag, (u32)(tag >> 32)}, c.rws, lane == 0 ? 0 : OOB, WS_E, SC1);
  }
}

__global__ __launch_bounds__(512, 2) void fused_kernel(const void* __restrict__ x, const void* __restrict__ wqkv, const void* __restrict__ wo,
                                                       const void* __restrict__ wg, const void* __restrict__ wu, const void* __restrict__ wd,
                                                       const void* __restrict__ gw, void* __restrict__ out, void* __restrict__ ws) {
  __shared__ __attribute__((aligned(1024))) char smem[163840];
  const int lane = threadIdx.x & 63, w = __builtin_amdgcn_readfirstlane(threadIdx.x >> 6);
  const int b = blockIdx.x, xc = b & 7, ix = b >> 3;
  const int mt = (xc >> 1) * 4 + (ix & 3), rl = ix >> 2, rh = xc & 1;
  u32 tdummy = 0;
  {
    u64 pc;
    asm volatile("s_getpc_b64 %0" : "=s"(pc));
    pc &= ~255ull;
    rsrc_t rc = mk((const void*)pc, CODE_BYTES);
    if (w == 7) asm volatile("buffer_load_dword %0, %1, %2, 0 offen" : "=v"(tdummy) : "v"(lane < 12 ? (ix * 12 + lane) * 128 : OOB), "s"(rc) : "memory");
  }
  Ctx c;
  c.sm = (LAS char*)smem;
  c.m0w = (u32)(size_t)c.sm + w * 1024;
  c.rx = mk(x, 2048 * 896); c.rqkv = mk(wqkv, 448 * 2688); c.rwg = mk(wg, 448 * 3584); c.rwu = mk(wu, 448 * 3584);
  c.rwd = mk(wd, 1792 * 896); c.rwo = mk(wo, 448 * 896); c.rws = mk(ws, WS_END);
  c.ws = (const char*)ws;
  {
    const int mq = xc >> 1;
    if (w == 0) { int i = ix * 56 + lane; asm volatile("buffer_load_dword %0, %1, %2, 0 offen" : "=v"(tdummy) : "v"(lane < 56 ? (xc * 1792 + i) * 128 : OOB), "s"(c.rx) : "memory"); }
    if (w == 1 || w == 2) { int i = ix * 49 + lane; int row = mq * 112 + i / 14, l = i % 14; l = rh * 7 + (l < 7 ? l : l + 7);
      u32 o = lane < 49 ? row * 3584 + l * 128 : OOB;
      if (w == 1) asm volatile("buffer_load_dword %0, %1, %2, 0 offen" : "=v"(tdummy) : "v"(o), "s"(c.rwg) : "memory");
      else asm volatile("buffer_load_dword %0, %1, %2, 0 offen" : "=v"(tdummy) : "v"(o), "s"(c.rwu) : "memory"); }
    if (w == 3) { int i = b * 37 + lane; asm volatile("buffer_load_dword %0, %1, %2, 0 offen" : "=v"(tdummy) : "v"(lane < 37 ? i * 128 : OOB), "s"(c.rqkv) : "memory"); }
    if (w == 4) { int i = b * 49 + lane; asm volatile("buffer_load_dword %0, %1, %2, 0 offen" : "=v"(tdummy) : "v"(lane < 49 ? i * 128 : OOB), "s"(c.rwd) : "memory"); }
    if (w == 5) { int i = b * 13 + lane; asm volatile("buffer_load_dword %0, %1, %2, 0 offen" : "=v"(tdummy) : "v"(lane < 13 ? i * 128 : OOB), "s"(c.rwo) : "memory"); }
  }
  c.td = tdummy;
  const int r8 = lane >> 3, c8 = lane & 7, R = 8 * w + r8;
  c.vx = (mt * 128 + 2 * R) * 896 + ((c8 ^ ((R >> 1) & 7)) << 4);
  const int sw = (c8 ^ ((r8 & 2) << 1)) << 4;
  if (rl < 7) {
    const int role = rh * 7 + rl;
    c.vkv = R * 2688 + 896 + role * 128 + sw;
    c.vg = R * 3584 + role * 128 + sw;
    c.vu = R * 3584 + role * 128 + (sw ^ 32);
    c.vw2 = R * 896 + rl * 128 + sw;
    if (rh) type_a<true>(c, w, lane, mt, role, xc * 4 + (ix & 3), rl, out);
    else type_a<false>(c, w, lane, mt, role, xc * 4 + (ix & 3), rl, out);
  } else {
    c.vkv = R * 2688 + rh * 384 + sw;
    type_b(c, w, lane, mt, rh, gw);
  }
}

extern "C" void kernel_launch(void* const* d_in, const int* in_sizes, int n_in, void* d_out, int out_size, void* d_ws, size_t ws_size,
                              hipStream_t stream) {
  fused_kernel<<<256, 512, 0, stream>>>(d_in[0], d_in[1], d_in[2], d_in[4], d_in[5], d_in[6], d_in[7], d_out, d_ws);
}
```

```cpp
#include <hip/hip_runtime.h>
#include <hip/hip_bf16.h>

typedef unsigned long long u64;
typedef unsigned u32;
typedef __bf16 bf16x8 __attribute__((ext_vector_type(8)));
typedef __bf16 bf16x2 __attribute__((ext_vector_type(2)));
typedef short s16x4 __attribute__((ext_vector_type(4)));
typedef u32 u32x4 __attribute__((ext_vector_type(4)));
typedef u32 u32x2 __attribute__((ext_vector_type(2)));
typedef float f32x16 __attribute__((ext_vector_type(16)));
typedef float f32x4 __attribute__((ext_vector_type(4)));
typedef __amdgpu_buffer_rsrc_t rsrc_t;
#define LAS __attribute__((address_space(3)))
#define GAS __attribute__((address_space(1)))
#define DI __device__ __forceinline__
#define SCHED() __builtin_amdgcn_sched_barrier(0)
template <int V> using IC = std::integral_constant<int, V>;

constexpr u32 WS_E = 0;
constexpr u32 WS_FLAG = 4096;
constexpr u32 WS_OMG = 32768;
constexpr u32 WS_K = 65536;
constexpr u32 KV_T = 28672;
constexpr u32 WS_V = WS_K + 64 * KV_T;
constexpr u32 WS_A2 = WS_V + 64 * KV_T;
constexpr u32 A2_T = 143360;
constexpr u32 WS_END = WS_A2 + 64 * A2_T;
constexpr u32 OOB = 0xFFFFFF00u;
constexpr int SC1 = 16;
constexpr int SLOT = 8192;
constexpr int SPIN_CAP = 1 << 15;

DI rsrc_t mk(const void* p, u32 bytes) { return __builtin_amdgcn_make_buffer_rsrc((void*)p, (short)0, (int)bytes, 0x00020000); }
DI void dma(u32 m0v, u32 voff, rsrc_t rs, u32 soff) {
  asm volatile("s_mov_b32 m0, %0\n\ts_nop 0\n\tbuffer_load_dwordx4 %1, %2, %3 offen lds" ::"s"(m0v), "v"(voff), "s"(rs), "s"(soff) : "memory");
}
DI u32x4 ld128_sc1(u32 voff, rsrc_t rs, u32 soff) {
  u32x4 v;
  asm volatile("buffer_load_dwordx4 %0, %1, %2, %3 offen sc1" : "=v"(v) : "v"(voff), "s"(rs), "s"(soff) : "memory");
  return v;
}
DI u32x2 ld64_sc1(u32 voff, rsrc_t rs, u32 soff) {
  u32x2 v;
  asm volatile("buffer_load_dwordx2 %0, %1, %2, %3 offen sc1" : "=v"(v) : "v"(voff), "s"(rs), "s"(soff) : "memory");
  return v;
}
DI u32 ld32_sc1(u32 voff, rsrc_t rs, u32 soff) {
  u32 v;
  asm volatile("buffer_load_dword %0, %1, %2, %3 offen sc1" : "=v"(v) : "v"(voff), "s"(rs), "s"(soff) : "memory");
  return v;
}
DI u32 touch(u32 voff, rsrc_t rs) {
  u32 v;
  asm volatile("buffer_load_dword %0, %1, %2, 0 offen" : "=v"(v) : "v"(voff), "s"(rs) : "memory");
  return v;
}
DI void st128(u32x4 v, rsrc_t rs, u32 voff, u32 soff) {
  asm volatile("buffer_store_dwordx4 %0, %1, %2, %3 offen sc1\n\ts_nop 1" ::"v"(v), "v"(voff), "s"(rs), "s"(soff) : "memory");
}
constexpr int clampvm(int n) { return n > 63 ? 63 : n; }
template <int N> DI void wait_vm() { static_assert(N >= 0, ""); asm volatile("s_waitcnt vmcnt(%0)" ::"n"(clampvm(N)) : "memory"); }
template <class T> DI void pin(T& v) { asm volatile("" : "+v"(v)); }

DI u32x4 trfrag(LAS char* p) {
  s16x4 lo = __builtin_amdgcn_ds_read_tr16_b64_v4i16((LAS s16x4*)p);
  s16x4 hi = __builtin_amdgcn_ds_read_tr16_b64_v4i16((LAS s16x4*)(p + 512));
  u32x2 a = __builtin_bit_cast(u32x2, lo), b = __builtin_bit_cast(u32x2, hi);
  return u32x4{a.x, a.y, b.x, b.y};
}
DI f32x16 mfma(u32x4 a, u32x4 b, f32x16 c) {
  return __builtin_amdgcn_mfma_f32_32x32x16_bf16(__builtin_bit_cast(bf16x8, a), __builtin_bit_cast(bf16x8, b), c, 0, 0, 0);
}
DI void mfma_at(f32x16& c, u32x4 a, u32x4 b) { c = mfma(a, b, c); asm volatile("" : "+v"(c)); }
DI u32 pk(float a, float b) { bf16x2 c; c[0] = (__bf16)a; c[1] = (__bf16)b; return __builtin_bit_cast(u32, c); }
DI u32x4 pk8(const f32x16& v, int s) { return u32x4{pk(v[8 * s], v[8 * s + 1]), pk(v[8 * s + 2], v[8 * s + 3]), pk(v[8 * s + 4], v[8 * s + 5]), pk(v[8 * s + 6], v[8 * s + 7])}; }
DI float glu(float g, float u) { return u * __builtin_amdgcn_rcpf(1.f + __builtin_amdgcn_exp2f(g * -1.4426950408889634f)); }
DI float xhalf_max(float v) { auto r = __builtin_amdgcn_permlane32_swap(__float_as_uint(v), __float_as_uint(v), false, false); return fmaxf(__uint_as_float(r[0]), __uint_as_float(r[1])); }
DI float xhalf_sum(float v) { auto r = __builtin_amdgcn_permlane32_swap(__float_as_uint(v), __float_as_uint(v), false, false); return __uint_as_float(r[0]) + __uint_as_float(r[1]); }

DI void spin(const char* ws, u32 off, bool need, u64 tag) {
  const GAS u64* p = (const GAS u64*)(ws + off);
#pragma unroll 1
  for (int i = 0; i < SPIN_CAP; ++i) {
    u64 v = __hip_atomic_load(p, __ATOMIC_RELAXED, __HIP_MEMORY_SCOPE_AGENT);
    if (__all(!need || v == tag)) break;
    __builtin_amdgcn_s_sleep(2);
  }
  asm volatile("" ::: "memory");
}
DI bool match(u32x2 v, bool need, u64 tag) { return __all(!need || (v.x == (u32)tag && v.y == (u32)(tag >> 32))); }
DI u64 tag_of(u32x2 ev) {
  u64 e = ((u64)__builtin_amdgcn_readfirstlane(ev.y) << 32) | (u32)__builtin_amdgcn_readfirstlane(ev.x);
  return e * 6364136223846793005ull + 1442695040888963407ull;
}

#define STAMP(i)
#define DIAGP
#define DIAGA
#define STAMP2(i)
template <int N> DI void sync_vm() { static_assert(N >= 0, ""); asm volatile("s_waitcnt vmcnt(%0)\n\ts_barrier" ::"n"(clampvm(N)) : "memory"); }

template <int I, int N, class Fn> DI void static_for(Fn&& f) {
  if constexpr (I < N) { f(IC<I>{}); static_for<I + 1, N>(f); }
}

#define PRO_DMA 16
#define P0_RATE 2
#define U_KVPUB 14
#define U_H1PUB 46
#define U_H2PUB 64
#define U_POLL1 54
#define U_POLL2 66
#define U_CHK2 69
#define U_POLL3 71
#define U_CHK3 74
#define LD_FIRST 6
#define LD_RATE 2
constexpr int NG = 98, U0 = 7;
constexpr int U_KVST = 0, U_H1STA = 30, U_H1STB = 32, U_H2ST = 56, U_CHK1 = 56;
constexpr int PRO_OPS = PRO_DMA + 1;
constexpr bool step_start(int u) { return u < 56 ? (u % 8 == 0) : (u < 91 && (u - 56) % 5 == 0); }
constexpr bool is_sync(int g) { return g < U0 ? true : step_start(g - U0 + 2); }
constexpr int last_seq_of_step(int u0) { return u0 < 56 ? 21 + (u0 / 4 + 2) * 2 - 1 : 49 + (u0 - 56) + 4; }
constexpr int need_seq(int g) { return g < 6 ? 3 * (g + 1) + 2 : (g == 6 ? last_seq_of_step(0) : last_seq_of_step(g - U0 + 2)); }
constexpr int freed_at(int g) { return g < U0 ? 3 * g : (g - U0 <= 56 ? 21 + 2 * ((g - U0) / 4) : 49 + (g - U0 - 56)); }
constexpr int freed(int g) { for (int j = g; j >= 0; --j) if (is_sync(j) || j == U0 + U_CHK1) return freed_at(j); return 0; }
constexpr int cum(int g) {
  int cc = PRO_DMA;
  for (int j = 0; j <= g; ++j) { int t = cc + (j < U0 ? P0_RATE : 1), cap = freed(j) + 20; t = t < cap ? t : cap; cc = t < 84 ? t : 84; }
  return cc;
}
constexpr int n_dma(int g) { return cum(g) - cum(g - 1); }
constexpr bool sched_ok() { for (int g = 0; g < NG; ++g) if (is_sync(g) && need_seq(g) >= cum(g - 1)) return false; return true; }
static_assert(sched_ok(), "a slot is needed before it is requested");
constexpr int n_st(int g) { int u = g - U0; return (u == U_POLL1) + (u == U_POLL2) + (u == U_POLL3) + 2 * (u == U_KVST) + (u == U_H1STA) + (u == U_H1STB) + 2 * (u == U_H2ST); }
constexpr int ld_rel(int k) { return k < LD_FIRST ? 0 : 1 + (k - LD_FIRST) / LD_RATE; }
constexpr int ld_iter(int it) { return U0 + (it < 14 ? U_CHK1 + ld_rel(it) : (it < 22 ? U_CHK2 + ld_rel(it - 14) : U_CHK3 + ld_rel(it - 22))); }
constexpr int ld_lo(int g) { for (int it = 0; it < 36; ++it) if (ld_iter(it) >= g) return it; return 36; }
constexpr int n_ld(int g) { return ld_lo(g + 1) - ld_lo(g); }
constexpr int ops_in(int g) { return n_st(g) + n_ld(g) + n_dma(g); }
constexpr int base(int g) { int t = PRO_OPS; for (int j = 0; j < g; ++j) t += ops_in(j); return t; }
constexpr int pos_st_last(int g) { return base(g) + n_st(g) - 1; }
constexpr int pos_ld(int it) { return base(ld_iter(it)) + n_st(ld_iter(it)) + (it - ld_lo(ld_iter(it))); }
constexpr int pos_dma(int n) {
  if (n < PRO_DMA) return n;
  for (int g = 0; g < NG; ++g)
    if (n < cum(g)) return base(g) + n_st(g) + n_ld(g) + (n - cum(g - 1));
  return -1000;
}
constexpr int imin(int a, int b) { return a < b ? a : b; }
constexpr int sync_cnt(int g) {
  int n = base(g) - pos_dma(need_seq(g)) - 1;
  if (g == U0 + U_KVPUB) n = imin(n, base(g) - pos_st_last(U0 + U_KVST) - 1);
  if (g == U0 + U_H1PUB) n = imin(n, base(g) - pos_st_last(U0 + U_H1STB) - 1);
  if (g == U0 + U_H2PUB) n = imin(n, base(g) - pos_st_last(U0 + U_H2ST) - 1);
  return n;
}
static_assert(is_sync(U0 + U_KVPUB) && is_sync(U0 + U_H1PUB) && is_sync(U0 + U_H2PUB) && is_sync(U0 + U_CHK2) && is_sync(U0 + U_CHK3), "");
static_assert(U_POLL1 < U_CHK1 && U_CHK1 < U_POLL2 && U_POLL2 < U_CHK2 && U_CHK2 < U_POLL3 && U_POLL3 < U_CHK3 && U_H2PUB <= U_CHK2, "");
constexpr int item_of(int i) { return i < 14 ? i : i + 1; }
constexpr int blk_of(int i) { return i < 14 ? i : (i < 21 ? i + 14 : i - 7); }
constexpr bool ld_ok() { for (int i = 0; i < 35; ++i) if (ld_iter(item_of(i)) > U0 + 56 + i) return false; for (int i = 1; i < 36; ++i) if (ld_iter(i) < ld_iter(i - 1)) return false; return true; }
static_assert(ld_ok(), "an operand is used before it is requested");

struct Ctx {
  LAS char* sm; u32 m0w;
  rsrc_t rx, rqkv, rwg, rwu, rwd, rwo, rws;
  const char* ws;
  rsrc_t rt; u32 toff;
  u32 vx, vkv, vg, vu, vw2;
};

template <int N> DI void issueA(const Ctx& c) {
  constexpr u32 dst = (N % 20) * SLOT;
  if constexpr (N < 21) {
    constexpr int kc = N / 3, j = N % 3;
    if constexpr (j < 2) dma(c.m0w + dst, c.vx, c.rx, j * 896 + kc * 128);
    else dma(c.m0w + dst, c.vkv, c.rqkv, kc * 64 * 2688);
  } else if constexpr (N < 49) {
    constexpr int kc = ((N - 21) / 2) % 7, ps = (N - 21) / 14;
    if constexpr ((N - 21) % 2 == 0) dma(c.m0w + dst, c.vg, c.rwg, kc * 64 * 3584 + ps * 1792);
    else dma(c.m0w + dst, c.vu, c.rwu, kc * 64 * 3584 + ps * 1792);
  } else if constexpr (blk_of(N - 49) < 28) dma(c.m0w + dst, c.vw2, c.rwd, blk_of(N - 49) * 64 * 896);
  else dma(c.m0w + dst, c.vw2, c.rwo, (blk_of(N - 49) - 28) * 64 * 896);
}

DI void type_a(Ctx& c, int w, int lane, int mt, int role, int R2, int n2, void* out DIAGP) {
  const int r = lane & 31, hi = lane >> 5, blk = (lane >> 4) & 1, q = (lane & 15) >> 2, p = lane & 3;
  const int sp = ((p & 1) << 1) | (p >> 1);
  const int rg = w & 3, cg = w >> 2, T = mt * 4 + rg;
  const bool isv = role >= 7;
  LAS char* sm = c.sm;
  static_for<0, PRO_DMA>([&](auto N) { issueA<N>(c); });
  u32x2 ev = ld64_sc1(c.toff, c.rt, 0);
  const int R = 32 * (rg & 1) + r, xm = (R >> 1) & 7;
  int xa[4];
#pragma unroll
  for (int s = 0; s < 4; ++s) xa[s] = (rg >> 1) * SLOT + R * 128 + (((2 * s + hi) ^ xm) << 4);
  const int rowb = (8 * hi + q) * 128, q2 = q & 2;
  const int akv = rowb + (((2 * cg + blk) ^ q2) << 5) + (isv ? p : sp) * 8;
  int agu[2];
#pragma unroll
  for (int i = 0; i < 2; ++i) agu[i] = blk * SLOT + rowb + (((2 * cg + i) ^ q2 ^ blk) << 5) + sp * 8;
  const int rt = w & 1, kq = w >> 1;
  int aw2[2];
#pragma unroll
  for (int i = 0; i < 2; ++i) aw2[i] = kq * 2048 + rowb + (((2 * i + blk) ^ q2) << 5) + p * 8;
  const int T2 = (R2 >> 1) * 4 + 2 * rt + (R2 & 1);

  u32x4 xf[28], kf[4], wf[2][2], af[35], hp[2];
  f32x16 akvacc = {}, g1[2] = {}, g2[2] = {}, ah[2] = {}, ao[2] = {};
  u32x2 pollv; u32 omg; u64 tag;
  const u32 lane16 = lane * 16;
  const u32 poll_off = WS_FLAG + (R2 >> 1) * 256 + (lane & 15) * 8;
  const bool need_h = (lane & 15) < 14;
  const u32 a2_off = WS_A2 + T2 * A2_T + kq * 1024 + lane16;
  const u32 h_off = WS_A2 + T * A2_T + (role * 4 + 2 * cg) * 1024 + lane16;

  auto read_kv = [&](auto J, auto S) {
    constexpr int j = J, sx = S;
    xf[4 * j + sx] = *(LAS u32x4*)(sm + xa[sx] + ((3 * j) % 20) * SLOT);
    kf[sx] = trfrag(sm + akv + ((3 * j + 2) % 20) * SLOT + sx * 2048);
  };
  auto read_w = [&](auto U, auto I) {
    constexpr int u = U, i = I;
    if constexpr (u < 56) {
      constexpr int sl = (21 + 2 * (u / 4)) % 20;
      const int wrap = sl == 19 ? blk * (-20 * SLOT) : 0;
      wf[u & 1][i] = trfrag(sm + agu[i] + wrap + sl * SLOT + (u % 4) * 2048);
    } else if constexpr (u < 91) {
      wf[u & 1][i] = trfrag(sm + aw2[i] + ((49 + u - 56) % 20) * SLOT);
    }
  };
  auto publish = [&](int cls) {
    if (w == 0) __builtin_amdgcn_raw_buffer_store_b64(u32x2{(u32)tag, (u32)(tag >> 32)}, c.rws, lane == 0 ? role * 8 : OOB, WS_FLAG + cls * 4096 + mt * 256, SC1);
  };
  auto load_item = [&](auto IT) {
    constexpr int it = IT;
    if constexpr (it == 14) omg = ld32_sc1(r * 4, c.rws, WS_OMG + T2 * 128);
    else { constexpr int i = it < 14 ? it : it - 1; af[i] = ld128_sc1(a2_off, c.rws, blk_of(i) * 4096); }
  };

  STAMP(1);
  sync_vm<PRO_OPS - 3>();
  STAMP(2);
  static_for<0, 4>([&](auto S) { read_kv(IC<0>{}, S); });
  SCHED();
  auto pass0 = [&](auto ISV) {
    static_for<0, U0>([&](auto G) {
      constexpr int g = G;
      sync_vm<sync_cnt(g)>();
      SCHED();
      static_for<0, 4>([&](auto S) {
        constexpr int sx = S;
        if constexpr (ISV) mfma_at(akvacc, xf[4 * g + sx], kf[sx]); else mfma_at(akvacc, kf[sx], xf[4 * g + sx]);
        SCHED();
        if constexpr (g < 6) read_kv(IC<g + 1>{}, S); else read_w(IC<sx / 2>{}, IC<sx % 2>{});
        if constexpr (cum(g - 1) + sx < cum(g)) issueA<cum(g - 1) + sx>(c);
        SCHED();
      });
    });
  };
  static_assert(P0_RATE <= 4, "");
  if (isv) pass0(IC<1>{}); else pass0(IC<0>{});
  auto check = [&](auto CNT, int cls, bool need) {
    wait_vm<CNT>();
    pin(pollv);
    if (w == 0 && !match(pollv, need, tag)) spin(c.ws, poll_off + cls * 4096, need, tag);
  };
  static_for<U0, NG>([&](auto G) {
    constexpr int g = G, u = g - U0, b = u & 1;
    if constexpr (u == 0) { STAMP(3); static_assert(pos_dma(need_seq(6)) > PRO_DMA, ""); pin(ev); tag = tag_of(ev); }
    if constexpr (u == 28) STAMP(4);
    if constexpr (u == 56) STAMP(5);
    if constexpr (u == 70) STAMP(8);
    if constexpr (u == 77) STAMP(9);
    if constexpr (is_sync(g)) STAMP2(16 + 2 * (u < 56 ? u / 8 : 7 + (u - 59) / 5));
    if constexpr (u == U_CHK2) check(IC<base(g) - base(U0 + U_POLL2) - 1>{}, 2, !need_h);
    if constexpr (u == U_CHK3) check(IC<base(g) - base(U0 + U_POLL3) - 1>{}, 2, need_h);
    if constexpr (is_sync(g)) {
      sync_vm<sync_cnt(g)>();
      STAMP2(17 + 2 * (u < 56 ? u / 8 : 7 + (u - 59) / 5));
    }
    if constexpr (u == U_KVPUB) publish(0);
    if constexpr (u == U_H1PUB) publish(1);
    if constexpr (u == U_H2PUB) publish(2);
    if constexpr (u == U_POLL1) pollv = ld64_sc1(w == 0 ? poll_off : OOB, c.rws, 4096);
    if constexpr (u == U_POLL2 || u == U_POLL3) pollv = ld64_sc1(w == 0 ? poll_off : OOB, c.rws, 8192);
    if constexpr (u == U_KVST) {
#pragma unroll
      for (int s = 0; s < 2; ++s) {
        const u32 off = isv ? WS_V + T * KV_T + (s * 2 + hi) * 7168 + ((role - 7) * 64 + cg * 32 + r) * 16
                            : WS_K + T * KV_T + (role * 8 + cg * 4 + 2 * s) * 512 + lane16;
        st128(pk8(akvacc, s), c.rws, off, 0);
      }
    }
    if constexpr (u == U_H1STA) st128(hp[0], c.rws, h_off, 0);
    if constexpr (u == U_H1STB) st128(hp[1], c.rws, h_off, 1024);
    if constexpr (u == U_H2ST) {
#pragma unroll
      for (int t = 0; t < 2; ++t) {
#pragma unroll
        for (int j = 0; j < 8; j += 2) hp[t][j / 2] = pk(glu(g2[t][j], g2[t][8 + j]), glu(g2[t][j + 1], g2[t][9 + j]));
        st128(hp[t], c.rws, h_off, (56 + t) * 1024);
        SCHED();
      }
    }
    asm volatile("" ::: "memory");
    if constexpr (u == U_CHK1) {
      STAMP2(44);
      check(IC<base(g) + n_st(g) - base(U0 + U_POLL1) - 1>{}, 1, need_h);
      asm volatile("s_barrier" ::: "memory");
      STAMP2(45);
    }
    static_for<ld_lo(g), ld_lo(g + 1)>(load_item);
    if constexpr (u >= 56) {
      wait_vm<base(g) + n_st(g) + n_ld(g) - pos_ld(item_of(u - 56)) - 1>();
      pin(af[u - 56]);
      if constexpr (u == 56) STAMP2(46);
      if constexpr (u == 70) STAMP2(47);
      if constexpr (u == 77) STAMP2(48);
    }
    SCHED();
    static_for<0, 2>([&](auto I) {
      constexpr int i = I;
      if constexpr (u < 28) mfma_at(g1[i], wf[b][i], xf[u]);
      else if constexpr (u < 56) mfma_at(g2[i], wf[b][i], xf[u - 28]);
      else if constexpr (blk_of(u - 56) < 28) mfma_at(ah[i], wf[b][i], af[u - 56]);
      else mfma_at(ao[i], wf[b][i], af[u - 56]);
      SCHED();
      read_w(IC<u + 2>{}, I);
      if constexpr (u >= 28 && u < 32) {
        constexpr int t = (u - 28) / 2, j = ((u - 28) % 2) * 4 + 2 * i;
        u32 hv = pk(glu(g1[t][j], g1[t][8 + j]), glu(g1[t][j + 1], g1[t][9 + j]));
        pin(hv);
        hp[t][j / 2] = hv;
      }
      if constexpr (i == 1) static_for<cum(g - 1), cum(g)>([&](auto N) { issueA<N>(c); });
      SCHED();
    });
  });
  pin(omg);
  STAMP(6);
  const float om = __uint_as_float(omg);
  LAS char* red = sm + 4 * SLOT;
#pragma unroll
  for (int t = 0; t < 2; ++t)
#pragma unroll
    for (int a = 0; a < 4; ++a) {
      f32x4 v;
#pragma unroll
      for (int e = 0; e < 4; ++e) v[e] = ah[t][4 * a + e] * om + ao[t][4 * a + e];
      *(LAS f32x4*)(red + kq * 16384 + (2 * r + rt) * 256 + (((8 * t + 2 * a + hi) ^ (r & 15)) << 4)) = v;
    }
  asm volatile("s_waitcnt lgkmcnt(0)\n\ts_barrier" ::: "memory");
  STAMP(7);
  {
    const int row = w * 8 + (lane >> 3), c8 = lane & 7, m = (row >> 1) & 15;
    f32x4 s0 = {}, s1 = {};
#pragma unroll
    for (int k = 0; k < 4; ++k) {
      s0 += *(LAS f32x4*)(red + k * 16384 + row * 256 + (((2 * c8) ^ m) << 4));
      s1 += *(LAS f32x4*)(red + k * 16384 + row * 256 + (((2 * c8 + 1) ^ m) << 4));
    }
    u32x4 o = {pk(s0[0], s0[1]), pk(s0[2], s0[3]), pk(s1[0], s1[1]), pk(s1[2], s1[3])};
    *(u32x4*)((char*)out + (size_t)(R2 * 64 + row) * 896 + n2 * 128 + c8 * 16) = o;
  }
}
static_assert(freed(U0 + 90) >= 72, "the reduction scratch (slots 4..11 = sequence 64..71) must be free at the end");

constexpr int PRO_B = 12;
constexpr int freedB(int g) { return 6 * ((g + 1) / 4); }
constexpr int cumB(int g) {
  int cc = PRO_B;
  for (int j = 0; j <= g; ++j) { int t = cc + 2, cap = freedB(j) + 18; t = t < cap ? t : cap; cc = t < 42 ? t : 42; }
  return cc;
}
#define GB_POLL 20
#define GB_CHK 23
constexpr int n_stB(int g) { return g == GB_POLL; }
constexpr int baseB(int g) { int t = PRO_B + 1; for (int j = 0; j < g; ++j) t += n_stB(j) + cumB(j) - cumB(j - 1); return t; }
constexpr int pos_dmaB(int n) {
  if (n < PRO_B) return n;
  for (int g = 0; g < 28; ++g) if (n < cumB(g)) return baseB(g) + n_stB(g) + (n - cumB(g - 1));
  return -1000;
}
constexpr int sync_cntB(int g) {
  int n = baseB(g) - pos_dmaB(6 * (g / 4 + 2) - 1) - 1;
  return n;
}
template <int N> DI void issueB(const Ctx& c) {
  constexpr int kc = N / 6, j = N % 6;
  constexpr u32 dst = (N % 18) * SLOT;
  if constexpr (j < 2) dma(c.m0w + dst, c.vx, c.rx, j * 896 + kc * 128);
  else dma(c.m0w + dst, c.vkv, c.rqkv, kc * 64 * 2688 + (j - 2) * 128);
}

DI void type_b(Ctx& c, int w, int lane, int mt, int hf, const void* gw DIAGP) {
  const int r = lane & 31, hi = lane >> 5, blk = (lane >> 4) & 1, q = (lane & 15) >> 2, p = lane & 3;
  const int sp = ((p & 1) << 1) | (p >> 1);
  const int rg = w & 3, cg = w >> 2, T = mt * 4 + rg;
  LAS char* sm = c.sm;
  if (w == 0) dma(c.m0w + 18 * SLOT, lane * 16, mk(gw, 896), 0);
  static_for<0, PRO_B>([&](auto N) { issueB<N>(c); });
  u32x2 ev = ld64_sc1(c.toff, c.rt, 0);
  const int R = 32 * (rg & 1) + r, xm = (R >> 1) & 7;
  int xa[4], aq[4];
#pragma unroll
  for (int s = 0; s < 4; ++s) xa[s] = (rg >> 1) * SLOT + R * 128 + (((2 * s + hi) ^ xm) << 4);
  const int h0 = 8 * hf + 4 * cg;
#pragma unroll
  for (int i = 0; i < 4; ++i) {
    const int col = 28 * (h0 + i) - 4 * (i & 1) - 192 * hf + 16 * blk + 4 * sp;
    aq[i] = (2 + (col >> 6)) * SLOT + (8 * hi + q) * 128 + (((col & 63) * 2) ^ ((q & 2) << 5));
  }
  const bool first = (mt == 0) && !(rg & 1);
  const int Tp = first ? T : ((rg & 1) ? T - 1 : T - 3);
  const u32 lane16 = lane * 16, lo16 = hi ? OOB : lane16;
  const u32 ob = WS_A2 + T * A2_T + (1792 + 28 * h0) * 64;
  const u32 omg_off = (hf == 0 && cg == 0 && hi == 0) ? r * 4 : OOB;
  const u32 poll_off = WS_FLAG + ((lane & 16) && mt > 0 ? mt - 1 : mt) * 256 + (lane & 15) * 8;
  const bool poll_need = (lane & 15) < 14;
  u32x4 xf[2], gf[2], wf[2][4];
  u32x4 kf[4][2], kp[2], vf[4][2], vp[4];
  f32x16 qa[4] = {};
  u32x2 pollv;
  const int sr = (r & 19) | ((r & 4) << 1) | ((r & 8) >> 1);
  const u32 kp_off = (28 * (r >> 3) - 4 * ((r >> 3) & 1)) * 64 + (hi * 32 + 24 + (r & 7)) * 16;
  const u32 v_off = hi * 7168 + sr * 16;
  auto load_kv = [&](auto IT) {
    constexpr int it = IT;
    if constexpr (it < 2) kp[it] = ld128_sc1(kp_off, c.rws, WS_K + Tp * KV_T + 28 * h0 * 64 + it * 1024);
    else {
      constexpr int i = (it - 2) / 5, k = (it - 2) % 5;
      const int wh = 28 * (h0 + i) - 4 * (i & 1);
      if constexpr (k < 2) kf[i][k] = ld128_sc1(lane16, c.rws, WS_K + T * KV_T + wh * 64 + k * 1024);
      else if constexpr (k == 2) vp[i] = ld128_sc1(v_off, c.rws, WS_V + Tp * KV_T + 14336 + wh * 16);
      else vf[i][k - 3] = ld128_sc1(v_off, c.rws, WS_V + T * KV_T + (k - 3) * 14336 + wh * 16);
    }
  };
  auto read_x = [&](auto G) {
    constexpr int g = G;
    if constexpr (g < 28) {
      xf[g & 1] = *(LAS u32x4*)(sm + xa[g % 4] + ((6 * (g / 4)) % 18) * SLOT);
      gf[g & 1] = *(LAS u32x4*)(sm + 18 * SLOT + g * 32 + hi * 16);
    }
  };
  auto read_w = [&](auto G, auto I) {
    constexpr int g = G;
    if constexpr (g < 28) wf[g & 1][I] = trfrag(sm + aq[I] + ((6 * (g / 4)) % 18) * SLOT + (g % 4) * 2048);
  };
  STAMP(1);
  sync_vm<PRO_B + 1 - 6>();
  STAMP(2);
  u64 tag;
  read_x(IC<0>{});
  static_for<0, 4>([&](auto I) { read_w(IC<0>{}, I); });
  SCHED();
  static_for<0, 28>([&](auto G) {
    constexpr int g = G, b = g & 1;
    if constexpr (g == GB_CHK) {
      STAMP(8);
      wait_vm<baseB(g) - baseB(GB_POLL) - 1>();
      pin(pollv);
      if (w == 0 && !match(pollv, poll_need, tag)) spin(c.ws, poll_off, poll_need, tag);
    }
    if constexpr (g % 4 == 3 && g < 27) { STAMP2(16 + 2 * (g / 4)); asm volatile("s_waitcnt lgkmcnt(0)" ::: "memory"); sync_vm<sync_cntB(g)>(); STAMP2(17 + 2 * (g / 4)); }
    if constexpr (g == 7) { pin(ev); tag = tag_of(ev); }
    if constexpr (g == GB_CHK) STAMP(9);
    if constexpr (g == GB_POLL) pollv = ld64_sc1(w == 0 ? poll_off : OOB, c.rws, 0);
    SCHED();
    static_for<0, 4>([&](auto I) {
      constexpr int i = I;
      mfma_at(qa[i], wf[b][i], xf[b]);
      SCHED();
      if constexpr (i == 0) read_x(IC<g + 1>{});
      read_w(IC<g + 1>{}, I);
      if constexpr (i < 2) {
        wf[b][2][2 * i] = r == 28 ? gf[b][2 * i] : wf[b][2][2 * i];
        wf[b][2][2 * i + 1] = r == 28 ? gf[b][2 * i + 1] : wf[b][2][2 * i + 1];
        pin(wf[b][2]);
      }
      if constexpr (i >= 2 && cumB(g - 1) + i - 2 < cumB(g)) issueB<cumB(g - 1) + i - 2>(c);
      if constexpr (g >= GB_CHK) if constexpr ((g - GB_CHK) * 4 + i < 22) load_kv(IC<(g - GB_CHK) * 4 + i>{});
      SCHED();
    });
  });
  static_assert(cumB(GB_CHK - 1) == 42, "no DMA after the first K/V load");
  static_for<(28 - GB_CHK) * 4, 22>(load_kv);
  STAMP(3);
  const float z = __uint_as_float(__builtin_amdgcn_permlane32_swap(__float_as_uint(qa[2][12]), __float_as_uint(qa[2][12]), false, false)[1]);
  const float gate = __builtin_amdgcn_rcpf(1.f + __builtin_amdgcn_exp2f(z * -1.4426950408889634f));
  const u32 omg = __float_as_uint(__builtin_amdgcn_rcpf(1.f + __builtin_amdgcn_exp2f(z * 1.4426950408889634f)));
  int ab[4];
  u32 msk[5][2];
#pragma unroll
  for (int b = 0; b < 4; ++b) ab[b] = (r >> 3) - ((r & 7) < 4 * hi + b ? 1 : 0);
#pragma unroll
  for (int a = 0; a < 5; ++a)
#pragma unroll
    for (int e = 0; e < 2; ++e) msk[a][e] = (ab[2 * e] == a - 1 ? 0xFFFFu : 0u) | (ab[2 * e + 1] == a - 1 ? 0xFFFF0000u : 0u);
  const float csc = 0.18898223650461362f * 1.4426950408889634f;
  u32x4 op[4][2];
  static_for<0, 4>([&](auto I) {
    constexpr int i = I;
    u32x4 qf[2] = {pk8(qa[i], 0), pk8(qa[i], 1)};
    if (i & 1) { qf[0][0] = hi ? qf[0][0] : 0u; qf[0][1] = hi ? qf[0][1] : 0u; }
    else       { qf[1][2] = hi ? 0u : qf[1][2]; qf[1][3] = hi ? 0u : qf[1][3]; }
    wait_vm<15 - 5 * i>();
    if constexpr (i == 0) { pin(kp[0]); pin(kp[1]); STAMP(4); }
    pin(kf[i][0]); pin(kf[i][1]); pin(vp[i]); pin(vf[i][0]); pin(vf[i][1]);
    f32x16 so = {}, sv = {};
    so = mfma(kf[i][0], qf[0], so); sv = mfma(kp[0], qf[0], sv);
    so = mfma(kf[i][1], qf[1], so); sv = mfma(kp[1], qf[1], sv);
    float v[4];
#pragma unroll
    for (int b = 0; b < 4; ++b) {
      float t = first ? -1e30f : sv[4 * i + b];
#pragma unroll
      for (int a = 0; a < 4; ++a) t = ab[b] == a ? so[4 * a + b] : t;
      v[b] = t;
    }
    const float mx = xhalf_max(fmaxf(fmaxf(v[0], v[1]), fmaxf(v[2], v[3])));
#pragma unroll
    for (int b = 0; b < 4; ++b) v[b] = __builtin_amdgcn_exp2f((v[b] - mx) * csc);
    const float sc = gate * __builtin_amdgcn_rcpf(xhalf_sum((v[0] + v[1]) + (v[2] + v[3])));
    const u32 p01 = pk(v[0] * sc, v[1] * sc), p23 = pk(v[2] * sc, v[3] * sc);
    f32x16 oa = {};
    oa = mfma(vp[i], u32x4{0u, 0u, p01 & msk[0][0], p23 & msk[0][1]}, oa);
    oa = mfma(vf[i][0], u32x4{p01 & msk[1][0], p23 & msk[1][1], p01 & msk[2][0], p23 & msk[2][1]}, oa);
    oa = mfma(vf[i][1], u32x4{p01 & msk[3][0], p23 & msk[3][1], p01 & msk[4][0], p23 & msk[4][1]}, oa);
    op[i][0] = pk8(oa, 0); op[i][1] = pk8(oa, 1);
  });
  asm volatile("" ::: "memory");
  __builtin_amdgcn_raw_buffer_store_b32(omg, c.rws, omg_off, WS_OMG + T * 128, SC1);
#pragma unroll
  for (int i = 1; i < 4; i += 2) {
#pragma unroll
    for (int e = 0; e < 2; ++e) { auto t = __builtin_amdgcn_permlane32_swap(op[i - 1][1][e], op[i][0][e], false, false); op[i - 1][1][e] = t[0]; op[i][0][e] = t[1]; }
    const u32 o2 = ob + (i >> 1) * 3584;
    st128(op[i - 1][0], c.rws, lane16, o2);
    st128(op[i - 1][1], c.rws, lo16, o2 + 1024);
    st128(op[i][0], c.rws, lane16, o2 + 1536);
    st128(op[i][1], c.rws, lane16, o2 + 2560);
  }
  STAMP(5);
  sync_vm<0>();
  if (w == 0) __builtin_amdgcn_raw_buffer_store_b64(u32x2{(u32)tag, (u32)(tag >> 32)}, c.rws, lane == 0 ? (14 + hf) * 8 : OOB, WS_FLAG + 8192 + mt * 256, SC1);
  STAMP(6);
  if (mt == 0 && hf == 0 && w == 0) {
#pragma unroll 1
    for (int i = 0; i < 4; ++i) spin(c.ws, WS_FLAG + 8192 + (i * 4 + (lane >> 4)) * 256 + (lane & 15) * 8, true, tag);
    __builtin_amdgcn_raw_buffer_store_b64(u32x2{(u32)tag, (u32)(tag >> 32)}, c.rws, lane == 0 ? 0 : OOB, WS_E, SC1);
  }
}

__global__ __launch_bounds__(512, 2) void fused_kernel(const void* __restrict__ x, const void* __restrict__ wqkv, const void* __restrict__ wo,
                                                       const void* __restrict__ wg, const void* __restrict__ wu, const void* __restrict__ wd,
                                                       const void* __restrict__ gw, void* __restrict__ out, void* __restrict__ ws) {
  __shared__ __attribute__((aligned(1024))) char smem[163840];
  const int lane = threadIdx.x & 63, w = __builtin_amdgcn_readfirstlane(threadIdx.x >> 6);
  const int b = blockIdx.x, xc = b & 7, ix = b >> 3;
  const int mq = xc >> 1, mt = mq * 4 + (ix & 3), rl = ix >> 2, rh = xc & 1;
  Ctx c;
  c.sm = (LAS char*)smem;
  c.m0w = (u32)(size_t)c.sm + w * 1024;
  c.rx = mk(x, 2048 * 896); c.rqkv = mk(wqkv, 448 * 2688); c.rwg = mk(wg, 448 * 3584); c.rwu = mk(wu, 448 * 3584);
  c.rwd = mk(wd, 1792 * 896); c.rwo = mk(wo, 448 * 896); c.rws = mk(ws, WS_END);
  c.ws = (const char*)ws;
  {
    const int i = ix * 49 + lane, row = mq * 112 + i / 14, l = i % 14;
    const u32 o12 = row * 3584 + (rh * 7 + (l < 7 ? l : l + 7)) * 128;
    const u32 o34 = ((w == 3 ? b * 49 : b * 13) + lane) * 128;
    const int n = w == 0 ? 1 : (w < 4 ? 49 : (w == 4 ? 13 : 0));
    c.toff = lane < n ? (w == 0 ? WS_E : (w < 3 ? o12 : o34)) : OOB;
    c.rt = w == 0 ? c.rws : (w == 1 ? c.rwg : (w == 2 ? c.rwu : (w == 3 ? c.rwd : c.rwo)));
  }
  const int r8 = lane >> 3, c8 = lane & 7, R = 8 * w + r8;
  c.vx = (mt * 128 + 2 * R) * 896 + ((c8 ^ ((R >> 1) & 7)) << 4);
  const int sw = (c8 ^ ((r8 & 2) << 1)) << 4;
  if (rl < 7) {
    const int role = rh * 7 + rl;
    c.vkv = R * 2688 + 896 + role * 128 + sw;
    c.vg = R * 3584 + role * 128 + sw;
    c.vu = R * 3584 + role * 128 + (sw ^ 32);
    c.vw2 = R * 896 + rl * 128 + sw;
    type_a(c, w, lane, mt, role, xc * 4 + (ix & 3), rl, out DIAGA);
  } else {
    c.vkv = R * 2688 + rh * 384 + sw;
    type_b(c, w, lane, mt, rh, gw DIAGA);
  }
}

extern "C" void kernel_launch(void* const* d_in, const int* in_sizes, int n_in, void* d_out, int out_size, void* d_ws, size_t ws_size,
                              hipStream_t stream) {
  fused_kernel<<<256, 512, 0, stream>>>(d_in[0], d_in[1], d_in[2], d_in[4], d_in[5], d_in[6], d_in[7], d_out, d_ws);
}
```

```cpp
#include <hip/hip_runtime.h>
#include <hip/hip_bf16.h>
#include <cstdio>

typedef unsigned long long u64;
typedef unsigned u32;
typedef __bf16 bf16x8 __attribute__((ext_vector_type(8)));
typedef __bf16 bf16x2 __attribute__((ext_vector_type(2)));
typedef short s16x4 __attribute__((ext_vector_type(4)));
typedef u32 u32x4 __attribute__((ext_vector_type(4)));
typedef u32 u32x2 __attribute__((ext_vector_type(2)));
typedef float f32x16 __attribute__((ext_vector_type(16)));
typedef float f32x4 __attribute__((ext_vector_type(4)));
typedef __amdgpu_buffer_rsrc_t rsrc_t;
#define LAS __attribute__((address_space(3)))
#define GAS __attribute__((address_space(1)))
#define DI __device__ __forceinline__
#define SCHED() __builtin_amdgcn_sched_barrier(0)
template <int V> using IC = std::integral_constant<int, V>;

constexpr u32 WS_E = 0;
constexpr u32 WS_FLAG = 4096;
constexpr u32 WS_OMG = 32768;
constexpr u32 WS_K = 65536;
constexpr u32 KV_T = 28672;
constexpr u32 WS_V = WS_K + 64 * KV_T;
constexpr u32 WS_A2 = WS_V + 64 * KV_T;
constexpr u32 A2_T = 143360;
constexpr u32 WS_END = WS_A2 + 64 * A2_T;
constexpr u32 OOB = 0xFFFFFF00u;
constexpr int SC1 = 16;
constexpr int SLOT = 8192;
constexpr int SPIN_CAP = 1 << 15;

DI rsrc_t mk(const void* p, u32 bytes) { return __builtin_amdgcn_make_buffer_rsrc((void*)p, (short)0, (int)bytes, 0x00020000); }
DI void dma(u32 m0v, u32 voff, rsrc_t rs, u32 soff) {
  asm volatile("s_mov_b32 m0, %0\n\ts_nop 0\n\tbuffer_load_dwordx4 %1, %2, %3 offen lds" ::"s"(m0v), "v"(voff), "s"(rs), "s"(soff) : "memory");
}
DI u32x4 ld128_sc1(u32 voff, rsrc_t rs, u32 soff) {
  u32x4 v;
  asm volatile("buffer_load_dwordx4 %0, %1, %2, %3 offen sc1" : "=v"(v) : "v"(voff), "s"(rs), "s"(soff) : "memory");
  return v;
}
DI u32x2 ld64_sc1(u32 voff, rsrc_t rs, u32 soff) {
  u32x2 v;
  asm volatile("buffer_load_dwordx2 %0, %1, %2, %3 offen sc1" : "=v"(v) : "v"(voff), "s"(rs), "s"(soff) : "memory");
  return v;
}
DI u32 ld32_sc1(u32 voff, rsrc_t rs, u32 soff) {
  u32 v;
  asm volatile("buffer_load_dword %0, %1, %2, %3 offen sc1" : "=v"(v) : "v"(voff), "s"(rs), "s"(soff) : "memory");
  return v;
}
DI u32 touch(u32 voff, rsrc_t rs) {
  u32 v;
  asm volatile("buffer_load_dword %0, %1, %2, 0 offen" : "=v"(v) : "v"(voff), "s"(rs) : "memory");
  return v;
}
DI void st128(u32x4 v, rsrc_t rs, u32 voff, u32 soff) {
  asm volatile("buffer_store_dwordx4 %0, %1, %2, %3 offen sc1\n\ts_nop 1" ::"v"(v), "v"(voff), "s"(rs), "s"(soff) : "memory");
}
constexpr int clampvm(int n) { return n > 63 ? 63 : n; }
template <int N> DI void wait_vm() { static_assert(N >= 0, ""); asm volatile("s_waitcnt vmcnt(%0)" ::"n"(clampvm(N)) : "memory"); }
template <class T> DI void pin(T& v) { asm volatile("" : "+v"(v)); }

DI u32x4 trfrag(LAS char* p) {
  s16x4 lo = __builtin_amdgcn_ds_read_tr16_b64_v4i16((LAS s16x4*)p);
  s16x4 hi = __builtin_amdgcn_ds_read_tr16_b64_v4i16((LAS s16x4*)(p + 512));
  u32x2 a = __builtin_bit_cast(u32x2, lo), b = __builtin_bit_cast(u32x2, hi);
  return u32x4{a.x, a.y, b.x, b.y};
}
DI f32x16 mfma(u32x4 a, u32x4 b, f32x16 c) {
  return __builtin_amdgcn_mfma_f32_32x32x16_bf16(__builtin_bit_cast(bf16x8, a), __builtin_bit_cast(bf16x8, b), c, 0, 0, 0);
}
DI void mfma_at(f32x16& c, u32x4 a, u32x4 b) { c = mfma(a, b, c); asm volatile("" : "+v"(c)); }
DI u32 pk(float a, float b) { bf16x2 c; c[0] = (__bf16)a; c[1] = (__bf16)b; return __builtin_bit_cast(u32, c); }
DI u32x4 pk8(const f32x16& v, int s) { return u32x4{pk(v[8 * s], v[8 * s + 1]), pk(v[8 * s + 2], v[8 * s + 3]), pk(v[8 * s + 4], v[8 * s + 5]), pk(v[8 * s + 6], v[8 * s + 7])}; }
DI float glu(float g, float u) { return u * __builtin_amdgcn_rcpf(1.f + __builtin_amdgcn_exp2f(g * -1.4426950408889634f)); }
DI float xhalf_max(float v) { auto r = __builtin_amdgcn_permlane32_swap(__float_as_uint(v), __float_as_uint(v), false, false); return fmaxf(__uint_as_float(r[0]), __uint_as_float(r[1])); }
DI float xhalf_sum(float v) { auto r = __builtin_amdgcn_permlane32_swap(__float_as_uint(v), __float_as_uint(v), false, false); return __uint_as_float(r[0]) + __uint_as_float(r[1]); }

DI void spin(const char* ws, u32 off, bool need, u64 tag) {
  const GAS u64* p = (const GAS u64*)(ws + off);
#pragma unroll 1
  for (int i = 0; i < SPIN_CAP; ++i) {
    u64 v = __hip_atomic_load(p, __ATOMIC_RELAXED, __HIP_MEMORY_SCOPE_AGENT);
    if (__all(!need || v == tag)) break;
    __builtin_amdgcn_s_sleep(2);
  }
  asm volatile("" ::: "memory");
}
DI bool match(u32x2 v, bool need, u64 tag) { return __all(!need || (v.x == (u32)tag && v.y == (u32)(tag >> 32))); }
DI u64 tag_of(u32x2 ev) {
  u64 e = ((u64)__builtin_amdgcn_readfirstlane(ev.y) << 32) | (u32)__builtin_amdgcn_readfirstlane(ev.x);
  return e * 6364136223846793005ull + 1442695040888963407ull;
}

#define STAMP(i)
#define DIAGP
#define DIAGA
#define STAMP2(i)
template <int N> DI void sync_vm() { static_assert(N >= 0, ""); asm volatile("s_waitcnt vmcnt(%0)\n\ts_barrier" ::"n"(clampvm(N)) : "memory"); }

template <int I, int N, class Fn> DI void static_for(Fn&& f) {
  if constexpr (I < N) { f(IC<I>{}); static_for<I + 1, N>(f); }
}

#define PRO_DMA 16
#define P0_RATE 2
#define U_KVPUB 14
#define U_H1PUB 46
#define U_H2PUB 64
#define U_POLL1 54
#define U_POLL2 66
#define U_CHK2 69
#define U_POLL3 71
#define U_CHK3 74
#define LD_FIRST 6
#define LD_RATE 2
constexpr int NG = 98, U0 = 7;
constexpr int U_KVST = 0, U_H1STA = 30, U_H1STB = 32, U_H2ST = 56, U_CHK1 = 56;
constexpr int PRO_OPS = PRO_DMA + 1;
constexpr bool step_start(int u) { return u < 56 ? (u % 8 == 0) : (u < 91 && (u - 56) % 5 == 0); }
constexpr bool is_sync(int g) { return g < U0 ? true : step_start(g - U0 + 2); }
constexpr int last_seq_of_step(int u0) { return u0 < 56 ? 21 + (u0 / 4 + 2) * 2 - 1 : 49 + (u0 - 56) + 4; }
constexpr int need_seq(int g) { return g < 6 ? 3 * (g + 1) + 2 : (g == 6 ? last_seq_of_step(0) : last_seq_of_step(g - U0 + 2)); }
constexpr int freed_at(int g) { return g < U0 ? 3 * g : (g - U0 <= 56 ? 21 + 2 * ((g - U0) / 4) : 49 + (g - U0 - 56)); }
constexpr int freed(int g) { for (int j = g; j >= 0; --j) if (is_sync(j) || j == U0 + U_CHK1) return freed_at(j); return 0; }
constexpr int cum(int g) {
  int cc = PRO_DMA;
  for (int j = 0; j <= g; ++j) { int t = cc + (j < U0 ? P0_RATE : 1), cap = freed(j) + 20; t = t < cap ? t : cap; cc = t < 84 ? t : 84; }
  return cc;
}
constexpr int n_dma(int g) { return cum(g) - cum(g - 1); }
constexpr bool sched_ok() { for (int g = 0; g < NG; ++g) if (is_sync(g) && need_seq(g) >= cum(g - 1)) return false; return true; }
static_assert(sched_ok(), "a slot is needed before it is requested");
constexpr int n_st(int g) { int u = g - U0; return (u == U_POLL1) + (u == U_POLL2) + (u == U_POLL3) + 2 * (u == U_KVST) + (u == U_H1STA) + (u == U_H1STB) + 2 * (u == U_H2ST); }
constexpr int ld_rel(int k) { return k < LD_FIRST ? 0 : 1 + (k - LD_FIRST) / LD_RATE; }
constexpr int ld_iter(int it) { return U0 + (it < 14 ? U_CHK1 + ld_rel(it) : (it < 22 ? U_CHK2 + ld_rel(it - 14) : U_CHK3 + ld_rel(it - 22))); }
constexpr int ld_lo(int g) { for (int it = 0; it < 36; ++it) if (ld_iter(it) >= g) return it; return 36; }
constexpr int n_ld(int g) { return ld_lo(g + 1) - ld_lo(g); }
constexpr int ops_in(int g) { return n_st(g) + n_ld(g) + n_dma(g); }
constexpr int base(int g) { int t = PRO_OPS; for (int j = 0; j < g; ++j) t += ops_in(j); return t; }
constexpr int pos_st_last(int g) { return base(g) + n_st(g) - 1; }
constexpr int pos_ld(int it) { return base(ld_iter(it)) + n_st(ld_iter(it)) + (it - ld_lo(ld_iter(it))); }
constexpr int pos_dma(int n) {
  if (n < PRO_DMA) return n;
  for (int g = 0; g < NG; ++g)
    if (n < cum(g)) return base(g) + n_st(g) + n_ld(g) + (n - cum(g - 1));
  return -1000;
}
constexpr int imin(int a, int b) { return a < b ? a : b; }
constexpr int sync_cnt(int g) {
  int n = base(g) - pos_dma(need_seq(g)) - 1;
  if (g == U0 + U_KVPUB) n = imin(n, base(g) - pos_st_last(U0 + U_KVST) - 1);
  if (g == U0 + U_H1PUB) n = imin(n, base(g) - pos_st_last(U0 + U_H1STB) - 1);
  if (g == U0 + U_H2PUB) n = imin(n, base(g) - pos_st_last(U0 + U_H2ST) - 1);
  return n;
}
static_assert(is_sync(U0 + U_KVPUB) && is_sync(U0 + U_H1PUB) && is_sync(U0 + U_H2PUB) && is_sync(U0 + U_CHK2) && is_sync(U0 + U_CHK3), "");
static_assert(U_POLL1 < U_CHK1 && U_CHK1 < U_POLL2 && U_POLL2 < U_CHK2 && U_CHK2 < U_POLL3 && U_POLL3 < U_CHK3 && U_H2PUB <= U_CHK2, "");
constexpr int item_of(int i) { return i < 14 ? i : i + 1; }
constexpr int blk_of(int i) { return i < 14 ? i : (i < 21 ? i + 14 : i - 7); }
constexpr bool ld_ok() { for (int i = 0; i < 35; ++i) if (ld_iter(item_of(i)) > U0 + 56 + i) return false; for (int i = 1; i < 36; ++i) if (ld_iter(i) < ld_iter(i - 1)) return false; return true; }
static_assert(ld_ok(), "an operand is used before it is requested");

struct Ctx {
  LAS char* sm; u32 m0w;
  rsrc_t rx, rqkv, rwg, rwu, rwd, rwo, rws;
  const char* ws;
  rsrc_t rt; u32 toff;
  u32 vx, vkv, vg, vu, vw2;
};

template <int N> DI void issueA(const Ctx& c) {
  constexpr u32 dst = (N % 20) * SLOT;
  if constexpr (N < 21) {
    constexpr int kc = N / 3, j = N % 3;
    if constexpr (j < 2) dma(c.m0w + dst, c.vx, c.rx, j * 896 + kc * 128);
    else dma(c.m0w + dst, c.vkv, c.rqkv, kc * 64 * 2688);
  } else if constexpr (N < 49) {
    constexpr int kc = ((N - 21) / 2) % 7, ps = (N - 21) / 14;
    if constexpr ((N - 21) % 2 == 0) dma(c.m0w + dst, c.vg, c.rwg, kc * 64 * 3584 + ps * 1792);
    else dma(c.m0w + dst, c.vu, c.rwu, kc * 64 * 3584 + ps * 1792);
  } else if constexpr (blk_of(N - 49) < 28) dma(c.m0w + dst, c.vw2, c.rwd, blk_of(N - 49) * 64 * 896);
  else dma(c.m0w + dst, c.vw2, c.rwo, (blk_of(N - 49) - 28) * 64 * 896);
}

DI void type_a(Ctx& c, int w, int lane, int mt, int role, int R2, int n2, void* out DIAGP) {
  const int r = lane & 31, hi = lane >> 5, blk = (lane >> 4) & 1, q = (lane & 15) >> 2, p = lane & 3;
  const int sp = ((p & 1) << 1) | (p >> 1);
  const int rg = w & 3, cg = w >> 2, T = mt * 4 + rg;
  const bool isv = role >= 7;
  LAS char* sm = c.sm;
  static_for<0, PRO_DMA>([&](auto N) { issueA<N>(c); });
  u32x2 ev = ld64_sc1(c.toff, c.rt, 0);
  const int R = 32 * (rg & 1) + r, xm = (R >> 1) & 7;
  int xa[4];
#pragma unroll
  for (int s = 0; s < 4; ++s) xa[s] = (rg >> 1) * SLOT + R * 128 + (((2 * s + hi) ^ xm) << 4);
  const int rowb = (8 * hi + q) * 128, q2 = q & 2;
  const int akv = rowb + (((2 * cg + blk) ^ q2) << 5) + (isv ? p : sp) * 8;
  int agu[2];
#pragma unroll
  for (int i = 0; i < 2; ++i) agu[i] = blk * SLOT + rowb + (((2 * cg + i) ^ q2 ^ blk) << 5) + sp * 8;
  const int rt = w & 1, kq = w >> 1;
  int aw2[2];
#pragma unroll
  for (int i = 0; i < 2; ++i) aw2[i] = kq * 2048 + rowb + (((2 * i + blk) ^ q2) << 5) + p * 8;
  const int T2 = (R2 >> 1) * 4 + 2 * rt + (R2 & 1);

  u32x4 xf[28], kf[4], wf[2][2], af[35], hp[2];
  f32x16 akvacc = {}, g1[2] = {}, g2[2] = {}, ah[2] = {}, ao[2] = {};
  u32x2 pollv; u32 omg; u64 tag;
  const u32 lane16 = lane * 16;
  const u32 poll_off = WS_FLAG + (R2 >> 1) * 256 + (lane & 15) * 8;
  const bool need_h = (lane & 15) < 14;
  const u32 a2_off = WS_A2 + T2 * A2_T + kq * 1024 + lane16;
  const u32 h_off = WS_A2 + T * A2_T + (role * 4 + 2 * cg) * 1024 + lane16;

  auto read_kv = [&](auto J, auto S) {
    constexpr int j = J, sx = S;
    xf[4 * j + sx] = *(LAS u32x4*)(sm + xa[sx] + ((3 * j) % 20) * SLOT);
    kf[sx] = trfrag(sm + akv + ((3 * j + 2) % 20) * SLOT + sx * 2048);
  };
  auto read_w = [&](auto U, auto I) {
    constexpr int u = U, i = I;
    if constexpr (u < 56) {
      constexpr int sl = (21 + 2 * (u / 4)) % 20;
      const int wrap = sl == 19 ? blk * (-20 * SLOT) : 0;
      wf[u & 1][i] = trfrag(sm + agu[i] + wrap + sl * SLOT + (u % 4) * 2048);
    } else if constexpr (u < 91) {
      wf[u & 1][i] = trfrag(sm + aw2[i] + ((49 + u - 56) % 20) * SLOT);
    }
  };
  auto publish = [&](int cls) {
    if (w == 0) __builtin_amdgcn_raw_buffer_store_b64(u32x2{(u32)tag, (u32)(tag >> 32)}, c.rws, lane == 0 ? role * 8 : OOB, WS_FLAG + cls * 4096 + mt * 256, SC1);
  };
  auto load_item = [&](auto IT) {
    constexpr int it = IT;
    if constexpr (it == 14) omg = ld32_sc1(r * 4, c.rws, WS_OMG + T2 * 128);
    else { constexpr int i = it < 14 ? it : it - 1; af[i] = ld128_sc1(a2_off, c.rws, blk_of(i) * 4096); }
  };

  STAMP(1);
  sync_vm<PRO_OPS - 3>();
  STAMP(2);
  static_for<0, 4>([&](auto S) { read_kv(IC<0>{}, S); });
  SCHED();
  auto pass0 = [&](auto ISV) {
    static_for<0, U0>([&](auto G) {
      constexpr int g = G;
      sync_vm<sync_cnt(g)>();
      SCHED();
      static_for<0, 4>([&](auto S) {
        constexpr int sx = S;
        if constexpr (ISV) mfma_at(akvacc, xf[4 * g + sx], kf[sx]); else mfma_at(akvacc, kf[sx], xf[4 * g + sx]);
        SCHED();
        if constexpr (g < 6) read_kv(IC<g + 1>{}, S); else read_w(IC<sx / 2>{}, IC<sx % 2>{});
        if constexpr (cum(g - 1) + sx < cum(g)) issueA<cum(g - 1) + sx>(c);
        SCHED();
      });
    });
  };
  static_assert(P0_RATE <= 4, "");
  if (isv) pass0(IC<1>{}); else pass0(IC<0>{});
  auto check = [&](auto CNT, int cls, bool need) {
    wait_vm<CNT>();
    pin(pollv);
    if (w == 0 && !match(pollv, need, tag)) spin(c.ws, poll_off + cls * 4096, need, tag);
  };
  static_for<U0, NG>([&](auto G) {
    constexpr int g = G, u = g - U0, b = u & 1;
    if constexpr (u == 0) { STAMP(3); static_assert(pos_dma(need_seq(6)) > PRO_DMA, ""); pin(ev); tag = tag_of(ev); }
    if constexpr (u == 28) STAMP(4);
    if constexpr (u == 56) STAMP(5);
    if constexpr (u == 70) STAMP(8);
    if constexpr (u == 77) STAMP(9);
    if constexpr (is_sync(g)) STAMP2(16 + 2 * (u < 56 ? u / 8 : 7 + (u - 59) / 5));
    if constexpr (u == U_CHK2) check(IC<base(g) - base(U0 + U_POLL2) - 1>{}, 2, !need_h);
    if constexpr (u == U_CHK3) check(IC<base(g) - base(U0 + U_POLL3) - 1>{}, 2, need_h);
    if constexpr (is_sync(g)) {
      sync_vm<sync_cnt(g)>();
      STAMP2(17 + 2 * (u < 56 ? u / 8 : 7 + (u - 59) / 5));
    }
    if constexpr (u == U_KVPUB) publish(0);
    if constexpr (u == U_H1PUB) publish(1);
    if constexpr (u == U_H2PUB) publish(2);
    if constexpr (u == U_POLL1) pollv = ld64_sc1(w == 0 ? poll_off : OOB, c.rws, 4096);
    if constexpr (u == U_POLL2 || u == U_POLL3) pollv = ld64_sc1(w == 0 ? poll_off : OOB, c.rws, 8192);
    if constexpr (u == U_KVST) {
#pragma unroll
      for (int s = 0; s < 2; ++s) {
        const u32 off = isv ? WS_V + T * KV_T + (s * 2 + hi) * 7168 + ((role - 7) * 64 + cg * 32 + r) * 16
                            : WS_K + T * KV_T + (role * 8 + cg * 4 + 2 * s) * 512 + lane16;
        st128(pk8(akvacc, s), c.rws, off, 0);
      }
    }
    if constexpr (u == U_H1STA) st128(hp[0], c.rws, h_off, 0);
    if constexpr (u == U_H1STB) st128(hp[1], c.rws, h_off, 1024);
    if constexpr (u == U_H2ST) {
#pragma unroll
      for (int t = 0; t < 2; ++t) {
#pragma unroll
        for (int j = 0; j < 8; j += 2) hp[t][j / 2] = pk(glu(g2[t][j], g2[t][8 + j]), glu(g2[t][j + 1], g2[t][9 + j]));
        st128(hp[t], c.rws, h_off, (56 + t) * 1024);
        SCHED();
      }
    }
    asm volatile("" ::: "memory");
    if constexpr (u == U_CHK1) {
      STAMP2(44);
      check(IC<base(g) + n_st(g) - base(U0 + U_POLL1) - 1>{}, 1, need_h);
      asm volatile("s_barrier" ::: "memory");
      STAMP2(45);
    }
    static_for<ld_lo(g), ld_lo(g + 1)>(load_item);
    if constexpr (u >= 56) {
      wait_vm<base(g) + n_st(g) + n_ld(g) - pos_ld(item_of(u - 56)) - 1>();
      pin(af[u - 56]);
      if constexpr (u == 56) STAMP2(46);
      if constexpr (u == 70) STAMP2(47);
      if constexpr (u == 77) STAMP2(48);
    }
    SCHED();
    static_for<0, 2>([&](auto I) {
      constexpr int i = I;
      if constexpr (u < 28) mfma_at(g1[i], wf[b][i], xf[u]);
      else if constexpr (u < 56) mfma_at(g2[i], wf[b][i], xf[u - 28]);
      else if constexpr (blk_of(u - 56) < 28) mfma_at(ah[i], wf[b][i], af[u - 56]);
      else mfma_at(ao[i], wf[b][i], af[u - 56]);
      SCHED();
      read_w(IC<u + 2>{}, I);
      if constexpr (u >= 28 && u < 32) {
        constexpr int t = (u - 28) / 2, j = ((u - 28) % 2) * 4 + 2 * i;
        u32 hv = pk(glu(g1[t][j], g1[t][8 + j]), glu(g1[t][j + 1], g1[t][9 + j]));
        pin(hv);
        hp[t][j / 2] = hv;
      }
      if constexpr (i == 1) static_for<cum(g - 1), cum(g)>([&](auto N) { issueA<N>(c); });
      SCHED();
    });
  });
  pin(omg);
  STAMP(6);
  const float om = __uint_as_float(omg);
  LAS char* red = sm + 4 * SLOT;
#pragma unroll
  for (int t = 0; t < 2; ++t)
#pragma unroll
    for (int a = 0; a < 4; ++a) {
      f32x4 v;
#pragma unroll
      for (int e = 0; e < 4; ++e) v[e] = ah[t][4 * a + e] * om + ao[t][4 * a + e];
      *(LAS f32x4*)(red + kq * 16384 + (2 * r + rt) * 256 + (((8 * t + 2 * a + hi) ^ (r & 15)) << 4)) = v;
    }
  asm volatile("s_waitcnt lgkmcnt(0)\n\ts_barrier" ::: "memory");
  STAMP(7);
  {
    const int row = w * 8 + (lane >> 3), c8 = lane & 7, m = (row >> 1) & 15;
    f32x4 s0 = {}, s1 = {};
#pragma unroll
    for (int k = 0; k < 4; ++k) {
      s0 += *(LAS f32x4*)(red + k * 16384 + row * 256 + (((2 * c8) ^ m) << 4));
      s1 += *(LAS f32x4*)(red + k * 16384 + row * 256 + (((2 * c8 + 1) ^ m) << 4));
    }
    u32x4 o = {pk(s0[0], s0[1]), pk(s0[2], s0[3]), pk(s1[0], s1[1]), pk(s1[2], s1[3])};
    *(u32x4*)((char*)out + (size_t)(R2 * 64 + row) * 896 + n2 * 128 + c8 * 16) = o;
  }
}
static_assert(freed(U0 + 90) >= 72, "the reduction scratch (slots 4..11 = sequence 64..71) must be free at the end");

constexpr int PRO_B = 12;
constexpr int freedB(int g) { return 6 * ((g + 1) / 4); }
constexpr int cumB(int g) {
  int cc = PRO_B;
  for (int j = 0; j <= g; ++j) { int t = cc + 2, cap = freedB(j) + 18; t = t < cap ? t : cap; cc = t < 42 ? t : 42; }
  return cc;
}
#define GB_POLL 20
#define GB_CHK 23
constexpr int n_stB(int g) { return g == GB_POLL; }
constexpr int baseB(int g) { int t = PRO_B + 1; for (int j = 0; j < g; ++j) t += n_stB(j) + cumB(j) - cumB(j - 1); return t; }
constexpr int pos_dmaB(int n) {
  if (n < PRO_B) return n;
  for (int g = 0; g < 28; ++g) if (n < cumB(g)) return baseB(g) + n_stB(g) + (n - cumB(g - 1));
  return -1000;
}
constexpr int sync_cntB(int g) {
  int n = baseB(g) - pos_dmaB(6 * (g / 4 + 2) - 1) - 1;
  return n;
}
template <int N> DI void issueB(const Ctx& c) {
  constexpr int kc = N / 6, j = N % 6;
  constexpr u32 dst = (N % 18) * SLOT;
  if constexpr (j < 2) dma(c.m0w + dst, c.vx, c.rx, j * 896 + kc * 128);
  else dma(c.m0w + dst, c.vkv, c.rqkv, kc * 64 * 2688 + (j - 2) * 128);
}

DI void type_b(Ctx& c, int w, int lane, int mt, int hf, const void* gw DIAGP) {
  const int r = lane & 31, hi = lane >> 5, blk = (lane >> 4) & 1, q = (lane & 15) >> 2, p = lane & 3;
  const int sp = ((p & 1) << 1) | (p >> 1);
  const int rg = w & 3, cg = w >> 2, T = mt * 4 + rg;
  LAS char* sm = c.sm;
  if (w == 0) dma(c.m0w + 18 * SLOT, lane * 16, mk(gw, 896), 0);
  static_for<0, PRO_B>([&](auto N) { issueB<N>(c); });
  u32x2 ev = ld64_sc1(c.toff, c.rt, 0);
  const int R = 32 * (rg & 1) + r, xm = (R >> 1) & 7;
  int xa[4], aq[4];
#pragma unroll
  for (int s = 0; s < 4; ++s) xa[s] = (rg >> 1) * SLOT + R * 128 + (((2 * s + hi) ^ xm) << 4);
  const int h0 = 8 * hf + 4 * cg;
#pragma unroll
  for (int i = 0; i < 4; ++i) {
    const int col = 28 * (h0 + i) - 4 * (i & 1) - 192 * hf + 16 * blk + 4 * sp;
    aq[i] = (2 + (col >> 6)) * SLOT + (8 * hi + q) * 128 + (((col & 63) * 2) ^ ((q & 2) << 5));
  }
  const bool first = (mt == 0) && !(rg & 1);
  const int Tp = first ? T : ((rg & 1) ? T - 1 : T - 3);
  const u32 lane16 = lane * 16, lo16 = hi ? OOB : lane16;
  const u32 ob = WS_A2 + T * A2_T + (1792 + 28 * h0) * 64;
  const u32 omg_off = (hf == 0 && cg == 0 && hi == 0) ? r * 4 : OOB;
  const u32 poll_off = WS_FLAG + ((lane & 16) && mt > 0 ? mt - 1 : mt) * 256 + (lane & 15) * 8;
  const bool poll_need = (lane & 15) < 14;
  u32x4 xf[2], gf[2], wf[2][4];
  u32x4 kf[4][2], kp[2], vf[4][2], vp[4];
  f32x16 qa[4] = {};
  u32x2 pollv;
  const int sr = (r & 19) | ((r & 4) << 1) | ((r & 8) >> 1);
  const u32 kp_off = (28 * (r >> 3) - 4 * ((r >> 3) & 1)) * 64 + (hi * 32 + 24 + (r & 7)) * 16;
  const u32 v_off = hi * 7168 + sr * 16;
  auto load_kv = [&](auto IT) {
    constexpr int it = IT;
    if constexpr (it < 2) kp[it] = ld128_sc1(kp_off, c.rws, WS_K + Tp * KV_T + 28 * h0 * 64 + it * 1024);
    else {
      constexpr int i = (it - 2) / 5, k = (it - 2) % 5;
      const int wh = 28 * (h0 + i) - 4 * (i & 1);
      if constexpr (k < 2) kf[i][k] = ld128_sc1(lane16, c.rws, WS_K + T * KV_T + wh * 64 + k * 1024);
      else if constexpr (k == 2) vp[i] = ld128_sc1(v_off, c.rws, WS_V + Tp * KV_T + 14336 + wh * 16);
      else vf[i][k - 3] = ld128_sc1(v_off, c.rws, WS_V + T * KV_T + (k - 3) * 14336 + wh * 16);
    }
  };
  auto read_x = [&](auto G) {
    constexpr int g = G;
    if constexpr (g < 28) {
      xf[g & 1] = *(LAS u32x4*)(sm + xa[g % 4] + ((6 * (g / 4)) % 18) * SLOT);
      gf[g & 1] = *(LAS u32x4*)(sm + 18 * SLOT + g * 32 + hi * 16);
    }
  };
  auto read_w = [&](auto G, auto I) {
    constexpr int g = G;
    if constexpr (g < 28) wf[g & 1][I] = trfrag(sm + aq[I] + ((6 * (g / 4)) % 18) * SLOT + (g % 4) * 2048);
  };
  STAMP(1);
  sync_vm<PRO_B + 1 - 6>();
  STAMP(2);
  u64 tag;
  read_x(IC<0>{});
  static_for<0, 4>([&](auto I) { read_w(IC<0>{}, I); });
  SCHED();
  static_for<0, 28>([&](auto G) {
    constexpr int g = G, b = g & 1;
    if constexpr (g == GB_CHK) {
      STAMP(8);
      wait_vm<baseB(g) - baseB(GB_POLL) - 1>();
      pin(pollv);
      if (w == 0 && !match(pollv, poll_need, tag)) spin(c.ws, poll_off, poll_need, tag);
    }
    if constexpr (g % 4 == 3 && g < 27) { STAMP2(16 + 2 * (g / 4)); asm volatile("s_waitcnt lgkmcnt(0)" ::: "memory"); sync_vm<sync_cntB(g)>(); STAMP2(17 + 2 * (g / 4)); }
    if constexpr (g == 7) { pin(ev); tag = tag_of(ev); }
    if constexpr (g == GB_CHK) STAMP(9);
    if constexpr (g == GB_POLL) pollv = ld64_sc1(w == 0 ? poll_off : OOB, c.rws, 0);
    SCHED();
    static_for<0, 4>([&](auto I) {
      constexpr int i = I;
      mfma_at(qa[i], wf[b][i], xf[b]);
      SCHED();
      if constexpr (i == 0) read_x(IC<g + 1>{});
      read_w(IC<g + 1>{}, I);
      if constexpr (i < 2) {
        wf[b][2][2 * i] = r == 28 ? gf[b][2 * i] : wf[b][2][2 * i];
        wf[b][2][2 * i + 1] = r == 28 ? gf[b][2 * i + 1] : wf[b][2][2 * i + 1];
        pin(wf[b][2]);
      }
      if constexpr (i >= 2 && cumB(g - 1) + i - 2 < cumB(g)) issueB<cumB(g - 1) + i - 2>(c);
      if constexpr (g >= GB_CHK) if constexpr ((g - GB_CHK) * 4 + i < 22) load_kv(IC<(g - GB_CHK) * 4 + i>{});
      SCHED();
    });
  });
  static_assert(cumB(GB_CHK - 1) == 42, "no DMA after the first K/V load");
  static_for<(28 - GB_CHK) * 4, 22>(load_kv);
  STAMP(3);
  const float z = __uint_as_float(__builtin_amdgcn_permlane32_swap(__float_as_uint(qa[2][12]), __float_as_uint(qa[2][12]), false, false)[1]);
  const float gate = __builtin_amdgcn_rcpf(1.f + __builtin_amdgcn_exp2f(z * -1.4426950408889634f));
  const u32 omg = __float_as_uint(__builtin_amdgcn_rcpf(1.f + __builtin_amdgcn_exp2f(z * 1.4426950408889634f)));
  int ab[4];
  u32 msk[5][2];
#pragma unroll
  for (int b = 0; b < 4; ++b) ab[b] = (r >> 3) - ((r & 7) < 4 * hi + b ? 1 : 0);
#pragma unroll
  for (int a = 0; a < 5; ++a)
#pragma unroll
    for (int e = 0; e < 2; ++e) msk[a][e] = (ab[2 * e] == a - 1 ? 0xFFFFu : 0u) | (ab[2 * e + 1] == a - 1 ? 0xFFFF0000u : 0u);
  const float csc = 0.18898223650461362f * 1.4426950408889634f;
  u32x4 op[4][2];
  static_for<0, 4>([&](auto I) {
    constexpr int i = I;
    u32x4 qf[2] = {pk8(qa[i], 0), pk8(qa[i], 1)};
    if (i & 1) { qf[0][0] = hi ? qf[0][0] : 0u; qf[0][1] = hi ? qf[0][1] : 0u; }
    else       { qf[1][2] = hi ? 0u : qf[1][2]; qf[1][3] = hi ? 0u : qf[1][3]; }
    wait_vm<15 - 5 * i>();
    if constexpr (i == 0) { pin(kp[0]); pin(kp[1]); STAMP(4); }
    pin(kf[i][0]); pin(kf[i][1]); pin(vp[i]); pin(vf[i][0]); pin(vf[i][1]);
    f32x16 so = {}, sv = {};
    so = mfma(kf[i][0], qf[0], so); sv = mfma(kp[0], qf[0], sv);
    so = mfma(kf[i][1], qf[1], so); sv = mfma(kp[1], qf[1], sv);
    float v[4];
#pragma unroll
    for (int b = 0; b < 4; ++b) {
      float t = first ? -1e30f : sv[4 * i + b];
#pragma unroll
      for (int a = 0; a < 4; ++a) t = ab[b] == a ? so[4 * a + b] : t;
      v[b] = t;
    }
    const float mx = xhalf_max(fmaxf(fmaxf(v[0], v[1]), fmaxf(v[2], v[3])));
#pragma unroll
    for (int b = 0; b < 4; ++b) v[b] = __builtin_amdgcn_exp2f((v[b] - mx) * csc);
    const float sc = gate * __builtin_amdgcn_rcpf(xhalf_sum((v[0] + v[1]) + (v[2] + v[3])));
    const u32 p01 = pk(v[0] * sc, v[1] * sc), p23 = pk(v[2] * sc, v[3] * sc);
    f32x16 oa = {};
    oa = mfma(vp[i], u32x4{0u, 0u, p01 & msk[0][0], p23 & msk[0][1]}, oa);
    oa = mfma(vf[i][0], u32x4{p01 & msk[1][0], p23 & msk[1][1], p01 & msk[2][0], p23 & msk[2][1]}, oa);
    oa = mfma(vf[i][1], u32x4{p01 & msk[3][0], p23 & msk[3][1], p01 & msk[4][0], p23 & msk[4][1]}, oa);
    op[i][0] = pk8(oa, 0); op[i][1] = pk8(oa, 1);
  });
  asm volatile("" ::: "memory");
  __builtin_amdgcn_raw_buffer_store_b32(omg, c.rws, omg_off, WS_OMG + T * 128, SC1);
#pragma unroll
  for (int i = 1; i < 4; i += 2) {
#pragma unroll
    for (int e = 0; e < 2; ++e) { auto t = __builtin_amdgcn_permlane32_swap(op[i - 1][1][e], op[i][0][e], false, false); op[i - 1][1][e] = t[0]; op[i][0][e] = t[1]; }
    const u32 o2 = ob + (i >> 1) * 3584;
    st128(op[i - 1][0], c.rws, lane16, o2);
    st128(op[i - 1][1], c.rws, lo16, o2 + 1024);
    st128(op[i][0], c.rws, lane16, o2 + 1536);
    st128(op[i][1], c.rws, lane16, o2 + 2560);
  }
  STAMP(5);
  sync_vm<0>();
  if (w == 0) __builtin_amdgcn_raw_buffer_store_b64(u32x2{(u32)tag, (u32)(tag >> 32)}, c.rws, lane == 0 ? (14 + hf) * 8 : OOB, WS_FLAG + 8192 + mt * 256, SC1);
  STAMP(6);
  if (mt == 0 && hf == 0 && w == 0) {
#pragma unroll 1
    for (int i = 0; i < 4; ++i) spin(c.ws, WS_FLAG + 8192 + (i * 4 + (lane >> 4)) * 256 + (lane & 15) * 8, true, tag);
    __builtin_amdgcn_raw_buffer_store_b64(u32x2{(u32)tag, (u32)(tag >> 32)}, c.rws, lane == 0 ? 0 : OOB, WS_E, SC1);
  }
}

__global__ __launch_bounds__(512, 2) void fused_kernel(const void* __restrict__ x, const void* __restrict__ wqkv, const void* __restrict__ wo,
                                                       const void* __restrict__ wg, const void* __restrict__ wu, const void* __restrict__ wd,
                                                       const void* __restrict__ gw, void* __restrict__ out, void* __restrict__ ws) {
  __shared__ __attribute__((aligned(1024))) char smem[163840];
  const int lane = threadIdx.x & 63, w = __builtin_amdgcn_readfirstlane(threadIdx.x >> 6);
  const int b = blockIdx.x, xc = b & 7, ix = b >> 3;
  const int mq = xc >> 1, mt = mq * 4 + (ix & 3), rl = ix >> 2, rh = xc & 1;
  Ctx c;
  c.sm = (LAS char*)smem;
  c.m0w = (u32)(size_t)c.sm + w * 1024;
  c.rx = mk(x, 2048 * 896); c.rqkv = mk(wqkv, 448 * 2688); c.rwg = mk(wg, 448 * 3584); c.rwu = mk(wu, 448 * 3584);
  c.rwd = mk(wd, 1792 * 896); c.rwo = mk(wo, 448 * 896); c.rws = mk(ws, WS_END);
  c.ws = (const char*)ws;
  {
    const int i = ix * 49 + lane, row = mq * 112 + i / 14, l = i % 14;
    const u32 o12 = row * 3584 + (rh * 7 + (l < 7 ? l : l + 7)) * 128;
    const u32 o34 = ((w == 3 ? b * 49 : b * 13) + lane) * 128;
    const int n = w == 0 ? 1 : (w < 4 ? 49 : (w == 4 ? 13 : 0));
    c.toff = lane < n ? (w == 0 ? WS_E : (w < 3 ? o12 : o34)) : OOB;
    c.rt = w == 0 ? c.rws : (w == 1 ? c.rwg : (w == 2 ? c.rwu : (w == 3 ? c.rwd : c.rwo)));
  }
  const int r8 = lane >> 3, c8 = lane & 7, R = 8 * w + r8;
  c.vx = (mt * 128 + 2 * R) * 896 + ((c8 ^ ((R >> 1) & 7)) << 4);
  const int sw = (c8 ^ ((r8 & 2) << 1)) << 4;
  if (rl < 7) {
    const int role = rh * 7 + rl;
    c.vkv = R * 2688 + 896 + role * 128 + sw;
    c.vg = R * 3584 + role * 128 + sw;
    c.vu = R * 3584 + role * 128 + (sw ^ 32);
    c.vw2 = R * 896 + rl * 128 + sw;
    type_a(c, w, lane, mt, role, xc * 4 + (ix & 3), rl, out DIAGA);
  } else {
    c.vkv = R * 2688 + rh * 384 + sw;
    type_b(c, w, lane, mt, rh, gw DIAGA);
  }
}

extern "C" void kernel_launch(void* const* d_in, const int* in_sizes, int n_in, void* d_out, int out_size, void* d_ws, size_t ws_size,
                              hipStream_t stream) {
  fprintf(stderr, "PTRS:"); for (int i = 0; i < n_in; ++i) fprintf(stderr, " %p", d_in[i]); fprintf(stderr, " out %p ws %p\n", d_out, d_ws);
  { hipPointerAttribute_t at; if (hipPointerGetAttributes(&at, d_ws) == hipSuccess) fprintf(stderr, "ws attr type %d flags %u managed %d\n", (int)at.type, at.allocationFlags, at.isManaged);
    if (hipPointerGetAttributes(&at, d_in[0]) == hipSuccess) fprintf(stderr, "in0 attr type %d flags %u\n", (int)at.type, at.allocationFlags); }
  fused_kernel<<<256, 512, 0, stream>>>(d_in[0], d_in[1], d_in[2], d_in[4], d_in[5], d_in[6], d_in[7], d_out, d_ws);
}
```

```cpp
#define MEAS 0
#define MEASB 0
#include <hip/hip_runtime.h>
#include <hip/hip_bf16.h>

typedef unsigned long long u64;
typedef unsigned u32;
typedef __bf16 bf16x8 __attribute__((ext_vector_type(8)));
typedef __bf16 bf16x2 __attribute__((ext_vector_type(2)));
typedef short s16x4 __attribute__((ext_vector_type(4)));
typedef u32 u32x4 __attribute__((ext_vector_type(4)));
typedef u32 u32x2 __attribute__((ext_vector_type(2)));
typedef float f32x16 __attribute__((ext_vector_type(16)));
typedef float f32x4 __attribute__((ext_vector_type(4)));
typedef __amdgpu_buffer_rsrc_t rsrc_t;
#define LAS __attribute__((address_space(3)))
#define GAS __attribute__((address_space(1)))
#define DI __device__ __forceinline__
#define SCHED() __builtin_amdgcn_sched_barrier(0)
template <int V> using IC = std::integral_constant<int, V>;

constexpr u32 WS_E = 0;
constexpr u32 WS_FLAG = 4096;
constexpr u32 WS_OMG = 32768;
constexpr u32 WS_K = 65536;
constexpr u32 KV_T = 28672;
constexpr u32 WS_V = WS_K + 64 * KV_T;
constexpr u32 WS_A2 = WS_V + 64 * KV_T;
constexpr u32 A2_T = 143360;
constexpr u32 WS_END = WS_A2 + 64 * A2_T;
constexpr u32 OOB = 0xFFFFFF00u;
constexpr int SC1 = 16;
constexpr int SLOT = 8192;
constexpr int SPIN_CAP = 1 << 15;

DI rsrc_t mk(const void* p, u32 bytes) { return __builtin_amdgcn_make_buffer_rsrc((void*)p, (short)0, (int)bytes, 0x00020000); }
DI void dma(u32 m0v, u32 voff, rsrc_t rs, u32 soff) {
  asm volatile("s_mov_b32 m0, %0\n\ts_nop 0\n\tbuffer_load_dwordx4 %1, %2, %3 offen lds" ::"s"(m0v), "v"(voff), "s"(rs), "s"(soff) : "memory");
}
DI u32x4 ld128_sc1(u32 voff, rsrc_t rs, u32 soff) {
  u32x4 v;
  asm volatile("buffer_load_dwordx4 %0, %1, %2, %3 offen sc1" : "=v"(v) : "v"(voff), "s"(rs), "s"(soff) : "memory");
  return v;
}
DI u32x2 ld64_sc1(u32 voff, rsrc_t rs, u32 soff) {
  u32x2 v;
  asm volatile("buffer_load_dwordx2 %0, %1, %2, %3 offen sc1" : "=v"(v) : "v"(voff), "s"(rs), "s"(soff) : "memory");
  return v;
}
DI u32 ld32_sc1(u32 voff, rsrc_t rs, u32 soff) {
  u32 v;
  asm volatile("buffer_load_dword %0, %1, %2, %3 offen sc1" : "=v"(v) : "v"(voff), "s"(rs), "s"(soff) : "memory");
  return v;
}
DI u32 touch(u32 voff, rsrc_t rs) {
  u32 v;
  asm volatile("buffer_load_dword %0, %1, %2, 0 offen" : "=v"(v) : "v"(voff), "s"(rs) : "memory");
  return v;
}
DI void st128(u32x4 v, rsrc_t rs, u32 voff, u32 soff) {
  asm volatile("buffer_store_dwordx4 %0, %1, %2, %3 offen sc1\n\ts_nop 1" ::"v"(v), "v"(voff), "s"(rs), "s"(soff) : "memory");
}
constexpr int clampvm(int n) { return n > 63 ? 63 : n; }
template <int N> DI void wait_vm() { static_assert(N >= 0, ""); asm volatile("s_waitcnt vmcnt(%0)" ::"n"(clampvm(N)) : "memory"); }
template <class T> DI void pin(T& v) { asm volatile("" : "+v"(v)); }

DI u32x4 trfrag(LAS char* p) {
  s16x4 lo = __builtin_amdgcn_ds_read_tr16_b64_v4i16((LAS s16x4*)p);
  s16x4 hi = __builtin_amdgcn_ds_read_tr16_b64_v4i16((LAS s16x4*)(p + 512));
  u32x2 a = __builtin_bit_cast(u32x2, lo), b = __builtin_bit_cast(u32x2, hi);
  return u32x4{a.x, a.y, b.x, b.y};
}
DI f32x16 mfma(u32x4 a, u32x4 b, f32x16 c) {
  return __builtin_amdgcn_mfma_f32_32x32x16_bf16(__builtin_bit_cast(bf16x8, a), __builtin_bit_cast(bf16x8, b), c, 0, 0, 0);
}
DI void mfma_at(f32x16& c, u32x4 a, u32x4 b) { c = mfma(a, b, c); asm volatile("" : "+v"(c)); }
DI u32 pk(float a, float b) { bf16x2 c; c[0] = (__bf16)a; c[1] = (__bf16)b; return __builtin_bit_cast(u32, c); }
DI u32x4 pk8(const f32x16& v, int s) { return u32x4{pk(v[8 * s], v[8 * s + 1]), pk(v[8 * s + 2], v[8 * s + 3]), pk(v[8 * s + 4], v[8 * s + 5]), pk(v[8 * s + 6], v[8 * s + 7])}; }
DI float glu(float g, float u) { return u * __builtin_amdgcn_rcpf(1.f + __builtin_amdgcn_exp2f(g * -1.4426950408889634f)); }
DI float xhalf_max(float v) { auto r = __builtin_amdgcn_permlane32_swap(__float_as_uint(v), __float_as_uint(v), false, false); return fmaxf(__uint_as_float(r[0]), __uint_as_float(r[1])); }
DI float xhalf_sum(float v) { auto r = __builtin_amdgcn_permlane32_swap(__float_as_uint(v), __float_as_uint(v), false, false); return __uint_as_float(r[0]) + __uint_as_float(r[1]); }

DI void spin(const char* ws, u32 off, bool need, u64 tag) {
  const GAS u64* p = (const GAS u64*)(ws + off);
#pragma unroll 1
  for (int i = 0; i < SPIN_CAP; ++i) {
    u64 v = __hip_atomic_load(p, __ATOMIC_RELAXED, __HIP_MEMORY_SCOPE_AGENT);
    if (__all(!need || v == tag)) break;
    __builtin_amdgcn_s_sleep(2);
  }
  asm volatile("" ::: "memory");
}
DI bool match(u32x2 v, bool need, u64 tag) { return __all(!need || (v.x == (u32)tag && v.y == (u32)(tag >> 32))); }
DI u64 tag_of(u32x2 ev) {
  u64 e = ((u64)__builtin_amdgcn_readfirstlane(ev.y) << 32) | (u32)__builtin_amdgcn_readfirstlane(ev.x);
  return e * 6364136223846793005ull + 1442695040888963407ull;
}

#define STAMP(i) do { if ((i) == MEAS) { SCHED(); asm volatile("s_memrealtime %0\n\ts_waitcnt lgkmcnt(0)" : "=s"(mrt) :: "memory"); SCHED(); } } while (0)
#define DIAGP , u64& mrt
#define DIAGA , mrt
#define STAMP2(i)
template <int N> DI void sync_vm() { static_assert(N >= 0, ""); asm volatile("s_waitcnt vmcnt(%0)\n\ts_barrier" ::"n"(clampvm(N)) : "memory"); }

template <int I, int N, class Fn> DI void static_for(Fn&& f) {
  if constexpr (I < N) { f(IC<I>{}); static_for<I + 1, N>(f); }
}

#define PRO_DMA 16
#define P0_RATE 2
#define U_KVPUB 14
#define U_H1PUB 46
#define U_H2PUB 64
#define U_POLL1 54
#define U_POLL2 66
#define U_CHK2 69
#define U_POLL3 71
#define U_CHK3 74
#define LD_FIRST 6
#define LD_RATE 2
constexpr int NG = 98, U0 = 7;
constexpr int U_KVST = 0, U_H1STA = 30, U_H1STB = 32, U_H2ST = 56, U_CHK1 = 56;
constexpr int PRO_OPS = PRO_DMA + 1;
constexpr bool step_start(int u) { return u < 56 ? (u % 8 == 0) : (u < 91 && (u - 56) % 5 == 0); }
constexpr bool is_sync(int g) { return g < U0 ? true : step_start(g - U0 + 2); }
constexpr int last_seq_of_step(int u0) { return u0 < 56 ? 21 + (u0 / 4 + 2) * 2 - 1 : 49 + (u0 - 56) + 4; }
constexpr int need_seq(int g) { return g < 6 ? 3 * (g + 1) + 2 : (g == 6 ? last_seq_of_step(0) : last_seq_of_step(g - U0 + 2)); }
constexpr int freed_at(int g) { return g < U0 ? 3 * g : (g - U0 <= 56 ? 21 + 2 * ((g - U0) / 4) : 49 + (g - U0 - 56)); }
constexpr int freed(int g) { for (int j = g; j >= 0; --j) if (is_sync(j) || j == U0 + U_CHK1) return freed_at(j); return 0; }
constexpr int cum(int g) {
  int cc = PRO_DMA;
  for (int j = 0; j <= g; ++j) { int t = cc + (j < U0 ? P0_RATE : 1), cap = freed(j) + 20; t = t < cap ? t : cap; cc = t < 84 ? t : 84; }
  return cc;
}
constexpr int n_dma(int g) { return cum(g) - cum(g - 1); }
constexpr bool sched_ok() { for (int g = 0; g < NG; ++g) if (is_sync(g) && need_seq(g) >= cum(g - 1)) return false; return true; }
static_assert(sched_ok(), "a slot is needed before it is requested");
constexpr int n_st(int g) { int u = g - U0; return (u == U_POLL1) + (u == U_POLL2) + (u == U_POLL3) + 2 * (u == U_KVST) + (u == U_H1STA) + (u == U_H1STB) + 2 * (u == U_H2ST); }
constexpr int ld_rel(int k) { return k < LD_FIRST ? 0 : 1 + (k - LD_FIRST) / LD_RATE; }
constexpr int ld_iter(int it) { return U0 + (it < 14 ? U_CHK1 + ld_rel(it) : (it < 22 ? U_CHK2 + ld_rel(it - 14) : U_CHK3 + ld_rel(it - 22))); }
constexpr int ld_lo(int g) { for (int it = 0; it < 36; ++it) if (ld_iter(it) >= g) return it; return 36; }
constexpr int n_ld(int g) { return ld_lo(g + 1) - ld_lo(g); }
constexpr int ops_in(int g) { return n_st(g) + n_ld(g) + n_dma(g); }
constexpr int base(int g) { int t = PRO_OPS; for (int j = 0; j < g; ++j) t += ops_in(j); return t; }
constexpr int pos_st_last(int g) { return base(g) + n_st(g) - 1; }
constexpr int pos_ld(int it) { return base(ld_iter(it)) + n_st(ld_iter(it)) + (it - ld_lo(ld_iter(it))); }
constexpr int pos_dma(int n) {
  if (n < PRO_DMA) return n;
  for (int g = 0; g < NG; ++g)
    if (n < cum(g)) return base(g) + n_st(g) + n_ld(g) + (n - cum(g - 1));
  return -1000;
}
constexpr int imin(int a, int b) { return a < b ? a : b; }
constexpr int sync_cnt(int g) {
  int n = base(g) - pos_dma(need_seq(g)) - 1;
  if (g == U0 + U_KVPUB) n = imin(n, base(g) - pos_st_last(U0 + U_KVST) - 1);
  if (g == U0 + U_H1PUB) n = imin(n, base(g) - pos_st_last(U0 + U_H1STB) - 1);
  if (g == U0 + U_H2PUB) n = imin(n, base(g) - pos_st_last(U0 + U_H2ST) - 1);
  return n;
}
static_assert(is_sync(U0 + U_KVPUB) && is_sync(U0 + U_H1PUB) && is_sync(U0 + U_H2PUB) && is_sync(U0 + U_CHK2) && is_sync(U0 + U_CHK3), "");
static_assert(U_POLL1 < U_CHK1 && U_CHK1 < U_POLL2 && U_POLL2 < U_CHK2 && U_CHK2 < U_POLL3 && U_POLL3 < U_CHK3 && U_H2PUB <= U_CHK2, "");
constexpr int item_of(int i) { return i < 14 ? i : i + 1; }
constexpr int blk_of(int i) { return i < 14 ? i : (i < 21 ? i + 14 : i - 7); }
constexpr bool ld_ok() { for (int i = 0; i < 35; ++i) if (ld_iter(item_of(i)) > U0 + 56 + i) return false; for (int i = 1; i < 36; ++i) if (ld_iter(i) < ld_iter(i - 1)) return false; return true; }
static_assert(ld_ok(), "an operand is used before it is requested");

struct Ctx {
  LAS char* sm; u32 m0w;
  rsrc_t rx, rqkv, rwg, rwu, rwd, rwo, rws;
  const char* ws;
  rsrc_t rt; u32 toff;
  u32 vx, vkv, vg, vu, vw2;
};

template <int N> DI void issueA(const Ctx& c) {
  constexpr u32 dst = (N % 20) * SLOT;
  if constexpr (N < 21) {
    constexpr int kc = N / 3, j = N % 3;
    if constexpr (j < 2) dma(c.m0w + dst, c.vx, c.rx, j * 896 + kc * 128);
    else dma(c.m0w + dst, c.vkv, c.rqkv, kc * 64 * 2688);
  } else if constexpr (N < 49) {
    constexpr int kc = ((N - 21) / 2) % 7, ps = (N - 21) / 14;
    if constexpr ((N - 21) % 2 == 0) dma(c.m0w + dst, c.vg, c.rwg, kc * 64 * 3584 + ps * 1792);
    else dma(c.m0w + dst, c.vu, c.rwu, kc * 64 * 3584 + ps * 1792);
  } else if constexpr (blk_of(N - 49) < 28) dma(c.m0w + dst, c.vw2, c.rwd, blk_of(N - 49) * 64 * 896);
  else dma(c.m0w + dst, c.vw2, c.rwo, (blk_of(N - 49) - 28) * 64 * 896);
}

DI void type_a(Ctx& c, int w, int lane, int mt, int role, int R2, int n2, void* out DIAGP) {
  const int r = lane & 31, hi = lane >> 5, blk = (lane >> 4) & 1, q = (lane & 15) >> 2, p = lane & 3;
  const int sp = ((p & 1) << 1) | (p >> 1);
  const int rg = w & 3, cg = w >> 2, T = mt * 4 + rg;
  const bool isv = role >= 7;
  LAS char* sm = c.sm;
  static_for<0, PRO_DMA>([&](auto N) { issueA<N>(c); });
  u32x2 ev = ld64_sc1(c.toff, c.rt, 0);
  const int R = 32 * (rg & 1) + r, xm = (R >> 1) & 7;
  int xa[4];
#pragma unroll
  for (int s = 0; s < 4; ++s) xa[s] = (rg >> 1) * SLOT + R * 128 + (((2 * s + hi) ^ xm) << 4);
  const int rowb = (8 * hi + q) * 128, q2 = q & 2;
  const int akv = rowb + (((2 * cg + blk) ^ q2) << 5) + (isv ? p : sp) * 8;
  int agu[2];
#pragma unroll
  for (int i = 0; i < 2; ++i) agu[i] = blk * SLOT + rowb + (((2 * cg + i) ^ q2 ^ blk) << 5) + sp * 8;
  const int rt = w & 1, kq = w >> 1;
  int aw2[2];
#pragma unroll
  for (int i = 0; i < 2; ++i) aw2[i] = kq * 2048 + rowb + (((2 * i + blk) ^ q2) << 5) + p * 8;
  const int T2 = (R2 >> 1) * 4 + 2 * rt + (R2 & 1);

  u32x4 xf[28], kf[4], wf[2][2], af[35], hp[2];
  f32x16 akvacc = {}, g1[2] = {}, g2[2] = {}, ah[2] = {}, ao[2] = {};
  u32x2 pollv; u32 omg; u64 tag;
  const u32 lane16 = lane * 16;
  const u32 poll_off = WS_FLAG + (R2 >> 1) * 256 + (lane & 15) * 8;
  const bool need_h = (lane & 15) < 14;
  const u32 a2_off = WS_A2 + T2 * A2_T + kq * 1024 + lane16;
  const u32 h_off = WS_A2 + T * A2_T + (role * 4 + 2 * cg) * 1024 + lane16;

  auto read_kv = [&](auto J, auto S) {
    constexpr int j = J, sx = S;
    xf[4 * j + sx] = *(LAS u32x4*)(sm + xa[sx] + ((3 * j) % 20) * SLOT);
    kf[sx] = trfrag(sm + akv + ((3 * j + 2) % 20) * SLOT + sx * 2048);
  };
  auto read_w = [&](auto U, auto I) {
    constexpr int u = U, i = I;
    if constexpr (u < 56) {
      constexpr int sl = (21 + 2 * (u / 4)) % 20;
      const int wrap = sl == 19 ? blk * (-20 * SLOT) : 0;
      wf[u & 1][i] = trfrag(sm + agu[i] + wrap + sl * SLOT + (u % 4) * 2048);
    } else if constexpr (u < 91) {
      wf[u & 1][i] = trfrag(sm + aw2[i] + ((49 + u - 56) % 20) * SLOT);
    }
  };
  auto publish = [&](int cls) {
    if (w == 0) __builtin_amdgcn_raw_buffer_store_b64(u32x2{(u32)tag, (u32)(tag >> 32)}, c.rws, lane == 0 ? role * 8 : OOB, WS_FLAG + cls * 4096 + mt * 256, SC1);
  };
  auto load_item = [&](auto IT) {
    constexpr int it = IT;
    if constexpr (it == 14) omg = ld32_sc1(r * 4, c.rws, WS_OMG + T2 * 128);
    else { constexpr int i = it < 14 ? it : it - 1; af[i] = ld128_sc1(a2_off, c.rws, blk_of(i) * 4096); }
  };

  STAMP(1);
  sync_vm<PRO_OPS - 3>();
  STAMP(2);
  static_for<0, 4>([&](auto S) { read_kv(IC<0>{}, S); });
  SCHED();
  auto pass0 = [&](auto ISV) {
    static_for<0, U0>([&](auto G) {
      constexpr int g = G;
      sync_vm<sync_cnt(g)>();
      SCHED();
      static_for<0, 4>([&](auto S) {
        constexpr int sx = S;
        if constexpr (ISV) mfma_at(akvacc, xf[4 * g + sx], kf[sx]); else mfma_at(akvacc, kf[sx], xf[4 * g + sx]);
        SCHED();
        if constexpr (g < 6) read_kv(IC<g + 1>{}, S); else read_w(IC<sx / 2>{}, IC<sx % 2>{});
        if constexpr (cum(g - 1) + sx < cum(g)) issueA<cum(g - 1) + sx>(c);
        SCHED();
      });
    });
  };
  static_assert(P0_RATE <= 4, "");
  if (isv) pass0(IC<1>{}); else pass0(IC<0>{});
  auto check = [&](auto CNT, int cls, bool need) {
    wait_vm<CNT>();
    pin(pollv);
    if (w == 0 && !match(pollv, need, tag)) spin(c.ws, poll_off + cls * 4096, need, tag);
  };
  static_for<U0, NG>([&](auto G) {
    constexpr int g = G, u = g - U0, b = u & 1;
    if constexpr (u == 0) { STAMP(3); static_assert(pos_dma(need_seq(6)) > PRO_DMA, ""); pin(ev); tag = tag_of(ev); }
    if constexpr (u == 28) STAMP(4);
    if constexpr (u == 56) STAMP(5);
    if constexpr (u == 70) STAMP(8);
    if constexpr (u == 77) STAMP(9);
    if constexpr (is_sync(g)) STAMP2(16 + 2 * (u < 56 ? u / 8 : 7 + (u - 59) / 5));
    if constexpr (u == U_CHK2) check(IC<base(g) - base(U0 + U_POLL2) - 1>{}, 2, !need_h);
    if constexpr (u == U_CHK3) check(IC<base(g) - base(U0 + U_POLL3) - 1>{}, 2, need_h);
    if constexpr (is_sync(g)) {
      sync_vm<sync_cnt(g)>();
      STAMP2(17 + 2 * (u < 56 ? u / 8 : 7 + (u - 59) / 5));
    }
    if constexpr (u == U_KVPUB) publish(0);
    if constexpr (u == U_H1PUB) publish(1);
    if constexpr (u == U_H2PUB) publish(2);
    if constexpr (u == U_POLL1) pollv = ld64_sc1(w == 0 ? poll_off : OOB, c.rws, 4096);
    if constexpr (u == U_POLL2 || u == U_POLL3) pollv = ld64_sc1(w == 0 ? poll_off : OOB, c.rws, 8192);
    if constexpr (u == U_KVST) {
#pragma unroll
      for (int s = 0; s < 2; ++s) {
        const u32 off = isv ? WS_V + T * KV_T + (s * 2 + hi) * 7168 + ((role - 7) * 64 + cg * 32 + r) * 16
                            : WS_K + T * KV_T + (role * 8 + cg * 4 + 2 * s) * 512 + lane16;
        st128(pk8(akvacc, s), c.rws, off, 0);
      }
    }
    if constexpr (u == U_H1STA) st128(hp[0], c.rws, h_off, 0);
    if constexpr (u == U_H1STB) st128(hp[1], c.rws, h_off, 1024);
    if constexpr (u == U_H2ST) {
#pragma unroll
      for (int t = 0; t < 2; ++t) {
#pragma unroll
        for (int j = 0; j < 8; j += 2) hp[t][j / 2] = pk(glu(g2[t][j], g2[t][8 + j]), glu(g2[t][j + 1], g2[t][9 + j]));
        st128(hp[t], c.rws, h_off, (56 + t) * 1024);
        SCHED();
      }
    }
    asm volatile("" ::: "memory");
    if constexpr (u == U_CHK1) {
      STAMP2(44);
      check(IC<base(g) + n_st(g) - base(U0 + U_POLL1) - 1>{}, 1, need_h);
      asm volatile("s_barrier" ::: "memory");
      STAMP2(45);
    }
    static_for<ld_lo(g), ld_lo(g + 1)>(load_item);
    if constexpr (u >= 56) {
      wait_vm<base(g) + n_st(g) + n_ld(g) - pos_ld(item_of(u - 56)) - 1>();
      pin(af[u - 56]);
      if constexpr (u == 56) STAMP2(46);
      if constexpr (u == 70) STAMP2(47);
      if constexpr (u == 77) STAMP2(48);
    }
    SCHED();
    static_for<0, 2>([&](auto I) {
      constexpr int i = I;
      if constexpr (u < 28) mfma_at(g1[i], wf[b][i], xf[u]);
      else if constexpr (u < 56) mfma_at(g2[i], wf[b][i], xf[u - 28]);
      else if constexpr (blk_of(u - 56) < 28) mfma_at(ah[i], wf[b][i], af[u - 56]);
      else mfma_at(ao[i], wf[b][i], af[u - 56]);
      SCHED();
      read_w(IC<u + 2>{}, I);
      if constexpr (u >= 28 && u < 32) {
        constexpr int t = (u - 28) / 2, j = ((u - 28) % 2) * 4 + 2 * i;
        u32 hv = pk(glu(g1[t][j], g1[t][8 + j]), glu(g1[t][j + 1], g1[t][9 + j]));
        pin(hv);
        hp[t][j / 2] = hv;
      }
      if constexpr (i == 1) static_for<cum(g - 1), cum(g)>([&](auto N) { issueA<N>(c); });
      SCHED();
    });
  });
  pin(omg);
  STAMP(6);
  const float om = __uint_as_float(omg);
  LAS char* red = sm + 4 * SLOT;
#pragma unroll
  for (int t = 0; t < 2; ++t)
#pragma unroll
    for (int a = 0; a < 4; ++a) {
      f32x4 v;
#pragma unroll
      for (int e = 0; e < 4; ++e) v[e] = ah[t][4 * a + e] * om + ao[t][4 * a + e];
      *(LAS f32x4*)(red + kq * 16384 + (2 * r + rt) * 256 + (((8 * t + 2 * a + hi) ^ (r & 15)) << 4)) = v;
    }
  asm volatile("s_waitcnt lgkmcnt(0)\n\ts_barrier" ::: "memory");
  STAMP(7);
  {
    const int row = w * 8 + (lane >> 3), c8 = lane & 7, m = (row >> 1) & 15;
    f32x4 s0 = {}, s1 = {};
#pragma unroll
    for (int k = 0; k < 4; ++k) {
      s0 += *(LAS f32x4*)(red + k * 16384 + row * 256 + (((2 * c8) ^ m) << 4));
      s1 += *(LAS f32x4*)(red + k * 16384 + row * 256 + (((2 * c8 + 1) ^ m) << 4));
    }
    u32x4 o = {pk(s0[0], s0[1]), pk(s0[2], s0[3]), pk(s1[0], s1[1]), pk(s1[2], s1[3])};
    *(u32x4*)((char*)out + (size_t)(R2 * 64 + row) * 896 + n2 * 128 + c8 * 16) = o;
  }
}
static_assert(freed(U0 + 90) >= 72, "the reduction scratch (slots 4..11 = sequence 64..71) must be free at the end");

constexpr int PRO_B = 12;
constexpr int freedB(int g) { return 6 * ((g + 1) / 4); }
constexpr int cumB(int g) {
  int cc = PRO_B;
  for (int j = 0; j <= g; ++j) { int t = cc + 2, cap = freedB(j) + 18; t = t < cap ? t : cap; cc = t < 42 ? t : 42; }
  return cc;
}
#define GB_POLL 20
#define GB_CHK 23
constexpr int n_stB(int g) { return g == GB_POLL; }
constexpr int baseB(int g) { int t = PRO_B + 1; for (int j = 0; j < g; ++j) t += n_stB(j) + cumB(j) - cumB(j - 1); return t; }
constexpr int pos_dmaB(int n) {
  if (n < PRO_B) return n;
  for (int g = 0; g < 28; ++g) if (n < cumB(g)) return baseB(g) + n_stB(g) + (n - cumB(g - 1));
  return -1000;
}
constexpr int sync_cntB(int g) {
  int n = baseB(g) - pos_dmaB(6 * (g / 4 + 2) - 1) - 1;
  return n;
}
template <int N> DI void issueB(const Ctx& c) {
  constexpr int kc = N / 6, j = N % 6;
  constexpr u32 dst = (N % 18) * SLOT;
  if constexpr (j < 2) dma(c.m0w + dst, c.vx, c.rx, j * 896 + kc * 128);
  else dma(c.m0w + dst, c.vkv, c.rqkv, kc * 64 * 2688 + (j - 2) * 128);
}

DI void type_b(Ctx& c, int w, int lane, int mt, int hf, const void* gw DIAGP) {
  const int r = lane & 31, hi = lane >> 5, blk = (lane >> 4) & 1, q = (lane & 15) >> 2, p = lane & 3;
  const int sp = ((p & 1) << 1) | (p >> 1);
  const int rg = w & 3, cg = w >> 2, T = mt * 4 + rg;
  LAS char* sm = c.sm;
  if (w == 0) dma(c.m0w + 18 * SLOT, lane * 16, mk(gw, 896), 0);
  static_for<0, PRO_B>([&](auto N) { issueB<N>(c); });
  u32x2 ev = ld64_sc1(c.toff, c.rt, 0);
  const int R = 32 * (rg & 1) + r, xm = (R >> 1) & 7;
  int xa[4], aq[4];
#pragma unroll
  for (int s = 0; s < 4; ++s) xa[s] = (rg >> 1) * SLOT + R * 128 + (((2 * s + hi) ^ xm) << 4);
  const int h0 = 8 * hf + 4 * cg;
#pragma unroll
  for (int i = 0; i < 4; ++i) {
    const int col = 28 * (h0 + i) - 4 * (i & 1) - 192 * hf + 16 * blk + 4 * sp;
    aq[i] = (2 + (col >> 6)) * SLOT + (8 * hi + q) * 128 + (((col & 63) * 2) ^ ((q & 2) << 5));
  }
  const bool first = (mt == 0) && !(rg & 1);
  const int Tp = first ? T : ((rg & 1) ? T - 1 : T - 3);
  const u32 lane16 = lane * 16, lo16 = hi ? OOB : lane16;
  const u32 ob = WS_A2 + T * A2_T + (1792 + 28 * h0) * 64;
  const u32 omg_off = (hf == 0 && cg == 0 && hi == 0) ? r * 4 : OOB;
  const u32 poll_off = WS_FLAG + ((lane & 16) && mt > 0 ? mt - 1 : mt) * 256 + (lane & 15) * 8;
  const bool poll_need = (lane & 15) < 14;
  u32x4 xf[2], gf[2], wf[2][4];
  u32x4 kf[4][2], kp[2], vf[4][2], vp[4];
  f32x16 qa[4] = {};
  u32x2 pollv;
  const int sr = (r & 19) | ((r & 4) << 1) | ((r & 8) >> 1);
  const u32 kp_off = (28 * (r >> 3) - 4 * ((r >> 3) & 1)) * 64 + (hi * 32 + 24 + (r & 7)) * 16;
  const u32 v_off = hi * 7168 + sr * 16;
  auto load_kv = [&](auto IT) {
    constexpr int it = IT;
    if constexpr (it < 2) kp[it] = ld128_sc1(kp_off, c.rws, WS_K + Tp * KV_T + 28 * h0 * 64 + it * 1024);
    else {
      constexpr int i = (it - 2) / 5, k = (it - 2) % 5;
      const int wh = 28 * (h0 + i) - 4 * (i & 1);
      if constexpr (k < 2) kf[i][k] = ld128_sc1(lane16, c.rws, WS_K + T * KV_T + wh * 64 + k * 1024);
      else if constexpr (k == 2) vp[i] = ld128_sc1(v_off, c.rws, WS_V + Tp * KV_T + 14336 + wh * 16);
      else vf[i][k - 3] = ld128_sc1(v_off, c.rws, WS_V + T * KV_T + (k - 3) * 14336 + wh * 16);
    }
  };
  auto read_x = [&](auto G) {
    constexpr int g = G;
    if constexpr (g < 28) {
      xf[g & 1] = *(LAS u32x4*)(sm + xa[g % 4] + ((6 * (g / 4)) % 18) * SLOT);
      gf[g & 1] = *(LAS u32x4*)(sm + 18 * SLOT + g * 32 + hi * 16);
    }
  };
  auto read_w = [&](auto G, auto I) {
    constexpr int g = G;
    if constexpr (g < 28) wf[g & 1][I] = trfrag(sm + aq[I] + ((6 * (g / 4)) % 18) * SLOT + (g % 4) * 2048);
  };
  STAMP(1);
  sync_vm<PRO_B + 1 - 6>();
  STAMP(2);
  u64 tag;
  read_x(IC<0>{});
  static_for<0, 4>([&](auto I) { read_w(IC<0>{}, I); });
  SCHED();
  static_for<0, 28>([&](auto G) {
    constexpr int g = G, b = g & 1;
    if constexpr (g == GB_CHK) {
      STAMP(8);
      wait_vm<baseB(g) - baseB(GB_POLL) - 1>();
      pin(pollv);
      if (w == 0 && !match(pollv, poll_need, tag)) spin(c.ws, poll_off, poll_need, tag);
    }
    if constexpr (g % 4 == 3 && g < 27) { STAMP2(16 + 2 * (g / 4)); asm volatile("s_waitcnt lgkmcnt(0)" ::: "memory"); sync_vm<sync_cntB(g)>(); STAMP2(17 + 2 * (g / 4)); }
    if constexpr (g == 7) { pin(ev); tag = tag_of(ev); }
    if constexpr (g == GB_CHK) STAMP(9);
    if constexpr (g == GB_POLL) pollv = ld64_sc1(w == 0 ? poll_off : OOB, c.rws, 0);
    SCHED();
    static_for<0, 4>([&](auto I) {
      constexpr int i = I;
      mfma_at(qa[i], wf[b][i], xf[b]);
      SCHED();
      if constexpr (i == 0) read_x(IC<g + 1>{});
      read_w(IC<g + 1>{}, I);
      if constexpr (i < 2) {
        wf[b][2][2 * i] = r == 28 ? gf[b][2 * i] : wf[b][2][2 * i];
        wf[b][2][2 * i + 1] = r == 28 ? gf[b][2 * i + 1] : wf[b][2][2 * i + 1];
        pin(wf[b][2]);
      }
      if constexpr (i >= 2 && cumB(g - 1) + i - 2 < cumB(g)) issueB<cumB(g - 1) + i - 2>(c);
      if constexpr (g >= GB_CHK) if constexpr ((g - GB_CHK) * 4 + i < 22) load_kv(IC<(g - GB_CHK) * 4 + i>{});
      SCHED();
    });
  });
  static_assert(cumB(GB_CHK - 1) == 42, "no DMA after the first K/V load");
  static_for<(28 - GB_CHK) * 4, 22>(load_kv);
  STAMP(3);
  const float z = __uint_as_float(__builtin_amdgcn_permlane32_swap(__float_as_uint(qa[2][12]), __float_as_uint(qa[2][12]), false, false)[1]);
  const float gate = __builtin_amdgcn_rcpf(1.f + __builtin_amdgcn_exp2f(z * -1.4426950408889634f));
  const u32 omg = __float_as_uint(__builtin_amdgcn_rcpf(1.f + __builtin_amdgcn_exp2f(z * 1.4426950408889634f)));
  int ab[4];
  u32 msk[5][2];
#pragma unroll
  for (int b = 0; b < 4; ++b) ab[b] = (r >> 3) - ((r & 7) < 4 * hi + b ? 1 : 0);
#pragma unroll
  for (int a = 0; a < 5; ++a)
#pragma unroll
    for (int e = 0; e < 2; ++e) msk[a][e] = (ab[2 * e] == a - 1 ? 0xFFFFu : 0u) | (ab[2 * e + 1] == a - 1 ? 0xFFFF0000u : 0u);
  const float csc = 0.18898223650461362f * 1.4426950408889634f;
  u32x4 op[4][2];
  static_for<0, 4>([&](auto I) {
    constexpr int i = I;
    u32x4 qf[2] = {pk8(qa[i], 0), pk8(qa[i], 1)};
    if (i & 1) { qf[0][0] = hi ? qf[0][0] : 0u; qf[0][1] = hi ? qf[0][1] : 0u; }
    else       { qf[1][2] = hi ? 0u : qf[1][2]; qf[1][3] = hi ? 0u : qf[1][3]; }
    wait_vm<15 - 5 * i>();
    if constexpr (i == 0) { pin(kp[0]); pin(kp[1]); STAMP(4); }
    pin(kf[i][0]); pin(kf[i][1]); pin(vp[i]); pin(vf[i][0]); pin(vf[i][1]);
    f32x16 so = {}, sv = {};
    so = mfma(kf[i][0], qf[0], so); sv = mfma(kp[0], qf[0], sv);
    so = mfma(kf[i][1], qf[1], so); sv = mfma(kp[1], qf[1], sv);
    float v[4];
#pragma unroll
    for (int b = 0; b < 4; ++b) {
      float t = first ? -1e30f : sv[4 * i + b];
#pragma unroll
      for (int a = 0; a < 4; ++a) t = ab[b] == a ? so[4 * a + b] : t;
      v[b] = t;
    }
    const float mx = xhalf_max(fmaxf(fmaxf(v[0], v[1]), fmaxf(v[2], v[3])));
#pragma unroll
    for (int b = 0; b < 4; ++b) v[b] = __builtin_amdgcn_exp2f((v[b] - mx) * csc);
    const float sc = gate * __builtin_amdgcn_rcpf(xhalf_sum((v[0] + v[1]) + (v[2] + v[3])));
    const u32 p01 = pk(v[0] * sc, v[1] * sc), p23 = pk(v[2] * sc, v[3] * sc);
    f32x16 oa = {};
    oa = mfma(vp[i], u32x4{0u, 0u, p01 & msk[0][0], p23 & msk[0][1]}, oa);
    oa = mfma(vf[i][0], u32x4{p01 & msk[1][0], p23 & msk[1][1], p01 & msk[2][0], p23 & msk[2][1]}, oa);
    oa = mfma(vf[i][1], u32x4{p01 & msk[3][0], p23 & msk[3][1], p01 & msk[4][0], p23 & msk[4][1]}, oa);
    op[i][0] = pk8(oa, 0); op[i][1] = pk8(oa, 1);
  });
  asm volatile("" ::: "memory");
  __builtin_amdgcn_raw_buffer_store_b32(omg, c.rws, omg_off, WS_OMG + T * 128, SC1);
#pragma unroll
  for (int i = 1; i < 4; i += 2) {
#pragma unroll
    for (int e = 0; e < 2; ++e) { auto t = __builtin_amdgcn_permlane32_swap(op[i - 1][1][e], op[i][0][e], false, false); op[i - 1][1][e] = t[0]; op[i][0][e] = t[1]; }
    const u32 o2 = ob + (i >> 1) * 3584;
    st128(op[i - 1][0], c.rws, lane16, o2);
    st128(op[i - 1][1], c.rws, lo16, o2 + 1024);
    st128(op[i][0], c.rws, lane16, o2 + 1536);
    st128(op[i][1], c.rws, lane16, o2 + 2560);
  }
  STAMP(5);
  sync_vm<0>();
  if (w == 0) __builtin_amdgcn_raw_buffer_store_b64(u32x2{(u32)tag, (u32)(tag >> 32)}, c.rws, lane == 0 ? (14 + hf) * 8 : OOB, WS_FLAG + 8192 + mt * 256, SC1);
  STAMP(6);
  if (mt == 0 && hf == 0 && w == 0) {
#pragma unroll 1
    for (int i = 0; i < 4; ++i) spin(c.ws, WS_FLAG + 8192 + (i * 4 + (lane >> 4)) * 256 + (lane & 15) * 8, true, tag);
    __builtin_amdgcn_raw_buffer_store_b64(u32x2{(u32)tag, (u32)(tag >> 32)}, c.rws, lane == 0 ? 0 : OOB, WS_E, SC1);
  }
}

__global__ __launch_bounds__(512, 2) void fused_kernel(const void* __restrict__ x, const void* __restrict__ wqkv, const void* __restrict__ wo,
                                                       const void* __restrict__ wg, const void* __restrict__ wu, const void* __restrict__ wd,
                                                       const void* __restrict__ gw, void* __restrict__ out, void* __restrict__ ws) {
  __shared__ __attribute__((aligned(1024))) char smem[163840];
  u64 mrt0 = __builtin_amdgcn_s_memrealtime(), mrt = mrt0;
  const int lane = threadIdx.x & 63, w = __builtin_amdgcn_readfirstlane(threadIdx.x >> 6);
  const int b = blockIdx.x, xc = b & 7, ix = b >> 3;
  const int mq = xc >> 1, mt = mq * 4 + (ix & 3), rl = ix >> 2, rh = xc & 1;
  Ctx c;
  c.sm = (LAS char*)smem;
  c.m0w = (u32)(size_t)c.sm + w * 1024;
  c.rx = mk(x, 2048 * 896); c.rqkv = mk(wqkv, 448 * 2688); c.rwg = mk(wg, 448 * 3584); c.rwu = mk(wu, 448 * 3584);
  c.rwd = mk(wd, 1792 * 896); c.rwo = mk(wo, 448 * 896); c.rws = mk(ws, WS_END);
  c.ws = (const char*)ws;
  {
    const int i = ix * 49 + lane, row = mq * 112 + i / 14, l = i % 14;
    const u32 o12 = row * 3584 + (rh * 7 + (l < 7 ? l : l + 7)) * 128;
    const u32 o34 = ((w == 3 ? b * 49 : b * 13) + lane) * 128;
    const int n = w == 0 ? 1 : (w < 4 ? 49 : (w == 4 ? 13 : 0));
    c.toff = lane < n ? (w == 0 ? WS_E : (w < 3 ? o12 : o34)) : OOB;
    c.rt = w == 0 ? c.rws : (w == 1 ? c.rwg : (w == 2 ? c.rwu : (w == 3 ? c.rwd : c.rwo)));
  }
  const int r8 = lane >> 3, c8 = lane & 7, R = 8 * w + r8;
  c.vx = (mt * 128 + 2 * R) * 896 + ((c8 ^ ((R >> 1) & 7)) << 4);
  const int sw = (c8 ^ ((r8 & 2) << 1)) << 4;
  if (rl < 7) {
    const int role = rh * 7 + rl;
    c.vkv = R * 2688 + 896 + role * 128 + sw;
    c.vg = R * 3584 + role * 128 + sw;
    c.vu = R * 3584 + role * 128 + (sw ^ 32);
    c.vw2 = R * 896 + rl * 128 + sw;
    type_a(c, w, lane, mt, role, xc * 4 + (ix & 3), rl, out DIAGA);
  } else {
    c.vkv = R * 2688 + rh * 384 + sw;
    type_b(c, w, lane, mt, rh, gw DIAGA);
  }
  STAMP(15);
  if (b == MEASB && w == 0) { const u64 tgt = mrt + 4000; for (int i = 0; i < 100000; ++i) { if (__builtin_amdgcn_s_memrealtime() >= tgt) break; } }
}

extern "C" void kernel_launch(void* const* d_in, const int* in_sizes, int n_in, void* d_out, int out_size, void* d_ws, size_t ws_size,
                              hipStream_t stream) {
  fused_kernel<<<256, 512, 0, stream>>>(d_in[0], d_in[1], d_in[2], d_in[4], d_in[5], d_in[6], d_in[7], d_out, d_ws);
}
```

```cpp
#define CAL 3
#include <hip/hip_runtime.h>
#include <hip/hip_bf16.h>

typedef unsigned long long u64;
typedef unsigned u32;
typedef __bf16 bf16x8 __attribute__((ext_vector_type(8)));
typedef __bf16 bf16x2 __attribute__((ext_vector_type(2)));
typedef short s16x4 __attribute__((ext_vector_type(4)));
typedef u32 u32x4 __attribute__((ext_vector_type(4)));
typedef u32 u32x2 __attribute__((ext_vector_type(2)));
typedef float f32x16 __attribute__((ext_vector_type(16)));
typedef float f32x4 __attribute__((ext_vector_type(4)));
typedef __amdgpu_buffer_rsrc_t rsrc_t;
#define LAS __attribute__((address_space(3)))
#define GAS __attribute__((address_space(1)))
#define DI __device__ __forceinline__
#define SCHED() __builtin_amdgcn_sched_barrier(0)
template <int V> using IC = std::integral_constant<int, V>;

constexpr u32 WS_E = 0;
constexpr u32 WS_FLAG = 4096;
constexpr u32 WS_OMG = 32768;
constexpr u32 WS_K = 65536;
constexpr u32 KV_T = 28672;
constexpr u32 WS_V = WS_K + 64 * KV_T;
constexpr u32 WS_A2 = WS_V + 64 * KV_T;
constexpr u32 A2_T = 143360;
constexpr u32 WS_END = WS_A2 + 64 * A2_T;
constexpr u32 OOB = 0xFFFFFF00u;
constexpr int SC1 = 16;
constexpr int SLOT = 8192;
constexpr int SPIN_CAP = 1 << 15;

DI rsrc_t mk(const void* p, u32 bytes) { return __builtin_amdgcn_make_buffer_rsrc((void*)p, (short)0, (int)bytes, 0x00020000); }
DI void dma(u32 m0v, u32 voff, rsrc_t rs, u32 soff) {
  asm volatile("s_mov_b32 m0, %0\n\ts_nop 0\n\tbuffer_load_dwordx4 %1, %2, %3 offen lds" ::"s"(m0v), "v"(voff), "s"(rs), "s"(soff) : "memory");
}
DI u32x4 ld128_sc1(u32 voff, rsrc_t rs, u32 soff) {
  u32x4 v;
  asm volatile("buffer_load_dwordx4 %0, %1, %2, %3 offen sc1" : "=v"(v) : "v"(voff), "s"(rs), "s"(soff) : "memory");
  return v;
}
DI u32x2 ld64_sc1(u32 voff, rsrc_t rs, u32 soff) {
  u32x2 v;
  asm volatile("buffer_load_dwordx2 %0, %1, %2, %3 offen sc1" : "=v"(v) : "v"(voff), "s"(rs), "s"(soff) : "memory");
  return v;
}
DI u32 ld32_sc1(u32 voff, rsrc_t rs, u32 soff) {
  u32 v;
  asm volatile("buffer_load_dword %0, %1, %2, %3 offen sc1" : "=v"(v) : "v"(voff), "s"(rs), "s"(soff) : "memory");
  return v;
}
DI u32 touch(u32 voff, rsrc_t rs) {
  u32 v;
  asm volatile("buffer_load_dword %0, %1, %2, 0 offen" : "=v"(v) : "v"(voff), "s"(rs) : "memory");
  return v;
}
DI void st128(u32x4 v, rsrc_t rs, u32 voff, u32 soff) {
  asm volatile("buffer_store_dwordx4 %0, %1, %2, %3 offen sc1\n\ts_nop 1" ::"v"(v), "v"(voff), "s"(rs), "s"(soff) : "memory");
}
constexpr int clampvm(int n) { return n > 63 ? 63 : n; }
template <int N> DI void wait_vm() { static_assert(N >= 0, ""); asm volatile("s_waitcnt vmcnt(%0)" ::"n"(clampvm(N)) : "memory"); }
template <class T> DI void pin(T& v) { asm volatile("" : "+v"(v)); }

DI u32x4 trfrag(LAS char* p) {
  s16x4 lo = __builtin_amdgcn_ds_read_tr16_b64_v4i16((LAS s16x4*)p);
  s16x4 hi = __builtin_amdgcn_ds_read_tr16_b64_v4i16((LAS s16x4*)(p + 512));
  u32x2 a = __builtin_bit_cast(u32x2, lo), b = __builtin_bit_cast(u32x2, hi);
  return u32x4{a.x, a.y, b.x, b.y};
}
DI f32x16 mfma(u32x4 a, u32x4 b, f32x16 c) {
  return __builtin_amdgcn_mfma_f32_32x32x16_bf16(__builtin_bit_cast(bf16x8, a), __builtin_bit_cast(bf16x8, b), c, 0, 0, 0);
}
DI void mfma_at(f32x16& c, u32x4 a, u32x4 b) { c = mfma(a, b, c); asm volatile("" : "+v"(c)); }
DI u32 pk(float a, float b) { bf16x2 c; c[0] = (__bf16)a; c[1] = (__bf16)b; return __builtin_bit_cast(u32, c); }
DI u32x4 pk8(const f32x16& v, int s) { return u32x4{pk(v[8 * s], v[8 * s + 1]), pk(v[8 * s + 2], v[8 * s + 3]), pk(v[8 * s + 4], v[8 * s + 5]), pk(v[8 * s + 6], v[8 * s + 7])}; }
DI float glu(float g, float u) { return u * __builtin_amdgcn_rcpf(1.f + __builtin_amdgcn_exp2f(g * -1.4426950408889634f)); }
DI float xhalf_max(float v) { auto r = __builtin_amdgcn_permlane32_swap(__float_as_uint(v), __float_as_uint(v), false, false); return fmaxf(__uint_as_float(r[0]), __uint_as_float(r[1])); }
DI float xhalf_sum(float v) { auto r = __builtin_amdgcn_permlane32_swap(__float_as_uint(v), __float_as_uint(v), false, false); return __uint_as_float(r[0]) + __uint_as_float(r[1]); }

DI void spin(const char* ws, u32 off, bool need, u64 tag) {
  const GAS u64* p = (const GAS u64*)(ws + off);
#pragma unroll 1
  for (int i = 0; i < SPIN_CAP; ++i) {
    u64 v = __hip_atomic_load(p, __ATOMIC_RELAXED, __HIP_MEMORY_SCOPE_AGENT);
    if (__all(!need || v == tag)) break;
    __builtin_amdgcn_s_sleep(2);
  }
  asm volatile("" ::: "memory");
}
DI bool match(u32x2 v, bool need, u64 tag) { return __all(!need || (v.x == (u32)tag && v.y == (u32)(tag >> 32))); }
DI u64 tag_of(u32x2 ev) {
  u64 e = ((u64)__builtin_amdgcn_readfirstlane(ev.y) << 32) | (u32)__builtin_amdgcn_readfirstlane(ev.x);
  return e * 6364136223846793005ull + 1442695040888963407ull;
}

#define STAMP(i)
#define DIAGP
#define DIAGA
#define STAMP2(i)
template <int N> DI void sync_vm() { static_assert(N >= 0, ""); asm volatile("s_waitcnt vmcnt(%0)\n\ts_barrier" ::"n"(clampvm(N)) : "memory"); }

template <int I, int N, class Fn> DI void static_for(Fn&& f) {
  if constexpr (I < N) { f(IC<I>{}); static_for<I + 1, N>(f); }
}

#define PRO_DMA 16
#define P0_RATE 2
#define U_KVPUB 14
#define U_H1PUB 46
#define U_H2PUB 64
#define U_POLL1 54
#define U_POLL2 66
#define U_CHK2 69
#define U_POLL3 71
#define U_CHK3 74
#define LD_FIRST 6
#define LD_RATE 2
constexpr int NG = 98, U0 = 7;
constexpr int U_KVST = 0, U_H1STA = 30, U_H1STB = 32, U_H2ST = 56, U_CHK1 = 56;
constexpr int PRO_OPS = PRO_DMA + 1;
constexpr bool step_start(int u) { return u < 56 ? (u % 8 == 0) : (u < 91 && (u - 56) % 5 == 0); }
constexpr bool is_sync(int g) { return g < U0 ? true : step_start(g - U0 + 2); }
constexpr int last_seq_of_step(int u0) { return u0 < 56 ? 21 + (u0 / 4 + 2) * 2 - 1 : 49 + (u0 - 56) + 4; }
constexpr int need_seq(int g) { return g < 6 ? 3 * (g + 1) + 2 : (g == 6 ? last_seq_of_step(0) : last_seq_of_step(g - U0 + 2)); }
constexpr int freed_at(int g) { return g < U0 ? 3 * g : (g - U0 <= 56 ? 21 + 2 * ((g - U0) / 4) : 49 + (g - U0 - 56)); }
constexpr int freed(int g) { for (int j = g; j >= 0; --j) if (is_sync(j) || j == U0 + U_CHK1) return freed_at(j); return 0; }
constexpr int cum(int g) {
  int cc = PRO_DMA;
  for (int j = 0; j <= g; ++j) { int t = cc + (j < U0 ? P0_RATE : 1), cap = freed(j) + 20; t = t < cap ? t : cap; cc = t < 84 ? t : 84; }
  return cc;
}
constexpr int n_dma(int g) { return cum(g) - cum(g - 1); }
constexpr bool sched_ok() { for (int g = 0; g < NG; ++g) if (is_sync(g) && need_seq(g) >= cum(g - 1)) return false; return true; }
static_assert(sched_ok(), "a slot is needed before it is requested");
constexpr int n_st(int g) { int u = g - U0; return (u == U_POLL1) + (u == U_POLL2) + (u == U_POLL3) + 2 * (u == U_KVST) + (u == U_H1STA) + (u == U_H1STB) + 2 * (u == U_H2ST); }
constexpr int ld_rel(int k) { return k < LD_FIRST ? 0 : 1 + (k - LD_FIRST) / LD_RATE; }
constexpr int ld_iter(int it) { return U0 + (it < 14 ? U_CHK1 + ld_rel(it) : (it < 22 ? U_CHK2 + ld_rel(it - 14) : U_CHK3 + ld_rel(it - 22))); }
constexpr int ld_lo(int g) { for (int it = 0; it < 36; ++it) if (ld_iter(it) >= g) return it; return 36; }
constexpr int n_ld(int g) { return ld_lo(g + 1) - ld_lo(g); }
constexpr int ops_in(int g) { return n_st(g) + n_ld(g) + n_dma(g); }
constexpr int base(int g) { int t = PRO_OPS; for (int j = 0; j < g; ++j) t += ops_in(j); return t; }
constexpr int pos_st_last(int g) { return base(g) + n_st(g) - 1; }
constexpr int pos_ld(int it) { return base(ld_iter(it)) + n_st(ld_iter(it)) + (it - ld_lo(ld_iter(it))); }
constexpr int pos_dma(int n) {
  if (n < PRO_DMA) return n;
  for (int g = 0; g < NG; ++g)
    if (n < cum(g)) return base(g) + n_st(g) + n_ld(g) + (n - cum(g - 1));
  return -1000;
}
constexpr int imin(int a, int b) { return a < b ? a : b; }
constexpr int sync_cnt(int g) {
  int n = base(g) - pos_dma(need_seq(g)) - 1;
  if (g == U0 + U_KVPUB) n = imin(n, base(g) - pos_st_last(U0 + U_KVST) - 1);
  if (g == U0 + U_H1PUB) n = imin(n, base(g) - pos_st_last(U0 + U_H1STB) - 1);
  if (g == U0 + U_H2PUB) n = imin(n, base(g) - pos_st_last(U0 + U_H2ST) - 1);
  return n;
}
static_assert(is_sync(U0 + U_KVPUB) && is_sync(U0 + U_H1PUB) && is_sync(U0 + U_H2PUB) && is_sync(U0 + U_CHK2) && is_sync(U0 + U_CHK3), "");
static_assert(U_POLL1 < U_CHK1 && U_CHK1 < U_POLL2 && U_POLL2 < U_CHK2 && U_CHK2 < U_POLL3 && U_POLL3 < U_CHK3 && U_H2PUB <= U_CHK2, "");
constexpr int item_of(int i) { return i < 14 ? i : i + 1; }
constexpr int blk_of(int i) { return i < 14 ? i : (i < 21 ? i + 14 : i - 7); }
constexpr bool ld_ok() { for (int i = 0; i < 35; ++i) if (ld_iter(item_of(i)) > U0 + 56 + i) return false; for (int i = 1; i < 36; ++i) if (ld_iter(i) < ld_iter(i - 1)) return false; return true; }
static_assert(ld_ok(), "an operand is used before it is requested");

struct Ctx {
  LAS char* sm; u32 m0w;
  rsrc_t rx, rqkv, rwg, rwu, rwd, rwo, rws;
  const char* ws;
  rsrc_t rt; u32 toff;
  u32 vx, vkv, vg, vu, vw2;
};

template <int N> DI void issueA(const Ctx& c) {
  constexpr u32 dst = (N % 20) * SLOT;
  if constexpr (N < 21) {
    constexpr int kc = N / 3, j = N % 3;
    if constexpr (j < 2) dma(c.m0w + dst, c.vx, c.rx, j * 896 + kc * 128);
    else dma(c.m0w + dst, c.vkv, c.rqkv, kc * 64 * 2688);
  } else if constexpr (N < 49) {
    constexpr int kc = ((N - 21) / 2) % 7, ps = (N - 21) / 14;
    if constexpr ((N - 21) % 2 == 0) dma(c.m0w + dst, c.vg, c.rwg, kc * 64 * 3584 + ps * 1792);
    else dma(c.m0w + dst, c.vu, c.rwu, kc * 64 * 3584 + ps * 1792);
  } else if constexpr (blk_of(N - 49) < 28) dma(c.m0w + dst, c.vw2, c.rwd, blk_of(N - 49) * 64 * 896);
  else dma(c.m0w + dst, c.vw2, c.rwo, (blk_of(N - 49) - 28) * 64 * 896);
}

DI void type_a(Ctx& c, int w, int lane, int mt, int role, int R2, int n2, void* out DIAGP) {
  const int r = lane & 31, hi = lane >> 5, blk = (lane >> 4) & 1, q = (lane & 15) >> 2, p = lane & 3;
  const int sp = ((p & 1) << 1) | (p >> 1);
  const int rg = w & 3, cg = w >> 2, T = mt * 4 + rg;
  const bool isv = role >= 7;
  LAS char* sm = c.sm;
  static_for<0, PRO_DMA>([&](auto N) { issueA<N>(c); });
  u32x2 ev = ld64_sc1(c.toff, c.rt, 0);
  const int R = 32 * (rg & 1) + r, xm = (R >> 1) & 7;
  int xa[4];
#pragma unroll
  for (int s = 0; s < 4; ++s) xa[s] = (rg >> 1) * SLOT + R * 128 + (((2 * s + hi) ^ xm) << 4);
  const int rowb = (8 * hi + q) * 128, q2 = q & 2;
  const int akv = rowb + (((2 * cg + blk) ^ q2) << 5) + (isv ? p : sp) * 8;
  int agu[2];
#pragma unroll
  for (int i = 0; i < 2; ++i) agu[i] = blk * SLOT + rowb + (((2 * cg + i) ^ q2 ^ blk) << 5) + sp * 8;
  const int rt = w & 1, kq = w >> 1;
  int aw2[2];
#pragma unroll
  for (int i = 0; i < 2; ++i) aw2[i] = kq * 2048 + rowb + (((2 * i + blk) ^ q2) << 5) + p * 8;
  const int T2 = (R2 >> 1) * 4 + 2 * rt + (R2 & 1);

  u32x4 xf[28], kf[4], wf[2][2], af[35], hp[2];
  f32x16 akvacc = {}, g1[2] = {}, g2[2] = {}, ah[2] = {}, ao[2] = {};
  u32x2 pollv; u32 omg; u64 tag;
  const u32 lane16 = lane * 16;
  const u32 poll_off = WS_FLAG + (R2 >> 1) * 256 + (lane & 15) * 8;
  const bool need_h = (lane & 15) < 14;
  const u32 a2_off = WS_A2 + T2 * A2_T + kq * 1024 + lane16;
  const u32 h_off = WS_A2 + T * A2_T + (role * 4 + 2 * cg) * 1024 + lane16;

  auto read_kv = [&](auto J, auto S) {
    constexpr int j = J, sx = S;
    xf[4 * j + sx] = *(LAS u32x4*)(sm + xa[sx] + ((3 * j) % 20) * SLOT);
    kf[sx] = trfrag(sm + akv + ((3 * j + 2) % 20) * SLOT + sx * 2048);
  };
  auto read_w = [&](auto U, auto I) {
    constexpr int u = U, i = I;
    if constexpr (u < 56) {
      constexpr int sl = (21 + 2 * (u / 4)) % 20;
      const int wrap = sl == 19 ? blk * (-20 * SLOT) : 0;
      wf[u & 1][i] = trfrag(sm + agu[i] + wrap + sl * SLOT + (u % 4) * 2048);
    } else if constexpr (u < 91) {
      wf[u & 1][i] = trfrag(sm + aw2[i] + ((49 + u - 56) % 20) * SLOT);
    }
  };
  auto publish = [&](int cls) {
    if (w == 0) __builtin_amdgcn_raw_buffer_store_b64(u32x2{(u32)tag, (u32)(tag >> 32)}, c.rws, lane == 0 ? role * 8 : OOB, WS_FLAG + cls * 4096 + mt * 256, SC1);
  };
  auto load_item = [&](auto IT) {
    constexpr int it = IT;
    if constexpr (it == 14) omg = ld32_sc1(r * 4, c.rws, WS_OMG + T2 * 128);
    else { constexpr int i = it < 14 ? it : it - 1; af[i] = ld128_sc1(a2_off, c.rws, blk_of(i) * 4096); }
  };

  STAMP(1);
  sync_vm<PRO_OPS - 3>();
  STAMP(2);
  static_for<0, 4>([&](auto S) { read_kv(IC<0>{}, S); });
  SCHED();
  auto pass0 = [&](auto ISV) {
    static_for<0, U0>([&](auto G) {
      constexpr int g = G;
      sync_vm<sync_cnt(g)>();
      SCHED();
      static_for<0, 4>([&](auto S) {
        constexpr int sx = S;
        if constexpr (ISV) mfma_at(akvacc, xf[4 * g + sx], kf[sx]); else mfma_at(akvacc, kf[sx], xf[4 * g + sx]);
        SCHED();
        if constexpr (g < 6) read_kv(IC<g + 1>{}, S); else read_w(IC<sx / 2>{}, IC<sx % 2>{});
        if constexpr (cum(g - 1) + sx < cum(g)) issueA<cum(g - 1) + sx>(c);
        SCHED();
      });
    });
  };
  static_assert(P0_RATE <= 4, "");
  if (isv) pass0(IC<1>{}); else pass0(IC<0>{});
  auto check = [&](auto CNT, int cls, bool need) {
    wait_vm<CNT>();
    pin(pollv);
    if (w == 0 && !match(pollv, need, tag)) spin(c.ws, poll_off + cls * 4096, need, tag);
  };
  static_for<U0, NG>([&](auto G) {
    constexpr int g = G, u = g - U0, b = u & 1;
    if constexpr (u == 0) { STAMP(3); static_assert(pos_dma(need_seq(6)) > PRO_DMA, ""); pin(ev); tag = tag_of(ev); }
    if constexpr (u == 28) STAMP(4);
    if constexpr (u == 56) STAMP(5);
    if constexpr (u == 70) STAMP(8);
    if constexpr (u == 77) STAMP(9);
    if constexpr (is_sync(g)) STAMP2(16 + 2 * (u < 56 ? u / 8 : 7 + (u - 59) / 5));
    if constexpr (u == U_CHK2) check(IC<base(g) - base(U0 + U_POLL2) - 1>{}, 2, !need_h);
    if constexpr (u == U_CHK3) check(IC<base(g) - base(U0 + U_POLL3) - 1>{}, 2, need_h);
    if constexpr (is_sync(g)) {
      sync_vm<sync_cnt(g)>();
      STAMP2(17 + 2 * (u < 56 ? u / 8 : 7 + (u - 59) / 5));
    }
    if constexpr (u == U_KVPUB) publish(0);
    if constexpr (u == U_H1PUB) publish(1);
    if constexpr (u == U_H2PUB) publish(2);
    if constexpr (u == U_POLL1) pollv = ld64_sc1(w == 0 ? poll_off : OOB, c.rws, 4096);
    if constexpr (u == U_POLL2 || u == U_POLL3) pollv = ld64_sc1(w == 0 ? poll_off : OOB, c.rws, 8192);
    if constexpr (u == U_KVST) {
#pragma unroll
      for (int s = 0; s < 2; ++s) {
        const u32 off = isv ? WS_V + T * KV_T + (s * 2 + hi) * 7168 + ((role - 7) * 64 + cg * 32 + r) * 16
                            : WS_K + T * KV_T + (role * 8 + cg * 4 + 2 * s) * 512 + lane16;
        st128(pk8(akvacc, s), c.rws, off, 0);
      }
    }
    if constexpr (u == U_H1STA) st128(hp[0], c.rws, h_off, 0);
    if constexpr (u == U_H1STB) st128(hp[1], c.rws, h_off, 1024);
    if constexpr (u == U_H2ST) {
#pragma unroll
      for (int t = 0; t < 2; ++t) {
#pragma unroll
        for (int j = 0; j < 8; j += 2) hp[t][j / 2] = pk(glu(g2[t][j], g2[t][8 + j]), glu(g2[t][j + 1], g2[t][9 + j]));
        st128(hp[t], c.rws, h_off, (56 + t) * 1024);
        SCHED();
      }
    }
    asm volatile("" ::: "memory");
    if constexpr (u == U_CHK1) {
      STAMP2(44);
      check(IC<base(g) + n_st(g) - base(U0 + U_POLL1) - 1>{}, 1, need_h);
      asm volatile("s_barrier" ::: "memory");
      STAMP2(45);
    }
    static_for<ld_lo(g), ld_lo(g + 1)>(load_item);
    if constexpr (u >= 56) {
      wait_vm<base(g) + n_st(g) + n_ld(g) - pos_ld(item_of(u - 56)) - 1>();
      pin(af[u - 56]);
      if constexpr (u == 56) STAMP2(46);
      if constexpr (u == 70) STAMP2(47);
      if constexpr (u == 77) STAMP2(48);
    }
    SCHED();
    static_for<0, 2>([&](auto I) {
      constexpr int i = I;
      if constexpr (u < 28) mfma_at(g1[i], wf[b][i], xf[u]);
      else if constexpr (u < 56) mfma_at(g2[i], wf[b][i], xf[u - 28]);
      else if constexpr (blk_of(u - 56) < 28) mfma_at(ah[i], wf[b][i], af[u - 56]);
      else mfma_at(ao[i], wf[b][i], af[u - 56]);
      SCHED();
      read_w(IC<u + 2>{}, I);
      if constexpr (u >= 28 && u < 32) {
        constexpr int t = (u - 28) / 2, j = ((u - 28) % 2) * 4 + 2 * i;
        u32 hv = pk(glu(g1[t][j], g1[t][8 + j]), glu(g1[t][j + 1], g1[t][9 + j]));
        pin(hv);
        hp[t][j / 2] = hv;
      }
      if constexpr (i == 1) static_for<cum(g - 1), cum(g)>([&](auto N) { issueA<N>(c); });
      SCHED();
    });
  });
  pin(omg);
  STAMP(6);
  const float om = __uint_as_float(omg);
  LAS char* red = sm + 4 * SLOT;
#pragma unroll
  for (int t = 0; t < 2; ++t)
#pragma unroll
    for (int a = 0; a < 4; ++a) {
      f32x4 v;
#pragma unroll
      for (int e = 0; e < 4; ++e) v[e] = ah[t][4 * a + e] * om + ao[t][4 * a + e];
      *(LAS f32x4*)(red + kq * 16384 + (2 * r + rt) * 256 + (((8 * t + 2 * a + hi) ^ (r & 15)) << 4)) = v;
    }
  asm volatile("s_waitcnt lgkmcnt(0)\n\ts_barrier" ::: "memory");
  STAMP(7);
  {
    const int row = w * 8 + (lane >> 3), c8 = lane & 7, m = (row >> 1) & 15;
    f32x4 s0 = {}, s1 = {};
#pragma unroll
    for (int k = 0; k < 4; ++k) {
      s0 += *(LAS f32x4*)(red + k * 16384 + row * 256 + (((2 * c8) ^ m) << 4));
      s1 += *(LAS f32x4*)(red + k * 16384 + row * 256 + (((2 * c8 + 1) ^ m) << 4));
    }
    u32x4 o = {pk(s0[0], s0[1]), pk(s0[2], s0[3]), pk(s1[0], s1[1]), pk(s1[2], s1[3])};
    *(u32x4*)((char*)out + (size_t)(R2 * 64 + row) * 896 + n2 * 128 + c8 * 16) = o;
  }
}
static_assert(freed(U0 + 90) >= 72, "the reduction scratch (slots 4..11 = sequence 64..71) must be free at the end");

constexpr int PRO_B = 12;
constexpr int freedB(int g) { return 6 * ((g + 1) / 4); }
constexpr int cumB(int g) {
  int cc = PRO_B;
  for (int j = 0; j <= g; ++j) { int t = cc + 2, cap = freedB(j) + 18; t = t < cap ? t : cap; cc = t < 42 ? t : 42; }
  return cc;
}
#define GB_POLL 20
#define GB_CHK 23
constexpr int n_stB(int g) { return g == GB_POLL; }
constexpr int baseB(int g) { int t = PRO_B + 1; for (int j = 0; j < g; ++j) t += n_stB(j) + cumB(j) - cumB(j - 1); return t; }
constexpr int pos_dmaB(int n) {
  if (n < PRO_B) return n;
  for (int g = 0; g < 28; ++g) if (n < cumB(g)) return baseB(g) + n_stB(g) + (n - cumB(g - 1));
  return -1000;
}
constexpr int sync_cntB(int g) {
  int n = baseB(g) - pos_dmaB(6 * (g / 4 + 2) - 1) - 1;
  return n;
}
template <int N> DI void issueB(const Ctx& c) {
  constexpr int kc = N / 6, j = N % 6;
  constexpr u32 dst = (N % 18) * SLOT;
  if constexpr (j < 2) dma(c.m0w + dst, c.vx, c.rx, j * 896 + kc * 128);
  else dma(c.m0w + dst, c.vkv, c.rqkv, kc * 64 * 2688 + (j - 2) * 128);
}

DI void type_b(Ctx& c, int w, int lane, int mt, int hf, const void* gw DIAGP) {
  const int r = lane & 31, hi = lane >> 5, blk = (lane >> 4) & 1, q = (lane & 15) >> 2, p = lane & 3;
  const int sp = ((p & 1) << 1) | (p >> 1);
  const int rg = w & 3, cg = w >> 2, T = mt * 4 + rg;
  LAS char* sm = c.sm;
  if (w == 0) dma(c.m0w + 18 * SLOT, lane * 16, mk(gw, 896), 0);
  static_for<0, PRO_B>([&](auto N) { issueB<N>(c); });
  u32x2 ev = ld64_sc1(c.toff, c.rt, 0);
  const int R = 32 * (rg & 1) + r, xm = (R >> 1) & 7;
  int xa[4], aq[4];
#pragma unroll
  for (int s = 0; s < 4; ++s) xa[s] = (rg >> 1) * SLOT + R * 128 + (((2 * s + hi) ^ xm) << 4);
  const int h0 = 8 * hf + 4 * cg;
#pragma unroll
  for (int i = 0; i < 4; ++i) {
    const int col = 28 * (h0 + i) - 4 * (i & 1) - 192 * hf + 16 * blk + 4 * sp;
    aq[i] = (2 + (col >> 6)) * SLOT + (8 * hi + q) * 128 + (((col & 63) * 2) ^ ((q & 2) << 5));
  }
  const bool first = (mt == 0) && !(rg & 1);
  const int Tp = first ? T : ((rg & 1) ? T - 1 : T - 3);
  const u32 lane16 = lane * 16, lo16 = hi ? OOB : lane16;
  const u32 ob = WS_A2 + T * A2_T + (1792 + 28 * h0) * 64;
  const u32 omg_off = (hf == 0 && cg == 0 && hi == 0) ? r * 4 : OOB;
  const u32 poll_off = WS_FLAG + ((lane & 16) && mt > 0 ? mt - 1 : mt) * 256 + (lane & 15) * 8;
  const bool poll_need = (lane & 15) < 14;
  u32x4 xf[2], gf[2], wf[2][4];
  u32x4 kf[4][2], kp[2], vf[4][2], vp[4];
  f32x16 qa[4] = {};
  u32x2 pollv;
  const int sr = (r & 19) | ((r & 4) << 1) | ((r & 8) >> 1);
  const u32 kp_off = (28 * (r >> 3) - 4 * ((r >> 3) & 1)) * 64 + (hi * 32 + 24 + (r & 7)) * 16;
  const u32 v_off = hi * 7168 + sr * 16;
  auto load_kv = [&](auto IT) {
    constexpr int it = IT;
    if constexpr (it < 2) kp[it] = ld128_sc1(kp_off, c.rws, WS_K + Tp * KV_T + 28 * h0 * 64 + it * 1024);
    else {
      constexpr int i = (it - 2) / 5, k = (it - 2) % 5;
      const int wh = 28 * (h0 + i) - 4 * (i & 1);
      if constexpr (k < 2) kf[i][k] = ld128_sc1(lane16, c.rws, WS_K + T * KV_T + wh * 64 + k * 1024);
      else if constexpr (k == 2) vp[i] = ld128_sc1(v_off, c.rws, WS_V + Tp * KV_T + 14336 + wh * 16);
      else vf[i][k - 3] = ld128_sc1(v_off, c.rws, WS_V + T * KV_T + (k - 3) * 14336 + wh * 16);
    }
  };
  auto read_x = [&](auto G) {
    constexpr int g = G;
    if constexpr (g < 28) {
      xf[g & 1] = *(LAS u32x4*)(sm + xa[g % 4] + ((6 * (g / 4)) % 18) * SLOT);
      gf[g & 1] = *(LAS u32x4*)(sm + 18 * SLOT + g * 32 + hi * 16);
    }
  };
  auto read_w = [&](auto G, auto I) {
    constexpr int g = G;
    if constexpr (g < 28) wf[g & 1][I] = trfrag(sm + aq[I] + ((6 * (g / 4)) % 18) * SLOT + (g % 4) * 2048);
  };
  STAMP(1);
  sync_vm<PRO_B + 1 - 6>();
  STAMP(2);
  u64 tag;
  read_x(IC<0>{});
  static_for<0, 4>([&](auto I) { read_w(IC<0>{}, I); });
  SCHED();
  static_for<0, 28>([&](auto G) {
    constexpr int g = G, b = g & 1;
    if constexpr (g == GB_CHK) {
      STAMP(8);
      wait_vm<baseB(g) - baseB(GB_POLL) - 1>();
      pin(pollv);
      if (w == 0 && !match(pollv, poll_need, tag)) spin(c.ws, poll_off, poll_need, tag);
    }
    if constexpr (g % 4 == 3 && g < 27) { STAMP2(16 + 2 * (g / 4)); asm volatile("s_waitcnt lgkmcnt(0)" ::: "memory"); sync_vm<sync_cntB(g)>(); STAMP2(17 + 2 * (g / 4)); }
    if constexpr (g == 7) { pin(ev); tag = tag_of(ev); }
    if constexpr (g == GB_CHK) STAMP(9);
    if constexpr (g == GB_POLL) pollv = ld64_sc1(w == 0 ? poll_off : OOB, c.rws, 0);
    SCHED();
    static_for<0, 4>([&](auto I) {
      constexpr int i = I;
      mfma_at(qa[i], wf[b][i], xf[b]);
      SCHED();
      if constexpr (i == 0) read_x(IC<g + 1>{});
      read_w(IC<g + 1>{}, I);
      if constexpr (i < 2) {
        wf[b][2][2 * i] = r == 28 ? gf[b][2 * i] : wf[b][2][2 * i];
        wf[b][2][2 * i + 1] = r == 28 ? gf[b][2 * i + 1] : wf[b][2][2 * i + 1];
        pin(wf[b][2]);
      }
      if constexpr (i >= 2 && cumB(g - 1) + i - 2 < cumB(g)) issueB<cumB(g - 1) + i - 2>(c);
      if constexpr (g >= GB_CHK) if constexpr ((g - GB_CHK) * 4 + i < 22) load_kv(IC<(g - GB_CHK) * 4 + i>{});
      SCHED();
    });
  });
  static_assert(cumB(GB_CHK - 1) == 42, "no DMA after the first K/V load");
  static_for<(28 - GB_CHK) * 4, 22>(load_kv);
  STAMP(3);
  const float z = __uint_as_float(__builtin_amdgcn_permlane32_swap(__float_as_uint(qa[2][12]), __float_as_uint(qa[2][12]), false, false)[1]);
  const float gate = __builtin_amdgcn_rcpf(1.f + __builtin_amdgcn_exp2f(z * -1.4426950408889634f));
  const u32 omg = __float_as_uint(__builtin_amdgcn_rcpf(1.f + __builtin_amdgcn_exp2f(z * 1.4426950408889634f)));
  int ab[4];
  u32 msk[5][2];
#pragma unroll
  for (int b = 0; b < 4; ++b) ab[b] = (r >> 3) - ((r & 7) < 4 * hi + b ? 1 : 0);
#pragma unroll
  for (int a = 0; a < 5; ++a)
#pragma unroll
    for (int e = 0; e < 2; ++e) msk[a][e] = (ab[2 * e] == a - 1 ? 0xFFFFu : 0u) | (ab[2 * e + 1] == a - 1 ? 0xFFFF0000u : 0u);
  const float csc = 0.18898223650461362f * 1.4426950408889634f;
  u32x4 op[4][2];
  static_for<0, 4>([&](auto I) {
    constexpr int i = I;
    u32x4 qf[2] = {pk8(qa[i], 0), pk8(qa[i], 1)};
    if (i & 1) { qf[0][0] = hi ? qf[0][0] : 0u; qf[0][1] = hi ? qf[0][1] : 0u; }
    else       { qf[1][2] = hi ? 0u : qf[1][2]; qf[1][3] = hi ? 0u : qf[1][3]; }
    wait_vm<15 - 5 * i>();
    if constexpr (i == 0) { pin(kp[0]); pin(kp[1]); STAMP(4); }
    pin(kf[i][0]); pin(kf[i][1]); pin(vp[i]); pin(vf[i][0]); pin(vf[i][1]);
    f32x16 so = {}, sv = {};
    so = mfma(kf[i][0], qf[0], so); sv = mfma(kp[0], qf[0], sv);
    so = mfma(kf[i][1], qf[1], so); sv = mfma(kp[1], qf[1], sv);
    float v[4];
#pragma unroll
    for (int b = 0; b < 4; ++b) {
      float t = first ? -1e30f : sv[4 * i + b];
#pragma unroll
      for (int a = 0; a < 4; ++a) t = ab[b] == a ? so[4 * a + b] : t;
      v[b] = t;
    }
    const float mx = xhalf_max(fmaxf(fmaxf(v[0], v[1]), fmaxf(v[2], v[3])));
#pragma unroll
    for (int b = 0; b < 4; ++b) v[b] = __builtin_amdgcn_exp2f((v[b] - mx) * csc);
    const float sc = gate * __builtin_amdgcn_rcpf(xhalf_sum((v[0] + v[1]) + (v[2] + v[3])));
    const u32 p01 = pk(v[0] * sc, v[1] * sc), p23 = pk(v[2] * sc, v[3] * sc);
    f32x16 oa = {};
    oa = mfma(vp[i], u32x4{0u, 0u, p01 & msk[0][0], p23 & msk[0][1]}, oa);
    oa = mfma(vf[i][0], u32x4{p01 & msk[1][0], p23 & msk[1][1], p01 & msk[2][0], p23 & msk[2][1]}, oa);
    oa = mfma(vf[i][1], u32x4{p01 & msk[3][0], p23 & msk[3][1], p01 & msk[4][0], p23 & msk[4][1]}, oa);
    op[i][0] = pk8(oa, 0); op[i][1] = pk8(oa, 1);
  });
  asm volatile("" ::: "memory");
  __builtin_amdgcn_raw_buffer_store_b32(omg, c.rws, omg_off, WS_OMG + T * 128, SC1);
#pragma unroll
  for (int i = 1; i < 4; i += 2) {
#pragma unroll
    for (int e = 0; e < 2; ++e) { auto t = __builtin_amdgcn_permlane32_swap(op[i - 1][1][e], op[i][0][e], false, false); op[i - 1][1][e] = t[0]; op[i][0][e] = t[1]; }
    const u32 o2 = ob + (i >> 1) * 3584;
    st128(op[i - 1][0], c.rws, lane16, o2);
    st128(op[i - 1][1], c.rws, lo16, o2 + 1024);
    st128(op[i][0], c.rws, lane16, o2 + 1536);
    st128(op[i][1], c.rws, lane16, o2 + 2560);
  }
  STAMP(5);
  sync_vm<0>();
  if (w == 0) __builtin_amdgcn_raw_buffer_store_b64(u32x2{(u32)tag, (u32)(tag >> 32)}, c.rws, lane == 0 ? (14 + hf) * 8 : OOB, WS_FLAG + 8192 + mt * 256, SC1);
  STAMP(6);
  if (mt == 0 && hf == 0 && w == 0) {
#pragma unroll 1
    for (int i = 0; i < 4; ++i) spin(c.ws, WS_FLAG + 8192 + (i * 4 + (lane >> 4)) * 256 + (lane & 15) * 8, true, tag);
    __builtin_amdgcn_raw_buffer_store_b64(u32x2{(u32)tag, (u32)(tag >> 32)}, c.rws, lane == 0 ? 0 : OOB, WS_E, SC1);
  }
}

__global__ __launch_bounds__(512, 2) void fused_kernel(const void* __restrict__ x, const void* __restrict__ wqkv, const void* __restrict__ wo,
                                                       const void* __restrict__ wg, const void* __restrict__ wu, const void* __restrict__ wd,
                                                       const void* __restrict__ gw, void* __restrict__ out, void* __restrict__ ws) {
  __shared__ __attribute__((aligned(1024))) char smem[163840];
#if defined(CAL) && !(CAL & 1)
  if (blockIdx.x == 0 && threadIdx.x == 0) __hip_atomic_store((u64*)((char*)ws + (32 << 20)), (u64)__builtin_amdgcn_s_memrealtime(), __ATOMIC_RELAXED, __HIP_MEMORY_SCOPE_AGENT);
#endif
  const int lane = threadIdx.x & 63, w = __builtin_amdgcn_readfirstlane(threadIdx.x >> 6);
  const int b = blockIdx.x, xc = b & 7, ix = b >> 3;
  const int mq = xc >> 1, mt = mq * 4 + (ix & 3), rl = ix >> 2, rh = xc & 1;
  Ctx c;
  c.sm = (LAS char*)smem;
  c.m0w = (u32)(size_t)c.sm + w * 1024;
  c.rx = mk(x, 2048 * 896); c.rqkv = mk(wqkv, 448 * 2688); c.rwg = mk(wg, 448 * 3584); c.rwu = mk(wu, 448 * 3584);
  c.rwd = mk(wd, 1792 * 896); c.rwo = mk(wo, 448 * 896); c.rws = mk(ws, WS_END);
  c.ws = (const char*)ws;
  {
    const int i = ix * 49 + lane, row = mq * 112 + i / 14, l = i % 14;
    const u32 o12 = row * 3584 + (rh * 7 + (l < 7 ? l : l + 7)) * 128;
    const u32 o34 = ((w == 3 ? b * 49 : b * 13) + lane) * 128;
    const int n = w == 0 ? 1 : (w < 4 ? 49 : (w == 4 ? 13 : 0));
    c.toff = lane < n ? (w == 0 ? WS_E : (w < 3 ? o12 : o34)) : OOB;
    c.rt = w == 0 ? c.rws : (w == 1 ? c.rwg : (w == 2 ? c.rwu : (w == 3 ? c.rwd : c.rwo)));
  }
  const int r8 = lane >> 3, c8 = lane & 7, R = 8 * w + r8;
  c.vx = (mt * 128 + 2 * R) * 896 + ((c8 ^ ((R >> 1) & 7)) << 4);
  const int sw = (c8 ^ ((r8 & 2) << 1)) << 4;
  if (rl < 7) {
    const int role = rh * 7 + rl;
    c.vkv = R * 2688 + 896 + role * 128 + sw;
    c.vg = R * 3584 + role * 128 + sw;
    c.vu = R * 3584 + role * 128 + (sw ^ 32);
    c.vw2 = R * 896 + rl * 128 + sw;
    type_a(c, w, lane, mt, role, xc * 4 + (ix & 3), rl, out DIAGA);
  } else {
    c.vkv = R * 2688 + rh * 384 + sw;
    type_b(c, w, lane, mt, rh, gw DIAGA);
  }
#if defined(CAL) && !(CAL & 2)
  if (b == 0 && w == 0) { const u64 tgt = __hip_atomic_load((u64*)((char*)ws + (32 << 20)), __ATOMIC_RELAXED, __HIP_MEMORY_SCOPE_AGENT) + 6000; for (int i = 0; i < 200000; ++i) { if (__builtin_amdgcn_s_memrealtime() >= tgt) break; } }
#endif
}

__global__ void tiny_first(u64* p) { if (threadIdx.x == 0) __hip_atomic_store(p, (u64)__builtin_amdgcn_s_memrealtime(), __ATOMIC_RELAXED, __HIP_MEMORY_SCOPE_AGENT); }
__global__ void tiny_last(u64* p) { const u64 tgt = __hip_atomic_load(p, __ATOMIC_RELAXED, __HIP_MEMORY_SCOPE_AGENT) + 6000; for (int i = 0; i < 200000; ++i) { if (__builtin_amdgcn_s_memrealtime() >= tgt) break; } }
extern "C" void kernel_launch(void* const* d_in, const int* in_sizes, int n_in, void* d_out, int out_size, void* d_ws, size_t ws_size,
                              hipStream_t stream) {
  tiny_first<<<1, 64, 0, stream>>>((u64*)((char*)d_ws + (32 << 20)));
  fused_kernel<<<256, 512, 0, stream>>>(d_in[0], d_in[1], d_in[2], d_in[4], d_in[5], d_in[6], d_in[7], d_out, d_ws);
  tiny_last<<<1, 64, 0, stream>>>((u64*)((char*)d_ws + (32 << 20)));
}
```

```cpp
#include <hip/hip_runtime.h>
#include <hip/hip_bf16.h>

typedef unsigned long long u64;
typedef unsigned u32;
typedef __bf16 bf16x8 __attribute__((ext_vector_type(8)));
typedef __bf16 bf16x2 __attribute__((ext_vector_type(2)));
typedef short s16x4 __attribute__((ext_vector_type(4)));
typedef u32 u32x4 __attribute__((ext_vector_type(4)));
typedef u32 u32x2 __attribute__((ext_vector_type(2)));
typedef float f32x16 __attribute__((ext_vector_type(16)));
typedef float f32x4 __attribute__((ext_vector_type(4)));
typedef __amdgpu_buffer_rsrc_t rsrc_t;
#define LAS __attribute__((address_space(3)))
#define GAS __attribute__((address_space(1)))
#define DI __device__ __forceinline__
#define SCHED() __builtin_amdgcn_sched_barrier(0)
template <int V> using IC = std::integral_constant<int, V>;

constexpr u32 WS_E = 0;
constexpr u32 WS_FLAG = 4096;
constexpr u32 WS_OMG = 32768;
constexpr u32 WS_K = 65536;
constexpr u32 KV_T = 28672;
constexpr u32 WS_V = WS_K + 64 * KV_T;
constexpr u32 WS_A2 = WS_V + 64 * KV_T;
constexpr u32 A2_T = 143360;
constexpr u32 WS_END = WS_A2 + 64 * A2_T;
constexpr u32 OOB = 0xFFFFFF00u;
constexpr int SC1 = 16;
constexpr int SLOT = 8192;
constexpr int SPIN_CAP = 1 << 15;

DI rsrc_t mk(const void* p, u32 bytes) { return __builtin_amdgcn_make_buffer_rsrc((void*)p, (short)0, (int)bytes, 0x00020000); }
DI void dma(u32 m0v, u32 voff, rsrc_t rs, u32 soff) {
  asm volatile("s_mov_b32 m0, %0\n\ts_nop 0\n\tbuffer_load_dwordx4 %1, %2, %3 offen lds" ::"s"(m0v), "v"(voff), "s"(rs), "s"(soff) : "memory");
}
DI u32x4 ld128_sc1(u32 voff, rsrc_t rs, u32 soff) {
  u32x4 v;
  asm volatile("buffer_load_dwordx4 %0, %1, %2, %3 offen sc1" : "=v"(v) : "v"(voff), "s"(rs), "s"(soff) : "memory");
  return v;
}
DI u32x2 ld64_sc1(u32 voff, rsrc_t rs, u32 soff) {
  u32x2 v;
  asm volatile("buffer_load_dwordx2 %0, %1, %2, %3 offen sc1" : "=v"(v) : "v"(voff), "s"(rs), "s"(soff) : "memory");
  return v;
}
DI u32 ld32_sc1(u32 voff, rsrc_t rs, u32 soff) {
  u32 v;
  asm volatile("buffer_load_dword %0, %1, %2, %3 offen sc1" : "=v"(v) : "v"(voff), "s"(rs), "s"(soff) : "memory");
  return v;
}
DI u32 touch(u32 voff, rsrc_t rs) {
  u32 v;
  asm volatile("buffer_load_dword %0, %1, %2, 0 offen" : "=v"(v) : "v"(voff), "s"(rs) : "memory");
  return v;
}
DI void st128(u32x4 v, rsrc_t rs, u32 voff, u32 soff) {
  asm volatile("buffer_store_dwordx4 %0, %1, %2, %3 offen sc1\n\ts_nop 1" ::"v"(v), "v"(voff), "s"(rs), "s"(soff) : "memory");
}
constexpr int clampvm(int n) { return n > 63 ? 63 : n; }
template <int N> DI void wait_vm() { static_assert(N >= 0, ""); asm volatile("s_waitcnt vmcnt(%0)" ::"n"(clampvm(N)) : "memory"); }
template <class T> DI void pin(T& v) { asm volatile("" : "+v"(v)); }

DI u32x4 trfrag(LAS char* p) {
  s16x4 lo = __builtin_amdgcn_ds_read_tr16_b64_v4i16((LAS s16x4*)p);
  s16x4 hi = __builtin_amdgcn_ds_read_tr16_b64_v4i16((LAS s16x4*)(p + 512));
  u32x2 a = __builtin_bit_cast(u32x2, lo), b = __builtin_bit_cast(u32x2, hi);
  return u32x4{a.x, a.y, b.x, b.y};
}
DI f32x16 mfma(u32x4 a, u32x4 b, f32x16 c) {
  return __builtin_amdgcn_mfma_f32_32x32x16_bf16(__builtin_bit_cast(bf16x8, a), __builtin_bit_cast(bf16x8, b), c, 0, 0, 0);
}
DI void mfma_at(f32x16& c, u32x4 a, u32x4 b) { c = mfma(a, b, c); asm volatile("" : "+v"(c)); }
DI u32 pk(float a, float b) { bf16x2 c; c[0] = (__bf16)a; c[1] = (__bf16)b; return __builtin_bit_cast(u32, c); }
DI u32x4 pk8(const f32x16& v, int s) { return u32x4{pk(v[8 * s], v[8 * s + 1]), pk(v[8 * s + 2], v[8 * s + 3]), pk(v[8 * s + 4], v[8 * s + 5]), pk(v[8 * s + 6], v[8 * s + 7])}; }
DI float glu(float g, float u) {
  return u * __builtin_amdgcn_rcpf(1.f + __builtin_amdgcn_exp2f(g * -1.4426950408889634f)); }
DI float xhalf_max(float v) { auto r = __builtin_amdgcn_permlane32_swap(__float_as_uint(v), __float_as_uint(v), false, false); return fmaxf(__uint_as_float(r[0]), __uint_as_float(r[1])); }
DI float xhalf_sum(float v) { auto r = __builtin_amdgcn_permlane32_swap(__float_as_uint(v), __float_as_uint(v), false, false); return __uint_as_float(r[0]) + __uint_as_float(r[1]); }

DI void spin(const char* ws, u32 off, bool need, u64 tag) {
  const GAS u64* p = (const GAS u64*)(ws + off);
#pragma unroll 1
  for (int i = 0; i < SPIN_CAP; ++i) {
    u64 v = __hip_atomic_load(p, __ATOMIC_RELAXED, __HIP_MEMORY_SCOPE_AGENT);
    if (__all(!need || v == tag)) break;
    __builtin_amdgcn_s_sleep(2);
  }
  asm volatile("" ::: "memory");
}
DI bool match(u32x2 v, bool need, u64 tag) {
  return __all(!need || (v.x == (u32)tag && v.y == (u32)(tag >> 32))); }
DI u64 tag_of(u32x2 ev) {
  u64 e = ((u64)__builtin_amdgcn_readfirstlane(ev.y) << 32) | (u32)__builtin_amdgcn_readfirstlane(ev.x);
  return e * 6364136223846793005ull + 1442695040888963407ull;
}

#define STAMP(i)
#define DIAGP
#define DIAGA
#define STAMP2(i)
template <int N> DI void sync_vm() { static_assert(N >= 0, ""); asm volatile("s_waitcnt vmcnt(%0)\n\ts_barrier" ::"n"(clampvm(N)) : "memory"); }

template <int I, int N, class Fn> DI void static_for(Fn&& f) {
  if constexpr (I < N) { f(IC<I>{}); static_for<I + 1, N>(f); }
}

#define DEFP(n, v) constexpr int n = v
#define CFG 16,3,17,8,40,57,52,56,64,68,70,76,2,6,6,2,6,6,2,0,1,2,16,16,0
constexpr int kCfg[] = {CFG};
constexpr int PRO_DMA = kCfg[0], P0_RATE = kCfg[1], G_TOUCH = kCfg[2], U_KVPUB = kCfg[3], U_H1PUB = kCfg[4], U_H2PUB = kCfg[5];
constexpr int U_POLL1 = kCfg[6], U_CHK1 = kCfg[7], U_POLL2 = kCfg[8], U_CHK2 = kCfg[9], U_POLL3 = kCfg[10], U_CHK3 = kCfg[11];
constexpr int EP1 = kCfg[12];
constexpr int ld_rel(int cl, int k) { int f = kCfg[13 + 3 * cl], a = kCfg[14 + 3 * cl], b = kCfg[15 + 3 * cl]; return k < f ? k / a : (f + a - 1) / a + (k - f) / b; }
#define ORDER 1
constexpr int N2 = ORDER ? 14 : 8, IT_OMG = ORDER ? 28 : 14;
constexpr int NG = 98, U0 = 7;
constexpr int U_KVST = 0, U_H1STA = 28 + EP1, U_H1STB = 28 + 2 * EP1, U_H2ST = 56;
constexpr int PRO_OPS = PRO_DMA;
constexpr bool step_start(int u) { return u < 56 ? (u % 8 == 0) : (u < 91 && (u - 56) % 5 == 0); }
constexpr bool is_sync(int g) { return g < U0 ? true : step_start(g - U0 + 2); }
constexpr bool is_bar(int g) { int u = g - U0; return is_sync(g) || u == U_KVPUB || u == U_H1PUB || u == U_H2PUB || u == U_CHK1 || u == U_CHK2 || u == U_CHK3; }
constexpr int last_seq_of_step(int u0) { return u0 < 56 ? 21 + (u0 / 4 + 2) * 2 - 1 : 49 + (u0 - 56) + 4; }
constexpr int need_seq(int g) { return g < 6 ? 3 * (g + 1) + 2 : (g == 6 ? last_seq_of_step(0) : last_seq_of_step(g - U0 + 2)); }
constexpr int freed_at(int g) { return g < U0 ? 3 * g : (g - U0 <= 56 ? 21 + 2 * ((g - U0) / 4) : 49 + (g - U0 - 56)); }
constexpr int freed(int g) { for (int j = g; j >= 0; --j) if (is_bar(j)) return freed_at(j); return 0; }
constexpr int n_pay(int g) { int u = g - U0; return 2 * (u == U_KVST) + (u == U_H1STA) + (u == U_H1STB) + 2 * (u == U_H2ST); }
constexpr int cum(int g) {
  int cc = PRO_DMA, cr = 0;
  for (int j = 0; j <= g; ++j) {
    int k = P0_RATE;
    if (j >= U0) { cr += (j - U0 < 56 ? kCfg[22] : kCfg[23]) - kCfg[24] * n_pay(j); k = cr >= 16 ? 1 : 0; }
    int t = cc + k, cap = freed(j) + 20; t = t < cap ? t : cap; t = t < 84 ? t : 84;
    if (j >= U0) { cr -= 16 * (t - cc); if (cr > 16) cr = 16; }
    cc = t;
  }
  return cc;
}
constexpr int n_dma(int g) { return cum(g) - cum(g - 1); }
constexpr bool sched_ok() { for (int g = 0; g < NG; ++g) if (is_sync(g) && need_seq(g) >= cum(g - 1)) return false; return true; }
static_assert(sched_ok(), "a slot is needed before it is requested");
constexpr int n_st(int g) { int u = g - U0; return (g == G_TOUCH) + (u == U_POLL1) + (u == U_POLL2) + (u == U_POLL3) + 2 * (u == U_KVST) + (u == U_H1STA) + (u == U_H1STB) + 2 * (u == U_H2ST); }
constexpr int ld_iter(int it) { return U0 + (it < 14 ? U_CHK1 + ld_rel(0, it) : (it < 14 + N2 ? U_CHK2 + ld_rel(1, it - 14) : U_CHK3 + ld_rel(2, it - 14 - N2))); }
constexpr int ld_lo(int g) { for (int it = 0; it < 36; ++it) if (ld_iter(it) >= g) return it; return 36; }
constexpr int n_ld(int g) { return ld_lo(g + 1) - ld_lo(g); }
constexpr int ops_in(int g) { return n_st(g) + n_ld(g) + n_dma(g); }
constexpr int base(int g) { int t = PRO_OPS; for (int j = 0; j < g; ++j) t += ops_in(j); return t; }
constexpr int pos_st_last(int g) { return base(g) + n_st(g) - 1; }
constexpr int pos_ld(int it) { return base(ld_iter(it)) + n_st(ld_iter(it)) + (it - ld_lo(ld_iter(it))); }
constexpr int pos_dma(int n) {
  if (n < PRO_DMA) return n;
  for (int g = 0; g < NG; ++g)
    if (n < cum(g)) return base(g) + n_st(g) + n_ld(g) + (n - cum(g - 1));
  return -1000;
}
constexpr int imin(int a, int b) { return a < b ? a : b; }
constexpr int sync_cnt(int g) {
  int n = is_sync(g) ? base(g) - pos_dma(need_seq(g)) - 1 : 63;
  if (g == U0 + U_KVPUB) n = imin(n, base(g) - pos_st_last(U0 + U_KVST) - 1);
  if (g == U0 + U_H1PUB) n = imin(n, base(g) - pos_st_last(U0 + U_H1STB) - 1);
  if (g == U0 + U_H2PUB) n = imin(n, base(g) - pos_st_last(U0 + U_H2ST) - 1);
  return n;
}
static_assert(U_POLL1 < U_CHK1 && U_CHK1 < U_POLL2 && U_POLL2 < U_CHK2 && U_CHK2 < U_POLL3 && U_POLL3 < U_CHK3, "");
static_assert(U_KVPUB > U_KVST && U_H1PUB > U_H1STB && U_H2PUB > U_H2ST && U_H1PUB < U_CHK1 && U_H2PUB < U_CHK2, "a workgroup publishes before it waits");
constexpr int item_of(int i) { return i < IT_OMG ? i : i + 1; }
constexpr int blk_of(int i) { return ORDER || i < 14 ? i : (i < 21 ? i + 14 : i - 7); }
constexpr bool ld_ok() { for (int i = 0; i < 35; ++i) if (ld_iter(item_of(i)) > U0 + 56 + i) return false; for (int i = 1; i < 36; ++i) if (ld_iter(i) < ld_iter(i - 1)) return false; return true; }
static_assert(ld_ok(), "an operand is used before it is requested");

struct Ctx {
  LAS char* sm; u32 m0w;
  rsrc_t rx, rqkv, rwg, rwu, rwd, rwo, rws;
  const char* ws;
  rsrc_t rt; u32 toff;
  u32 vx, vkv, vg, vu, vw2;
};

template <int N> DI void issueA(const Ctx& c) {
  constexpr u32 dst = (N % 20) * SLOT;
  if constexpr (N < 21) {
    constexpr int kc = N / 3, j = N % 3;
    if constexpr (j < 2) dma(c.m0w + dst, c.vx, c.rx, j * 896 + kc * 128);
    else dma(c.m0w + dst, c.vkv, c.rqkv, kc * 64 * 2688);
  } else if constexpr (N < 49) {
    constexpr int kc = ((N - 21) / 2) % 7, ps = (N - 21) / 14;
    if constexpr ((N - 21) % 2 == 0) dma(c.m0w + dst, c.vg, c.rwg, kc * 64 * 3584 + ps * 1792);
    else dma(c.m0w + dst, c.vu, c.rwu, kc * 64 * 3584 + ps * 1792);
  } else if constexpr (blk_of(N - 49) < 28) dma(c.m0w + dst, c.vw2, c.rwd, blk_of(N - 49) * 64 * 896);
  else dma(c.m0w + dst, c.vw2, c.rwo, (blk_of(N - 49) - 28) * 64 * 896);
}

DI void type_a(Ctx& c, int w, int lane, int mt, int role, int R2, int n2, void* out DIAGP) {
  const int r = lane & 31, hi = lane >> 5, blk = (lane >> 4) & 1, q = (lane & 15) >> 2, p = lane & 3;
  const int sp = ((p & 1) << 1) | (p >> 1);
  const int rg = w & 3, cg = w >> 2, T = mt * 4 + rg;
  const bool isv = role >= 7;
  LAS char* sm = c.sm;
  u32x2 ev = ld64_sc1(w == 0 ? WS_E : OOB, c.rws, 0);
  u32 td;
  static_for<0, PRO_DMA>([&](auto N) { issueA<N>(c); });
  const int R = 32 * (rg & 1) + r, xm = (R >> 1) & 7;
  int xa[4];
#pragma unroll
  for (int s = 0; s < 4; ++s) xa[s] = (rg >> 1) * SLOT + R * 128 + (((2 * s + hi) ^ xm) << 4);
  const int rowb = (8 * hi + q) * 128, q2 = q & 2;
  const int akv = rowb + (((2 * cg + blk) ^ q2) << 5) + (isv ? p : sp) * 8;
  int agu[2];
#pragma unroll
  for (int i = 0; i < 2; ++i) agu[i] = blk * SLOT + rowb + (((2 * cg + i) ^ q2 ^ blk) << 5) + sp * 8;
  int aguw[2][3];
#pragma unroll
  for (int i = 0; i < 2; ++i)
#pragma unroll
    for (int k = 0; k < 3; ++k) { aguw[i][k] = agu[i] + k * 65536; pin(aguw[i][k]); }
  const int rt = w & 1, kq = w >> 1;
  int aw2[2], aw2w[2][3];
#pragma unroll
  for (int i = 0; i < 2; ++i) aw2[i] = kq * 2048 + rowb + (((2 * i + blk) ^ q2) << 5) + p * 8;
  const int T2 = (R2 >> 1) * 4 + 2 * rt + (R2 & 1);

  u32x4 xf[28], kf[4], wf[2][2], af[35], hp[2];
  f32x16 akvacc = {}, g1[2] = {}, g2[2] = {}, ah[2] = {}, ao[2] = {};
  u32x2 pollv; u32 omg; u64 tag;
  const u32 lane16 = lane * 16;
  const u32 poll_off = WS_FLAG + (R2 >> 1) * 256 + (lane & 15) * 8;
  const bool need_h = (lane & 15) < 14;
  const u32 a2_off = WS_A2 + T2 * A2_T + kq * 1024 + lane16;
  const u32 h_off = WS_A2 + T * A2_T + (role * 4 + 2 * cg) * 1024 + lane16;

  auto read_kv = [&](auto J, auto S) {
    constexpr int j = J, sx = S;
    xf[4 * j + sx] = *(LAS u32x4*)(sm + xa[sx] + ((3 * j) % 20) * SLOT);
    kf[sx] = trfrag(sm + akv + ((3 * j + 2) % 20) * SLOT + sx * 2048);
  };
  auto read_w = [&](auto U, auto I) {
    constexpr int u = U, i = I;
    if constexpr (u < 56) {
      constexpr int sl = (21 + 2 * (u / 4)) % 20;
      const int wrap = sl == 19 ? blk * (-20 * SLOT) : 0;
      wf[u & 1][i] = trfrag(sm + aguw[i][sl / 8] + wrap + (sl % 8) * SLOT + (u % 4) * 2048);
    } else if constexpr (u < 91) {
      constexpr int sl = (49 + u - 56) % 20;
      if constexpr (u == 56) {
#pragma unroll
        for (int k = 0; k < 3; ++k) { aw2w[i][k] = aw2[i] + k * 65536; pin(aw2w[i][k]); }
      }
      wf[u & 1][i] = trfrag(sm + aw2w[i][sl / 8] + (sl % 8) * SLOT);
    }
  };
  auto publish = [&](int cls) {
    if (w == 0) __builtin_amdgcn_raw_buffer_store_b64(u32x2{(u32)tag, (u32)(tag >> 32)}, c.rws, lane == 0 ? role * 8 : OOB, WS_FLAG + cls * 4096 + mt * 256, SC1);
  };
  auto load_item = [&](auto IT) {
    constexpr int it = IT;
    if constexpr (it == IT_OMG) omg = ld32_sc1(r * 4, c.rws, WS_OMG + T2 * 128);
    else { constexpr int i = it < IT_OMG ? it : it - 1; af[i] = ld128_sc1(a2_off, c.rws, blk_of(i) * 4096); }
  };

  STAMP(1);
  sync_vm<PRO_OPS - 3>();
  STAMP(2);
  static_for<0, 4>([&](auto S) { read_kv(IC<0>{}, S); });
  SCHED();
  auto pass0 = [&](auto ISV) {
    static_for<0, U0>([&](auto G) {
      constexpr int g = G;
      sync_vm<sync_cnt(g)>();
      if constexpr (g == G_TOUCH) td = touch(c.toff, c.rt);
      SCHED();
      static_for<0, 4>([&](auto S) {
        constexpr int sx = S;
        if constexpr (ISV) mfma_at(akvacc, xf[4 * g + sx], kf[sx]); else mfma_at(akvacc, kf[sx], xf[4 * g + sx]);
        SCHED();
        if constexpr (g < 6) read_kv(IC<g + 1>{}, S); else read_w(IC<sx / 2>{}, IC<sx % 2>{});
        if constexpr (cum(g - 1) + sx < cum(g)) issueA<cum(g - 1) + sx>(c);
        SCHED();
      });
    });
  };
  static_assert(P0_RATE <= 4, "");
  if (isv) pass0(IC<1>{}); else pass0(IC<0>{});
  auto check = [&](auto CNT, int cls, bool need) {
    wait_vm<CNT>();
    pin(pollv);
    if (w == 0 && !match(pollv, need, tag)) spin(c.ws, poll_off + cls * 4096, need, tag);
  };
  static_for<U0, NG>([&](auto G) {
    constexpr int g = G, u = g - U0, b = u & 1;
    if constexpr (u == 0) STAMP(3);
    if constexpr (u == 28) STAMP(4);
    if constexpr (u == 56) STAMP(5);
    if constexpr (u == 70) STAMP(8);
    if constexpr (u == 77) STAMP(9);
    if constexpr (u == U_CHK2) STAMP2(56);
    if constexpr (u == U_CHK3) STAMP2(58);
    if constexpr (is_sync(g)) STAMP2(16 + 2 * (u < 56 ? u / 8 : 7 + (u - 59) / 5));
    if constexpr (u == U_KVPUB) STAMP2(50);
    if constexpr (u == U_H1PUB) STAMP2(52);
    if constexpr (u == U_H2PUB) STAMP2(54);
    if constexpr (u == U_CHK1 && u != 56) check(IC<base(g) - base(U0 + U_POLL1) - 1>{}, 1, need_h);
    if constexpr (u == U_CHK2) check(IC<base(g) - base(U0 + U_POLL2) - 1>{}, 2, ORDER ? need_h : !need_h);
    if constexpr (u == U_CHK3) check(IC<base(g) - base(U0 + U_POLL3) - 1>{}, 2, ORDER ? !need_h : need_h);
    if constexpr (is_bar(g) && u != 56) sync_vm<sync_cnt(g)>();
    if constexpr (is_sync(g)) STAMP2(17 + 2 * (u < 56 ? u / 8 : 7 + (u - 59) / 5));
    if constexpr (u == U_CHK2) STAMP2(57);
    if constexpr (u == U_CHK3) STAMP2(59);
    if constexpr (u == U_KVPUB) { pin(ev); tag = tag_of(ev); publish(0); STAMP2(51); }
    if constexpr (u == U_H1PUB) { publish(1); STAMP2(53); }
    if constexpr (u == U_H2PUB) { publish(2); STAMP2(55); }
    if constexpr (g == G_TOUCH) td = touch(c.toff, c.rt);
    if constexpr (u == U_H1PUB) pin(td);
    if constexpr (u == U_POLL1) pollv = ld64_sc1(w == 0 ? poll_off : OOB, c.rws, 4096);
    if constexpr (u == U_POLL2 || u == U_POLL3) pollv = ld64_sc1(w == 0 ? poll_off : OOB, c.rws, 8192);
    if constexpr (u == U_KVST) {
#pragma unroll
      for (int s = 0; s < 2; ++s) {
        const u32 off = isv ? WS_V + T * KV_T + (s * 2 + hi) * 7168 + ((role - 7) * 64 + cg * 32 + r) * 16
                            : WS_K + T * KV_T + (role * 8 + cg * 4 + 2 * s) * 512 + lane16;
        st128(pk8(akvacc, s), c.rws, off, 0);
      }
    }
    if constexpr (u == U_H1STA) st128(hp[0], c.rws, h_off, 0);
    if constexpr (u == U_H1STB) st128(hp[1], c.rws, h_off, 1024);
    if constexpr (u == U_H2ST) {
#pragma unroll
      for (int t = 0; t < 2; ++t) {
#pragma unroll
        for (int j = 0; j < 8; j += 2) hp[t][j / 2] = pk(glu(g2[t][j], g2[t][8 + j]), glu(g2[t][j + 1], g2[t][9 + j]));
        st128(hp[t], c.rws, h_off, (56 + t) * 1024);
        SCHED();
      }
    }
    asm volatile("" ::: "memory");
    if constexpr (u == 56) STAMP2(44);
    if constexpr (u == 56 && U_CHK1 == 56) {
      check(IC<base(g) + n_st(g) - base(U0 + U_POLL1) - 1>{}, 1, need_h);
      asm volatile("s_barrier" ::: "memory");
      STAMP2(45);
    }
    constexpr bool ld_first = u >= 56 && ld_iter(item_of(u < 56 ? 0 : u - 56)) == g;
    if constexpr (ld_first) static_for<ld_lo(g), ld_lo(g + 1)>(load_item);
    if constexpr (u >= 56) {
      wait_vm<base(g) + n_st(g) + (ld_first ? n_ld(g) : 0) - pos_ld(item_of(u - 56)) - 1>();
      pin(af[u - 56]);
      if constexpr (u == 56) STAMP2(46);
      if constexpr (u == 70) STAMP2(47);
      if constexpr (u == 77) STAMP2(48);
    }
    SCHED();
    static_for<0, 2>([&](auto I) {
      constexpr int i = I;
      if constexpr (u < 28) mfma_at(g1[i], wf[b][i], xf[u]);
      else if constexpr (u < 56) mfma_at(g2[i], wf[b][i], xf[u - 28]);
      else if constexpr (blk_of(u - 56) < 28) mfma_at(ah[i], wf[b][i], af[u - 56]);
      else mfma_at(ao[i], wf[b][i], af[u - 56]);
      SCHED();
      read_w(IC<u + 2>{}, I);
      if constexpr (u >= 28 && u < 28 + 2 * EP1) {
        constexpr int t = (u - 28) / EP1, per = 4 / EP1, k0 = ((u - 28) % EP1) * per;
#pragma unroll
        for (int k = k0 + (i * per + 1) / 2; k < k0 + ((i + 1) * per + 1) / 2; ++k) {
          u32 hv = pk(glu(g1[t][2 * k], g1[t][8 + 2 * k]), glu(g1[t][2 * k + 1], g1[t][9 + 2 * k]));
          pin(hv);
          hp[t][k] = hv;
        }
      }
      if constexpr (i == 0 && !ld_first) static_for<ld_lo(g), ld_lo(g + 1)>(load_item);
      if constexpr (i == 1) static_for<cum(g - 1), cum(g)>([&](auto N) { issueA<N>(c); });
      SCHED();
    });
  });
  pin(omg);
  STAMP(6);
  const float om = __uint_as_float(omg);
  LAS char* red = sm + 4 * SLOT;
#pragma unroll
  for (int t = 0; t < 2; ++t)
#pragma unroll
    for (int a = 0; a < 4; ++a) {
      f32x4 v;
#pragma unroll
      for (int e = 0; e < 4; ++e) v[e] = ah[t][4 * a + e] * om + ao[t][4 * a + e];
      *(LAS f32x4*)(red + kq * 16384 + (2 * r + rt) * 256 + (((8 * t + 2 * a + hi) ^ (r & 15)) << 4)) = v;
    }
  asm volatile("s_waitcnt lgkmcnt(0)\n\ts_barrier" ::: "memory");
  STAMP(7);
  {
    const int row = w * 8 + (lane >> 3), c8 = lane & 7, m = (row >> 1) & 15;
    f32x4 s0 = {}, s1 = {};
#pragma unroll
    for (int k = 0; k < 4; ++k) {
      s0 += *(LAS f32x4*)(red + k * 16384 + row * 256 + (((2 * c8) ^ m) << 4));
      s1 += *(LAS f32x4*)(red + k * 16384 + row * 256 + (((2 * c8 + 1) ^ m) << 4));
    }
    u32x4 o = {pk(s0[0], s0[1]), pk(s0[2], s0[3]), pk(s1[0], s1[1]), pk(s1[2], s1[3])};
    *(u32x4*)((char*)out + (size_t)(R2 * 64 + row) * 896 + n2 * 128 + c8 * 16) = o;
  }
}
static_assert(freed(U0 + 90) >= 72, "the reduction scratch (slots 4..11 = sequence 64..71) must be free at the end");

constexpr int PRO_B = 12;
constexpr int freedB(int g) { return 6 * ((g + 1) / 4); }
constexpr int cumB(int g) {
  int cc = PRO_B;
  for (int j = 0; j <= g; ++j) { int t = cc + 2, cap = freedB(j) + 18; t = t < cap ? t : cap; cc = t < 42 ? t : 42; }
  return cc;
}
#define GB_POLL 20
#define GB_CHK 23
constexpr int GB_TOUCH = 3;
#define KV_RATE 3
constexpr int n_stB(int g) { return (g == GB_POLL); }
constexpr int baseB(int g) { int t = PRO_B; for (int j = 0; j < g; ++j) t += n_stB(j) + cumB(j) - cumB(j - 1); return t; }
constexpr int pos_dmaB(int n) {
  if (n < PRO_B) return n;
  for (int g = 0; g < 28; ++g) if (n < cumB(g)) return baseB(g) + n_stB(g) + (n - cumB(g - 1));
  return -1000;
}
constexpr int sync_cntB(int g) {
  int n = baseB(g) - pos_dmaB(6 * (g / 4 + 2) - 1) - 1 + (g > GB_CHK ? imin(22, (g - GB_CHK) * KV_RATE) : 0);
  return n;
}
template <int N> DI void issueB(const Ctx& c) {
  constexpr int kc = N / 6, j = N % 6;
  constexpr u32 dst = (N % 18) * SLOT;
  if constexpr (j < 2) dma(c.m0w + dst, c.vx, c.rx, j * 896 + kc * 128);
  else dma(c.m0w + dst, c.vkv, c.rqkv, kc * 64 * 2688 + (j - 2) * 128);
}

DI void type_b(Ctx& c, int w, int lane, int mt, int hf, const void* gw DIAGP) {
  const int r = lane & 31, hi = lane >> 5, blk = (lane >> 4) & 1, q = (lane & 15) >> 2, p = lane & 3;
  const int sp = ((p & 1) << 1) | (p >> 1);
  const int rg = w & 3, cg = w >> 2, T = mt * 4 + rg;
  LAS char* sm = c.sm;
  if (w == 0) dma(c.m0w + 18 * SLOT, lane * 16, mk(gw, 896), 0);
  u32x2 ev = ld64_sc1(w == 0 ? WS_E : OOB, c.rws, 0);
  static_for<0, PRO_B>([&](auto N) { issueB<N>(c); });
  const int R = 32 * (rg & 1) + r, xm = (R >> 1) & 7;
  int xa[4], aq[4];
#pragma unroll
  for (int s = 0; s < 4; ++s) xa[s] = (rg >> 1) * SLOT + R * 128 + (((2 * s + hi) ^ xm) << 4);
  const int h0 = 8 * hf + 4 * cg;
#pragma unroll
  for (int i = 0; i < 4; ++i) {
    const int col = 28 * (h0 + i) - 4 * (i & 1) - 192 * hf + 16 * blk + 4 * sp;
    aq[i] = (2 + (col >> 6)) * SLOT + (8 * hi + q) * 128 + (((col & 63) * 2) ^ ((q & 2) << 5));
  }
  const bool first = (mt == 0) && !(rg & 1);
  const int Tp = first ? T : ((rg & 1) ? T - 1 : T - 3);
  const u32 lane16 = lane * 16, lo16 = hi ? OOB : lane16;
  const u32 ob = WS_A2 + T * A2_T + (1792 + 28 * h0) * 64;
  const u32 omg_off = (hf == 0 && cg == 0 && hi == 0) ? r * 4 : OOB;
  const u32 poll_off = WS_FLAG + ((lane & 16) && mt > 0 ? mt - 1 : mt) * 256 + (lane & 15) * 8;
  const bool poll_need = (lane & 15) < 14;
  u32x4 xf[2], gf[2], wf[2][4];
  u32x4 kf[4][2], kp[2], vf[4][2], vp[4];
  f32x16 qa[4] = {};
  u32x2 pollv;
  const int sr = (r & 19) | ((r & 4) << 1) | ((r & 8) >> 1);
  const u32 kp_off = (28 * (r >> 3) - 4 * ((r >> 3) & 1)) * 64 + (hi * 32 + 24 + (r & 7)) * 16;
  const u32 v_off = hi * 7168 + sr * 16;
  auto load_kv = [&](auto IT) {
    constexpr int it = IT;
    if constexpr (it < 2) kp[it] = ld128_sc1(kp_off, c.rws, WS_K + Tp * KV_T + 28 * h0 * 64 + it * 1024);
    else if constexpr (it < 10) {
      constexpr int i = (it - 2) / 2, k = it & 1;
      kf[i][k] = ld128_sc1(lane16, c.rws, WS_K + T * KV_T + (28 * (h0 + i) - 4 * (i & 1)) * 64 + k * 1024);
    } else {
      constexpr int i = (it - 10) / 3, k = (it - 10) % 3;
      const int wh = 28 * (h0 + i) - 4 * (i & 1);
      if constexpr (k == 0) vp[i] = ld128_sc1(v_off, c.rws, WS_V + Tp * KV_T + 14336 + wh * 16);
      else vf[i][k - 1] = ld128_sc1(v_off, c.rws, WS_V + T * KV_T + (k - 1) * 14336 + wh * 16);
    }
  };
  auto read_x = [&](auto G) {
    constexpr int g = G;
    if constexpr (g < 28) {
      xf[g & 1] = *(LAS u32x4*)(sm + xa[g % 4] + ((6 * (g / 4)) % 18) * SLOT);
      gf[g & 1] = *(LAS u32x4*)(sm + 18 * SLOT + g * 32 + hi * 16);
    }
  };
  auto read_w = [&](auto G, auto I) {
    constexpr int g = G;
    if constexpr (g < 28) wf[g & 1][I] = trfrag(sm + aq[I] + ((6 * (g / 4)) % 18) * SLOT + (g % 4) * 2048);
  };
  STAMP(1);
  sync_vm<PRO_B - 6>();
  STAMP(2);
  u64 tag;
  read_x(IC<0>{});
  static_for<0, 4>([&](auto I) { read_w(IC<0>{}, I); });
  SCHED();
  static_for<0, 28>([&](auto G) {
    constexpr int g = G, b = g & 1;
    if constexpr (g == GB_CHK) {
      STAMP(8);
      wait_vm<baseB(g) - baseB(GB_POLL) - 1>();
      pin(pollv); pin(ev);
      tag = tag_of(ev);
      if (w == 0 && !match(pollv, poll_need, tag)) spin(c.ws, poll_off, poll_need, tag);
    }
    if constexpr (g % 4 == 3 && g < 27) { STAMP2(16 + 2 * (g / 4)); asm volatile("s_waitcnt lgkmcnt(0)" ::: "memory"); sync_vm<sync_cntB(g)>(); STAMP2(17 + 2 * (g / 4)); }
    if constexpr (g == GB_CHK) STAMP(9);
    if constexpr (g == GB_POLL) pollv = ld64_sc1(w == 0 ? poll_off : OOB, c.rws, 0);
    SCHED();
    static_for<0, 4>([&](auto I) {
      constexpr int i = I;
      mfma_at(qa[i], wf[b][i], xf[b]);
      SCHED();
      if constexpr (i == 0) read_x(IC<g + 1>{});
      read_w(IC<g + 1>{}, I);
      if constexpr (i < 2) {
        wf[b][2][2 * i] = r == 28 ? gf[b][2 * i] : wf[b][2][2 * i];
        wf[b][2][2 * i + 1] = r == 28 ? gf[b][2 * i + 1] : wf[b][2][2 * i + 1];
        pin(wf[b][2]);
      }
      if constexpr (i >= 2 && cumB(g - 1) + i - 2 < cumB(g)) issueB<cumB(g - 1) + i - 2>(c);
      if constexpr (g >= GB_CHK && i < KV_RATE) if constexpr ((g - GB_CHK) * KV_RATE + i < 22) load_kv(IC<(g - GB_CHK) * KV_RATE + i>{});
      SCHED();
    });
  });
  static_assert(cumB(GB_CHK - 1) == 42, "no DMA after the first K/V load");
  static_for<(28 - GB_CHK) * KV_RATE, 22>(load_kv);
  STAMP(3);
  const float z = __uint_as_float(__builtin_amdgcn_permlane32_swap(__float_as_uint(qa[2][12]), __float_as_uint(qa[2][12]), false, false)[1]);
  const float gate = __builtin_amdgcn_rcpf(1.f + __builtin_amdgcn_exp2f(z * -1.4426950408889634f));
  const u32 omg = __float_as_uint(__builtin_amdgcn_rcpf(1.f + __builtin_amdgcn_exp2f(z * 1.4426950408889634f)));
  int ab[4];
  u32 msk[5][2];
#pragma unroll
  for (int b = 0; b < 4; ++b) ab[b] = (r >> 3) - ((r & 7) < 4 * hi + b ? 1 : 0);
#pragma unroll
  for (int a = 0; a < 5; ++a)
#pragma unroll
    for (int e = 0; e < 2; ++e) msk[a][e] = (ab[2 * e] == a - 1 ? 0xFFFFu : 0u) | (ab[2 * e + 1] == a - 1 ? 0xFFFF0000u : 0u);
  const float csc = 0.18898223650461362f * 1.4426950408889634f;
  __builtin_amdgcn_raw_buffer_store_b32(omg, c.rws, omg_off, WS_OMG + T * 128, SC1);
  asm volatile("" ::: "memory");
  u32 p01[4], p23[4];
  wait_vm<13>();
  STAMP(4);
  pin(kp[0]); pin(kp[1]);
  static_for<0, 4>([&](auto I) { pin(kf[I][0]); pin(kf[I][1]); });
  static_for<0, 4>([&](auto I) {
    constexpr int i = I;
    u32x4 qf[2] = {pk8(qa[i], 0), pk8(qa[i], 1)};
    if (i & 1) { qf[0][0] = hi ? qf[0][0] : 0u; qf[0][1] = hi ? qf[0][1] : 0u; }
    else       { qf[1][2] = hi ? 0u : qf[1][2]; qf[1][3] = hi ? 0u : qf[1][3]; }
    f32x16 so = {}, sv = {};
    so = mfma(kf[i][0], qf[0], so); sv = mfma(kp[0], qf[0], sv);
    so = mfma(kf[i][1], qf[1], so); sv = mfma(kp[1], qf[1], sv);
    float v[4];
#pragma unroll
    for (int b = 0; b < 4; ++b) {
      float t = first ? -1e30f : sv[4 * i + b];
#pragma unroll
      for (int a = 0; a < 4; ++a) t = ab[b] == a ? so[4 * a + b] : t;
      v[b] = t;
    }
    const float mx = xhalf_max(fmaxf(fmaxf(v[0], v[1]), fmaxf(v[2], v[3])));
#pragma unroll
    for (int b = 0; b < 4; ++b) v[b] = __builtin_amdgcn_exp2f((v[b] - mx) * csc);
    const float sc = gate * __builtin_amdgcn_rcpf(xhalf_sum((v[0] + v[1]) + (v[2] + v[3])));
    p01[i] = pk(v[0] * sc, v[1] * sc); p23[i] = pk(v[2] * sc, v[3] * sc);
  });
  static_for<0, 4>([&](auto I) { pin(p01[I]); pin(p23[I]); });
  STAMP(10);
  static_for<0, 2>([&](auto J) {
    constexpr int j = J;
    wait_vm<(j == 0 ? 7 : 5)>();
    u32x4 op[2][2];
#pragma unroll
    for (int e = 0; e < 2; ++e) {
      const int i = 2 * j + e;
      pin(vp[i]); pin(vf[i][0]); pin(vf[i][1]);
      f32x16 oa = {};
      oa = mfma(vp[i], u32x4{0u, 0u, p01[i] & msk[0][0], p23[i] & msk[0][1]}, oa);
      oa = mfma(vf[i][0], u32x4{p01[i] & msk[1][0], p23[i] & msk[1][1], p01[i] & msk[2][0], p23[i] & msk[2][1]}, oa);
      oa = mfma(vf[i][1], u32x4{p01[i] & msk[3][0], p23[i] & msk[3][1], p01[i] & msk[4][0], p23[i] & msk[4][1]}, oa);
      op[e][0] = pk8(oa, 0); op[e][1] = pk8(oa, 1);
    }
#pragma unroll
    for (int e = 0; e < 2; ++e) { auto t = __builtin_amdgcn_permlane32_swap(op[0][1][e], op[1][0][e], false, false); op[0][1][e] = t[0]; op[1][0][e] = t[1]; }
    const u32 o2 = ob + j * 3584;
    st128(op[0][0], c.rws, lane16, o2);
    st128(op[0][1], c.rws, lo16, o2 + 1024);
    st128(op[1][0], c.rws, lane16, o2 + 1536);
    st128(op[1][1], c.rws, lane16, o2 + 2560);
  });
  STAMP(5);
  sync_vm<0>();
  if (w == 0) __builtin_amdgcn_raw_buffer_store_b64(u32x2{(u32)tag, (u32)(tag >> 32)}, c.rws, lane == 0 ? (14 + hf) * 8 : OOB, WS_FLAG + 8192 + mt * 256, SC1);
  STAMP(6);
  if (mt == 0 && hf == 0 && w == 0) {
#pragma unroll 1
    for (int i = 0; i < 4; ++i) spin(c.ws, WS_FLAG + 8192 + (i * 4 + (lane >> 4)) * 256 + (lane & 15) * 8, true, tag);
    __builtin_amdgcn_raw_buffer_store_b64(u32x2{(u32)tag, (u32)(tag >> 32)}, c.rws, lane == 0 ? 0 : OOB, WS_E, SC1);
  }
}

__global__ __launch_bounds__(512, 2) void fused_kernel(const void* __restrict__ x, const void* __restrict__ wqkv, const void* __restrict__ wo,
                                                       const void* __restrict__ wg, const void* __restrict__ wu, const void* __restrict__ wd,
                                                       const void* __restrict__ gw, void* __restrict__ out, void* __restrict__ ws) {
  __shared__ __attribute__((aligned(1024))) char smem[163840];
  const int lane = threadIdx.x & 63, w = __builtin_amdgcn_readfirstlane(threadIdx.x >> 6);
  const int b = blockIdx.x, xc = b & 7, ix = b >> 3;
  const int mq = xc >> 1, mt = mq * 4 + (ix & 3), rl = ix >> 2, rh = xc & 1;
  Ctx c;
  c.sm = (LAS char*)smem;
  c.m0w = (u32)(size_t)c.sm + w * 1024;
  c.rx = mk(x, 2048 * 896); c.rqkv = mk(wqkv, 448 * 2688); c.rwg = mk(wg, 448 * 3584); c.rwu = mk(wu, 448 * 3584);
  c.rwd = mk(wd, 1792 * 896); c.rwo = mk(wo, 448 * 896); c.rws = mk(ws, WS_END);
  c.ws = (const char*)ws;
  {
#define TOUCHSET 0
    const int i = ix * 56 + lane, row = mq * 112 + i / 14, l = i % 14;
    const u32 o12 = row * 3584 + (rh * 7 + (l < 7 ? l : l + 7)) * 128;
    const u32 o34 = ((w == 3 ? b * 56 : b * 14) + lane) * 128;
    const int n = w == 0 ? 0 : (w < 3 ? (TOUCHSET ? 56 : 0) : (w == 3 ? 56 : (w == 4 ? 14 : 0)));
    c.toff = lane < n ? (w < 3 ? o12 : o34) : OOB;
    c.rt = w == 1 ? c.rwg : (w == 2 ? c.rwu : (w == 3 ? c.rwd : c.rwo));
  }
  const int r8 = lane >> 3, c8 = lane & 7, R = 8 * w + r8;
  c.vx = (mt * 128 + 2 * R) * 896 + ((c8 ^ ((R >> 1) & 7)) << 4);
  const int sw = (c8 ^ ((r8 & 2) << 1)) << 4;
  if (rl < 7) {
    const int role = rh * 7 + rl;
    c.vkv = R * 2688 + 896 + role * 128 + sw;
    c.vg = R * 3584 + role * 128 + sw;
    c.vu = R * 3584 + role * 128 + (sw ^ 32);
    c.vw2 = R * 896 + rl * 128 + sw;
    type_a(c, w, lane, mt, role, xc * 4 + (ix & 3), rl, out DIAGA);
  } else {
    c.vkv = R * 2688 + rh * 384 + sw;
    type_b(c, w, lane, mt, rh, gw DIAGA);
  }
}

extern "C" void kernel_launch(void* const* d_in, const int* in_sizes, int n_in, void* d_out, int out_size, void* d_ws, size_t ws_size,
                              hipStream_t stream) {
  fused_kernel<<<256, 512, 0, stream>>>(d_in[0], d_in[1], d_in[2], d_in[4], d_in[5], d_in[6], d_in[7], d_out, d_ws);
}
```

```cpp
#define CFG 16,3,-2,8,40,57,52,56,64,68,70,76,2,6,6,2,6,6,2,0,1,2,16,16,0
#include <hip/hip_runtime.h>
#include <hip/hip_bf16.h>

typedef unsigned long long u64;
typedef unsigned u32;
typedef __bf16 bf16x8 __attribute__((ext_vector_type(8)));
typedef __bf16 bf16x2 __attribute__((ext_vector_type(2)));
typedef short s16x4 __attribute__((ext_vector_type(4)));
typedef u32 u32x4 __attribute__((ext_vector_type(4)));
typedef u32 u32x2 __attribute__((ext_vector_type(2)));
typedef float f32x16 __attribute__((ext_vector_type(16)));
typedef float f32x4 __attribute__((ext_vector_type(4)));
typedef __amdgpu_buffer_rsrc_t rsrc_t;
#define LAS __attribute__((address_space(3)))
#define GAS __attribute__((address_space(1)))
#define DI __device__ __forceinline__
#define SCHED() __builtin_amdgcn_sched_barrier(0)
template <int V> using IC = std::integral_constant<int, V>;

constexpr u32 WS_E = 0;
constexpr u32 WS_FLAG = 4096;
constexpr u32 WS_OMG = 32768;
constexpr u32 WS_K = 65536;
constexpr u32 KV_T = 28672;
constexpr u32 WS_V = WS_K + 64 * KV_T;
constexpr u32 WS_A2 = WS_V + 64 * KV_T;
constexpr u32 A2_T = 143360;
constexpr u32 WS_END = WS_A2 + 64 * A2_T;
constexpr u32 OOB = 0xFFFFFF00u;
constexpr int SC1 = 16;
constexpr int SLOT = 8192;
constexpr int SPIN_CAP = 1 << 15;

DI rsrc_t mk(const void* p, u32 bytes) { return __builtin_amdgcn_make_buffer_rsrc((void*)p, (short)0, (int)bytes, 0x00020000); }
DI void dma(u32 m0v, u32 voff, rsrc_t rs, u32 soff) {
  asm volatile("s_mov_b32 m0, %0\n\ts_nop 0\n\tbuffer_load_dwordx4 %1, %2, %3 offen lds" ::"s"(m0v), "v"(voff), "s"(rs), "s"(soff) : "memory");
}
DI u32x4 ld128_sc1(u32 voff, rsrc_t rs, u32 soff) {
  u32x4 v;
  asm volatile("buffer_load_dwordx4 %0, %1, %2, %3 offen sc1" : "=v"(v) : "v"(voff), "s"(rs), "s"(soff) : "memory");
  return v;
}
DI u32x2 ld64_sc1(u32 voff, rsrc_t rs, u32 soff) {
  u32x2 v;
  asm volatile("buffer_load_dwordx2 %0, %1, %2, %3 offen sc1" : "=v"(v) : "v"(voff), "s"(rs), "s"(soff) : "memory");
  return v;
}
DI u32 ld32_sc1(u32 voff, rsrc_t rs, u32 soff) {
  u32 v;
  asm volatile("buffer_load_dword %0, %1, %2, %3 offen sc1" : "=v"(v) : "v"(voff), "s"(rs), "s"(soff) : "memory");
  return v;
}
DI u32 touch(u32 voff, rsrc_t rs) {
  u32 v;
  asm volatile("buffer_load_dword %0, %1, %2, 0 offen" : "=v"(v) : "v"(voff), "s"(rs) : "memory");
  return v;
}
DI void st128(u32x4 v, rsrc_t rs, u32 voff, u32 soff) {
  asm volatile("buffer_store_dwordx4 %0, %1, %2, %3 offen sc1\n\ts_nop 1" ::"v"(v), "v"(voff), "s"(rs), "s"(soff) : "memory");
}
constexpr int clampvm(int n) { return n > 63 ? 63 : n; }
template <int N> DI void wait_vm() { static_assert(N >= 0, ""); asm volatile("s_waitcnt vmcnt(%0)" ::"n"(clampvm(N)) : "memory"); }
template <class T> DI void pin(T& v) { asm volatile("" : "+v"(v)); }

DI u32x4 trfrag(LAS char* p) {
  s16x4 lo = __builtin_amdgcn_ds_read_tr16_b64_v4i16((LAS s16x4*)p);
  s16x4 hi = __builtin_amdgcn_ds_read_tr16_b64_v4i16((LAS s16x4*)(p + 512));
  u32x2 a = __builtin_bit_cast(u32x2, lo), b = __builtin_bit_cast(u32x2, hi);
  return u32x4{a.x, a.y, b.x, b.y};
}
DI f32x16 mfma(u32x4 a, u32x4 b, f32x16 c) {
  return __builtin_amdgcn_mfma_f32_32x32x16_bf16(__builtin_bit_cast(bf16x8, a), __builtin_bit_cast(bf16x8, b), c, 0, 0, 0);
}
DI void mfma_at(f32x16& c, u32x4 a, u32x4 b) { c = mfma(a, b, c); asm volatile("" : "+v"(c)); }
DI u32 pk(float a, float b) { bf16x2 c; c[0] = (__bf16)a; c[1] = (__bf16)b; return __builtin_bit_cast(u32, c); }
DI u32x4 pk8(const f32x16& v, int s) { return u32x4{pk(v[8 * s], v[8 * s + 1]), pk(v[8 * s + 2], v[8 * s + 3]), pk(v[8 * s + 4], v[8 * s + 5]), pk(v[8 * s + 6], v[8 * s + 7])}; }
DI float glu(float g, float u) {
  return u * __builtin_amdgcn_rcpf(1.f + __builtin_amdgcn_exp2f(g * -1.4426950408889634f)); }
DI float xhalf_max(float v) { auto r = __builtin_amdgcn_permlane32_swap(__float_as_uint(v), __float_as_uint(v), false, false); return fmaxf(__uint_as_float(r[0]), __uint_as_float(r[1])); }
DI float xhalf_sum(float v) { auto r = __builtin_amdgcn_permlane32_swap(__float_as_uint(v), __float_as_uint(v), false, false); return __uint_as_float(r[0]) + __uint_as_float(r[1]); }

DI void spin(const char* ws, u32 off, bool need, u64 tag) {
  const GAS u64* p = (const GAS u64*)(ws + off);
#pragma unroll 1
  for (int i = 0; i < SPIN_CAP; ++i) {
    u64 v = __hip_atomic_load(p, __ATOMIC_RELAXED, __HIP_MEMORY_SCOPE_AGENT);
    if (__all(!need || v == tag)) break;
    __builtin_amdgcn_s_sleep(2);
  }
  asm volatile("" ::: "memory");
}
DI bool match(u32x2 v, bool need, u64 tag) {
  return __all(!need || (v.x == (u32)tag && v.y == (u32)(tag >> 32))); }
DI u64 tag_of(u32x2 ev) {
  u64 e = ((u64)__builtin_amdgcn_readfirstlane(ev.y) << 32) | (u32)__builtin_amdgcn_readfirstlane(ev.x);
  return e * 6364136223846793005ull + 1442695040888963407ull;
}

#define STAMP(i)
#define DIAGP
#define DIAGA
#define STAMP2(i)
template <int N> DI void sync_vm() { static_assert(N >= 0, ""); asm volatile("s_waitcnt vmcnt(%0)\n\ts_barrier" ::"n"(clampvm(N)) : "memory"); }

template <int I, int N, class Fn> DI void static_for(Fn&& f) {
  if constexpr (I < N) { f(IC<I>{}); static_for<I + 1, N>(f); }
}

#define DEFP(n, v) constexpr int n = v
constexpr int kCfg[] = {CFG};
constexpr int PRO_DMA = kCfg[0], P0_RATE = kCfg[1], G_TOUCH = kCfg[2], U_KVPUB = kCfg[3], U_H1PUB = kCfg[4], U_H2PUB = kCfg[5];
constexpr int U_POLL1 = kCfg[6], U_CHK1 = kCfg[7], U_POLL2 = kCfg[8], U_CHK2 = kCfg[9], U_POLL3 = kCfg[10], U_CHK3 = kCfg[11];
constexpr int EP1 = kCfg[12];
constexpr int ld_rel(int cl, int k) { int f = kCfg[13 + 3 * cl], a = kCfg[14 + 3 * cl], b = kCfg[15 + 3 * cl]; return k < f ? k / a : (f + a - 1) / a + (k - f) / b; }
#define ORDER 1
constexpr int N2 = ORDER ? 14 : 8, IT_OMG = ORDER ? 28 : 14;
constexpr int NG = 98, U0 = 7;
constexpr int U_KVST = 0, U_H1STA = 28 + EP1, U_H1STB = 28 + 2 * EP1, U_H2ST = 56;
constexpr int PRO_OPS = PRO_DMA + 1;
constexpr bool step_start(int u) { return u < 56 ? (u % 8 == 0) : (u < 91 && (u - 56) % 5 == 0); }
constexpr bool is_sync(int g) { return g < U0 ? true : step_start(g - U0 + 2); }
constexpr bool is_bar(int g) { int u = g - U0; return is_sync(g) || u == U_KVPUB || u == U_H1PUB || u == U_H2PUB || u == U_CHK1 || u == U_CHK2 || u == U_CHK3; }
constexpr int last_seq_of_step(int u0) { return u0 < 56 ? 21 + (u0 / 4 + 2) * 2 - 1 : 49 + (u0 - 56) + 4; }
constexpr int need_seq(int g) { return g < 6 ? 3 * (g + 1) + 2 : (g == 6 ? last_seq_of_step(0) : last_seq_of_step(g - U0 + 2)); }
constexpr int freed_at(int g) { return g < U0 ? 3 * g : (g - U0 <= 56 ? 21 + 2 * ((g - U0) / 4) : 49 + (g - U0 - 56)); }
constexpr int freed(int g) { for (int j = g; j >= 0; --j) if (is_bar(j)) return freed_at(j); return 0; }
constexpr int n_pay(int g) { int u = g - U0; return 2 * (u == U_KVST) + (u == U_H1STA) + (u == U_H1STB) + 2 * (u == U_H2ST); }
constexpr int cum(int g) {
  int cc = PRO_DMA, cr = 0;
  for (int j = 0; j <= g; ++j) {
    int k = P0_RATE;
    if (j >= U0) { cr += (j - U0 < 56 ? kCfg[22] : kCfg[23]) - kCfg[24] * n_pay(j); k = cr >= 16 ? 1 : 0; }
    int t = cc + k, cap = freed(j) + 20; t = t < cap ? t : cap; t = t < 84 ? t : 84;
    if (j >= U0) { cr -= 16 * (t - cc); if (cr > 16) cr = 16; }
    cc = t;
  }
  return cc;
}
constexpr int n_dma(int g) { return cum(g) - cum(g - 1); }
constexpr bool sched_ok() { for (int g = 0; g < NG; ++g) if (is_sync(g) && need_seq(g) >= cum(g - 1)) return false; return true; }
static_assert(sched_ok(), "a slot is needed before it is requested");
constexpr int n_st(int g) { int u = g - U0; return (g == G_TOUCH) + (u == U_POLL1) + (u == U_POLL2) + (u == U_POLL3) + 2 * (u == U_KVST) + (u == U_H1STA) + (u == U_H1STB) + 2 * (u == U_H2ST); }
constexpr int ld_iter(int it) { return U0 + (it < 14 ? U_CHK1 + ld_rel(0, it) : (it < 14 + N2 ? U_CHK2 + ld_rel(1, it - 14) : U_CHK3 + ld_rel(2, it - 14 - N2))); }
constexpr int ld_lo(int g) { for (int it = 0; it < 36; ++it) if (ld_iter(it) >= g) return it; return 36; }
constexpr int n_ld(int g) { return ld_lo(g + 1) - ld_lo(g); }
constexpr int ops_in(int g) { return n_st(g) + n_ld(g) + n_dma(g); }
constexpr int base(int g) { int t = PRO_OPS; for (int j = 0; j < g; ++j) t += ops_in(j); return t; }
constexpr int pos_st_last(int g) { return base(g) + n_st(g) - 1; }
constexpr int pos_ld(int it) { return base(ld_iter(it)) + n_st(ld_iter(it)) + (it - ld_lo(ld_iter(it))); }
constexpr int pos_dma(int n) {
  if (n < PRO_DMA) return n;
  for (int g = 0; g < NG; ++g)
    if (n < cum(g)) return base(g) + n_st(g) + n_ld(g) + (n - cum(g - 1));
  return -1000;
}
constexpr int imin(int a, int b) { return a < b ? a : b; }
constexpr int sync_cnt(int g) {
  int n = is_sync(g) ? base(g) - pos_dma(need_seq(g)) - 1 : 63;
  if (g == U0 + U_KVPUB) n = imin(n, base(g) - pos_st_last(U0 + U_KVST) - 1);
  if (g == U0 + U_H1PUB) n = imin(n, base(g) - pos_st_last(U0 + U_H1STB) - 1);
  if (g == U0 + U_H2PUB) n = imin(n, base(g) - pos_st_last(U0 + U_H2ST) - 1);
  return n;
}
static_assert(U_POLL1 < U_CHK1 && U_CHK1 < U_POLL2 && U_POLL2 < U_CHK2 && U_CHK2 < U_POLL3 && U_POLL3 < U_CHK3, "");
static_assert(U_KVPUB > U_KVST && U_H1PUB > U_H1STB && U_H2PUB > U_H2ST && U_H1PUB < U_CHK1 && U_H2PUB < U_CHK2, "a workgroup publishes before it waits");
constexpr int item_of(int i) { return i < IT_OMG ? i : i + 1; }
constexpr int blk_of(int i) { return ORDER || i < 14 ? i : (i < 21 ? i + 14 : i - 7); }
constexpr bool ld_ok() { for (int i = 0; i < 35; ++i) if (ld_iter(item_of(i)) > U0 + 56 + i) return false; for (int i = 1; i < 36; ++i) if (ld_iter(i) < ld_iter(i - 1)) return false; return true; }
static_assert(ld_ok(), "an operand is used before it is requested");

struct Ctx {
  LAS char* sm; u32 m0w;
  rsrc_t rx, rqkv, rwg, rwu, rwd, rwo, rws;
  const char* ws;
  rsrc_t rt; u32 toff, tw;
  u32 vx, vkv, vg, vu, vw2;
};

template <int N> DI void issueA(const Ctx& c) {
  constexpr u32 dst = (N % 20) * SLOT;
  if constexpr (N < 21) {
    constexpr int kc = N / 3, j = N % 3;
    if constexpr (j < 2) dma(c.m0w + dst, c.vx, c.rx, j * 896 + kc * 128);
    else dma(c.m0w + dst, c.vkv, c.rqkv, kc * 64 * 2688);
  } else if constexpr (N < 49) {
    constexpr int kc = ((N - 21) / 2) % 7, ps = (N - 21) / 14;
    if constexpr ((N - 21) % 2 == 0) dma(c.m0w + dst, c.vg, c.rwg, kc * 64 * 3584 + ps * 1792);
    else dma(c.m0w + dst, c.vu, c.rwu, kc * 64 * 3584 + ps * 1792);
  } else if constexpr (blk_of(N - 49) < 28) dma(c.m0w + dst, c.vw2, c.rwd, blk_of(N - 49) * 64 * 896);
  else dma(c.m0w + dst, c.vw2, c.rwo, (blk_of(N - 49) - 28) * 64 * 896);
}

DI void type_a(Ctx& c, int w, int lane, int mt, int role, int R2, int n2, void* out DIAGP) {
  const int r = lane & 31, hi = lane >> 5, blk = (lane >> 4) & 1, q = (lane & 15) >> 2, p = lane & 3;
  const int sp = ((p & 1) << 1) | (p >> 1);
  const int rg = w & 3, cg = w >> 2, T = mt * 4 + rg;
  const bool isv = role >= 7;
  LAS char* sm = c.sm;
  u32 td = 0;
  static_for<3, PRO_DMA>([&](auto N) { issueA<N>(c); });
  u32x2 ev = ld64_sc1(w == 0 ? WS_E : OOB, c.rws, 0);
  const int R = 32 * (rg & 1) + r, xm = (R >> 1) & 7;
  int xa[4];
#pragma unroll
  for (int s = 0; s < 4; ++s) xa[s] = (rg >> 1) * SLOT + R * 128 + (((2 * s + hi) ^ xm) << 4);
  const int rowb = (8 * hi + q) * 128, q2 = q & 2;
  const int akv = rowb + (((2 * cg + blk) ^ q2) << 5) + (isv ? p : sp) * 8;
  int agu[2];
#pragma unroll
  for (int i = 0; i < 2; ++i) agu[i] = blk * SLOT + rowb + (((2 * cg + i) ^ q2 ^ blk) << 5) + sp * 8;
  int aguw[2][3];
#pragma unroll
  for (int i = 0; i < 2; ++i)
#pragma unroll
    for (int k = 0; k < 3; ++k) { aguw[i][k] = agu[i] + k * 65536; pin(aguw[i][k]); }
  const int rt = w & 1, kq = w >> 1;
  int aw2[2], aw2w[2][3];
#pragma unroll
  for (int i = 0; i < 2; ++i) aw2[i] = kq * 2048 + rowb + (((2 * i + blk) ^ q2) << 5) + p * 8;
  const int T2 = (R2 >> 1) * 4 + 2 * rt + (R2 & 1);

  u32x4 xf[28], kf[4], wf[2][2], af[35], hp[2];
  f32x16 akvacc = {}, g1[2] = {}, g2[2] = {}, ah[2] = {}, ao[2] = {};
  u32x2 pollv; u32 omg; u64 tag;
  const u32 lane16 = lane * 16;
  const u32 poll_off = WS_FLAG + (R2 >> 1) * 256 + (lane & 15) * 8;
  const bool need_h = (lane & 15) < 14;
  const u32 a2_off = WS_A2 + T2 * A2_T + kq * 1024 + lane16;
  const u32 h_off = WS_A2 + T * A2_T + (role * 4 + 2 * cg) * 1024 + lane16;

  auto read_kv = [&](auto J, auto S) {
    constexpr int j = J, sx = S;
    xf[4 * j + sx] = *(LAS u32x4*)(sm + xa[sx] + ((3 * j) % 20) * SLOT);
    kf[sx] = trfrag(sm + akv + ((3 * j + 2) % 20) * SLOT + sx * 2048);
  };
  auto read_w = [&](auto U, auto I) {
    constexpr int u = U, i = I;
    if constexpr (u < 56) {
      constexpr int sl = (21 + 2 * (u / 4)) % 20;
      const int wrap = sl == 19 ? blk * (-20 * SLOT) : 0;
      wf[u & 1][i] = trfrag(sm + aguw[i][sl / 8] + wrap + (sl % 8) * SLOT + (u % 4) * 2048);
    } else if constexpr (u < 91) {
      constexpr int sl = (49 + u - 56) % 20;
      if constexpr (u == 56) {
#pragma unroll
        for (int k = 0; k < 3; ++k) { aw2w[i][k] = aw2[i] + k * 65536; pin(aw2w[i][k]); }
      }
      wf[u & 1][i] = trfrag(sm + aw2w[i][sl / 8] + (sl % 8) * SLOT);
    }
  };
  auto publish = [&](int cls) {
    if (w == 0) __builtin_amdgcn_raw_buffer_store_b64(u32x2{(u32)tag, (u32)(tag >> 32)}, c.rws, lane == 0 ? role * 8 : OOB, WS_FLAG + cls * 4096 + mt * 256, SC1);
  };
  auto load_item = [&](auto IT) {
    constexpr int it = IT;
    if constexpr (it == IT_OMG) omg = ld32_sc1(r * 4, c.rws, WS_OMG + T2 * 128);
    else { constexpr int i = it < IT_OMG ? it : it - 1; af[i] = ld128_sc1(a2_off, c.rws, blk_of(i) * 4096); }
  };

  STAMP(1);
  sync_vm<PRO_OPS - 3 + (G_TOUCH == -2)>();
  STAMP(2);
  static_for<0, 4>([&](auto S) { read_kv(IC<0>{}, S); });
  SCHED();
  auto pass0 = [&](auto ISV) {
    static_for<0, U0>([&](auto G) {
      constexpr int g = G;
      sync_vm<sync_cnt(g)>();
      if constexpr (g == G_TOUCH) td = touch(c.toff, c.rt);
      SCHED();
      static_for<0, 4>([&](auto S) {
        constexpr int sx = S;
        if constexpr (ISV) mfma_at(akvacc, xf[4 * g + sx], kf[sx]); else mfma_at(akvacc, kf[sx], xf[4 * g + sx]);
        SCHED();
        if constexpr (g < 6) read_kv(IC<g + 1>{}, S); else read_w(IC<sx / 2>{}, IC<sx % 2>{});
        if constexpr (cum(g - 1) + sx < cum(g)) issueA<cum(g - 1) + sx>(c);
        SCHED();
      });
    });
  };
  static_assert(P0_RATE <= 4, "");
  if (isv) pass0(IC<1>{}); else pass0(IC<0>{});
  auto check = [&](auto CNT, int cls, bool need) {
    wait_vm<CNT>();
    pin(pollv);
    if (w == 0 && !match(pollv, need, tag)) spin(c.ws, poll_off + cls * 4096, need, tag);
  };
  static_for<U0, NG>([&](auto G) {
    constexpr int g = G, u = g - U0, b = u & 1;
    if constexpr (u == 0) { STAMP(3); if constexpr (G_TOUCH < 0) pin(c.tw); }
    if constexpr (u == 28) STAMP(4);
    if constexpr (u == 56) STAMP(5);
    if constexpr (u == 70) STAMP(8);
    if constexpr (u == 77) STAMP(9);
    if constexpr (u == U_CHK2) STAMP2(56);
    if constexpr (u == U_CHK3) STAMP2(58);
    if constexpr (is_sync(g)) STAMP2(16 + 2 * (u < 56 ? u / 8 : 7 + (u - 59) / 5));
    if constexpr (u == U_KVPUB) STAMP2(50);
    if constexpr (u == U_H1PUB) STAMP2(52);
    if constexpr (u == U_H2PUB) STAMP2(54);
    if constexpr (u == U_CHK1 && u != 56) check(IC<base(g) - base(U0 + U_POLL1) - 1>{}, 1, need_h);
    if constexpr (u == U_CHK2) check(IC<base(g) - base(U0 + U_POLL2) - 1>{}, 2, ORDER ? need_h : !need_h);
    if constexpr (u == U_CHK3) check(IC<base(g) - base(U0 + U_POLL3) - 1>{}, 2, ORDER ? !need_h : need_h);
    if constexpr (is_bar(g) && u != 56) sync_vm<sync_cnt(g)>();
    if constexpr (is_sync(g)) STAMP2(17 + 2 * (u < 56 ? u / 8 : 7 + (u - 59) / 5));
    if constexpr (u == U_CHK2) STAMP2(57);
    if constexpr (u == U_CHK3) STAMP2(59);
    if constexpr (u == U_KVPUB) { pin(ev); tag = tag_of(ev); publish(0); STAMP2(51); }
    if constexpr (u == U_H1PUB) { publish(1); STAMP2(53); }
    if constexpr (u == U_H2PUB) { publish(2); STAMP2(55); }
    if constexpr (g == G_TOUCH) td = touch(c.toff, c.rt);
    if constexpr (u == U_H1PUB) pin(td);
    if constexpr (u == U_POLL1) pollv = ld64_sc1(w == 0 ? poll_off : OOB, c.rws, 4096);
    if constexpr (u == U_POLL2 || u == U_POLL3) pollv = ld64_sc1(w == 0 ? poll_off : OOB, c.rws, 8192);
    if constexpr (u == U_KVST) {
#pragma unroll
      for (int s = 0; s < 2; ++s) {
        const u32 off = isv ? WS_V + T * KV_T + (s * 2 + hi) * 7168 + ((role - 7) * 64 + cg * 32 + r) * 16
                            : WS_K + T * KV_T + (role * 8 + cg * 4 + 2 * s) * 512 + lane16;
        st128(pk8(akvacc, s), c.rws, off, 0);
      }
    }
    if constexpr (u == U_H1STA) st128(hp[0], c.rws, h_off, 0);
    if constexpr (u == U_H1STB) st128(hp[1], c.rws, h_off, 1024);
    if constexpr (u == U_H2ST) {
#pragma unroll
      for (int t = 0; t < 2; ++t) {
#pragma unroll
        for (int j = 0; j < 8; j += 2) hp[t][j / 2] = pk(glu(g2[t][j], g2[t][8 + j]), glu(g2[t][j + 1], g2[t][9 + j]));
        st128(hp[t], c.rws, h_off, (56 + t) * 1024);
        SCHED();
      }
    }
    asm volatile("" ::: "memory");
    if constexpr (u == 56) STAMP2(44);
    if constexpr (u == 56 && U_CHK1 == 56) {
      check(IC<base(g) + n_st(g) - base(U0 + U_POLL1) - 1>{}, 1, need_h);
      asm volatile("s_barrier" ::: "memory");
      STAMP2(45);
    }
    constexpr bool ld_first = u >= 56 && ld_iter(item_of(u < 56 ? 0 : u - 56)) == g;
    if constexpr (ld_first) static_for<ld_lo(g), ld_lo(g + 1)>(load_item);
    if constexpr (u >= 56) {
      wait_vm<base(g) + n_st(g) + (ld_first ? n_ld(g) : 0) - pos_ld(item_of(u - 56)) - 1>();
      pin(af[u - 56]);
      if constexpr (u == 56) STAMP2(46);
      if constexpr (u == 70) STAMP2(47);
      if constexpr (u == 77) STAMP2(48);
    }
    SCHED();
    static_for<0, 2>([&](auto I) {
      constexpr int i = I;
      if constexpr (u < 28) mfma_at(g1[i], wf[b][i], xf[u]);
      else if constexpr (u < 56) mfma_at(g2[i], wf[b][i], xf[u - 28]);
      else if constexpr (blk_of(u - 56) < 28) mfma_at(ah[i], wf[b][i], af[u - 56]);
      else mfma_at(ao[i], wf[b][i], af[u - 56]);
      SCHED();
      read_w(IC<u + 2>{}, I);
      if constexpr (u >= 28 && u < 28 + 2 * EP1) {
        constexpr int t = (u - 28) / EP1, per = 4 / EP1, k0 = ((u - 28) % EP1) * per;
#pragma unroll
        for (int k = k0 + (i * per + 1) / 2; k < k0 + ((i + 1) * per + 1) / 2; ++k) {
          u32 hv = pk(glu(g1[t][2 * k], g1[t][8 + 2 * k]), glu(g1[t][2 * k + 1], g1[t][9 + 2 * k]));
          pin(hv);
          hp[t][k] = hv;
        }
      }
      if constexpr (i == 0 && !ld_first) static_for<ld_lo(g), ld_lo(g + 1)>(load_item);
      if constexpr (i == 1) static_for<cum(g - 1), cum(g)>([&](auto N) { issueA<N>(c); });
      SCHED();
    });
  });
  pin(omg);
  STAMP(6);
  const float om = __uint_as_float(omg);
  LAS char* red = sm + 4 * SLOT;
#pragma unroll
  for (int t = 0; t < 2; ++t)
#pragma unroll
    for (int a = 0; a < 4; ++a) {
      f32x4 v;
#pragma unroll
      for (int e = 0; e < 4; ++e) v[e] = ah[t][4 * a + e] * om + ao[t][4 * a + e];
      *(LAS f32x4*)(red + kq * 16384 + (2 * r + rt) * 256 + (((8 * t + 2 * a + hi) ^ (r & 15)) << 4)) = v;
    }
  asm volatile("s_waitcnt lgkmcnt(0)\n\ts_barrier" ::: "memory");
  STAMP(7);
  {
    const int row = w * 8 + (lane >> 3), c8 = lane & 7, m = (row >> 1) & 15;
    f32x4 s0 = {}, s1 = {};
#pragma unroll
    for (int k = 0; k < 4; ++k) {
      s0 += *(LAS f32x4*)(red + k * 16384 + row * 256 + (((2 * c8) ^ m) << 4));
      s1 += *(LAS f32x4*)(red + k * 16384 + row * 256 + (((2 * c8 + 1) ^ m) << 4));
    }
    u32x4 o = {pk(s0[0], s0[1]), pk(s0[2], s0[3]), pk(s1[0], s1[1]), pk(s1[2], s1[3])};
    *(u32x4*)((char*)out + (size_t)(R2 * 64 + row) * 896 + n2 * 128 + c8 * 16) = o;
  }
}
static_assert(freed(U0 + 90) >= 72, "the reduction scratch (slots 4..11 = sequence 64..71) must be free at the end");

constexpr int PRO_B = 12;
constexpr int freedB(int g) { return 6 * ((g + 1) / 4); }
constexpr int cumB(int g) {
  int cc = PRO_B;
  for (int j = 0; j <= g; ++j) { int t = cc + 2, cap = freedB(j) + 18; t = t < cap ? t : cap; cc = t < 42 ? t : 42; }
  return cc;
}
#define GB_POLL 20
#define GB_CHK 23
constexpr int GB_TOUCH = 3;
#define KV_RATE 3
constexpr int n_stB(int g) { return (g == GB_POLL); }
constexpr int baseB(int g) { int t = PRO_B; for (int j = 0; j < g; ++j) t += n_stB(j) + cumB(j) - cumB(j - 1); return t; }
constexpr int pos_dmaB(int n) {
  if (n < PRO_B) return n;
  for (int g = 0; g < 28; ++g) if (n < cumB(g)) return baseB(g) + n_stB(g) + (n - cumB(g - 1));
  return -1000;
}
constexpr int sync_cntB(int g) {
  int n = baseB(g) - pos_dmaB(6 * (g / 4 + 2) - 1) - 1 + (g > GB_CHK ? imin(22, (g - GB_CHK) * KV_RATE) : 0);
  return n;
}
template <int N> DI void issueB(const Ctx& c) {
  constexpr int kc = N / 6, j = N % 6;
  constexpr u32 dst = (N % 18) * SLOT;
  if constexpr (j < 2) dma(c.m0w + dst, c.vx, c.rx, j * 896 + kc * 128);
  else dma(c.m0w + dst, c.vkv, c.rqkv, kc * 64 * 2688 + (j - 2) * 128);
}

DI void type_b(Ctx& c, int w, int lane, int mt, int hf, const void* gw DIAGP) {
  const int r = lane & 31, hi = lane >> 5, blk = (lane >> 4) & 1, q = (lane & 15) >> 2, p = lane & 3;
  const int sp = ((p & 1) << 1) | (p >> 1);
  const int rg = w & 3, cg = w >> 2, T = mt * 4 + rg;
  LAS char* sm = c.sm;
  if (w == 0) dma(c.m0w + 18 * SLOT, lane * 16, mk(gw, 896), 0);
  u32x2 ev = ld64_sc1(w == 0 ? WS_E : OOB, c.rws, 0);
  static_for<3, PRO_B>([&](auto N) { issueB<N>(c); });
  const int R = 32 * (rg & 1) + r, xm = (R >> 1) & 7;
  int xa[4], aq[4];
#pragma unroll
  for (int s = 0; s < 4; ++s) xa[s] = (rg >> 1) * SLOT + R * 128 + (((2 * s + hi) ^ xm) << 4);
  const int h0 = 8 * hf + 4 * cg;
#pragma unroll
  for (int i = 0; i < 4; ++i) {
    const int col = 28 * (h0 + i) - 4 * (i & 1) - 192 * hf + 16 * blk + 4 * sp;
    aq[i] = (2 + (col >> 6)) * SLOT + (8 * hi + q) * 128 + (((col & 63) * 2) ^ ((q & 2) << 5));
  }
  const bool first = (mt == 0) && !(rg & 1);
  const int Tp = first ? T : ((rg & 1) ? T - 1 : T - 3);
  const u32 lane16 = lane * 16, lo16 = hi ? OOB : lane16;
  const u32 ob = WS_A2 + T * A2_T + (1792 + 28 * h0) * 64;
  const u32 omg_off = (hf == 0 && cg == 0 && hi == 0) ? r * 4 : OOB;
  const u32 poll_off = WS_FLAG + ((lane & 16) && mt > 0 ? mt - 1 : mt) * 256 + (lane & 15) * 8;
  const bool poll_need = (lane & 15) < 14;
  u32x4 xf[2], gf[2], wf[2][4];
  u32x4 kf[4][2], kp[2], vf[4][2], vp[4];
  f32x16 qa[4] = {};
  u32x2 pollv;
  const int sr = (r & 19) | ((r & 4) << 1) | ((r & 8) >> 1);
  const u32 kp_off = (28 * (r >> 3) - 4 * ((r >> 3) & 1)) * 64 + (hi * 32 + 24 + (r & 7)) * 16;
  const u32 v_off = hi * 7168 + sr * 16;
  auto load_kv = [&](auto IT) {
    constexpr int it = IT;
    if constexpr (it < 2) kp[it] = ld128_sc1(kp_off, c.rws, WS_K + Tp * KV_T + 28 * h0 * 64 + it * 1024);
    else if constexpr (it < 10) {
      constexpr int i = (it - 2) / 2, k = it & 1;
      kf[i][k] = ld128_sc1(lane16, c.rws, WS_K + T * KV_T + (28 * (h0 + i) - 4 * (i & 1)) * 64 + k * 1024);
    } else {
      constexpr int i = (it - 10) / 3, k = (it - 10) % 3;
      const int wh = 28 * (h0 + i) - 4 * (i & 1);
      if constexpr (k == 0) vp[i] = ld128_sc1(v_off, c.rws, WS_V + Tp * KV_T + 14336 + wh * 16);
      else vf[i][k - 1] = ld128_sc1(v_off, c.rws, WS_V + T * KV_T + (k - 1) * 14336 + wh * 16);
    }
  };
  auto read_x = [&](auto G) {
    constexpr int g = G;
    if constexpr (g < 28) {
      xf[g & 1] = *(LAS u32x4*)(sm + xa[g % 4] + ((6 * (g / 4)) % 18) * SLOT);
      gf[g & 1] = *(LAS u32x4*)(sm + 18 * SLOT + g * 32 + hi * 16);
    }
  };
  auto read_w = [&](auto G, auto I) {
    constexpr int g = G;
    if constexpr (g < 28) wf[g & 1][I] = trfrag(sm + aq[I] + ((6 * (g / 4)) % 18) * SLOT + (g % 4) * 2048);
  };
  STAMP(1);
  sync_vm<PRO_B - 6>();
  if constexpr (G_TOUCH < 0) pin(c.tw);
  STAMP(2);
  u64 tag;
  read_x(IC<0>{});
  static_for<0, 4>([&](auto I) { read_w(IC<0>{}, I); });
  SCHED();
  static_for<0, 28>([&](auto G) {
    constexpr int g = G, b = g & 1;
    if constexpr (g == GB_CHK) {
      STAMP(8);
      wait_vm<baseB(g) - baseB(GB_POLL) - 1>();
      pin(pollv); pin(ev);
      tag = tag_of(ev);
      if (w == 0 && !match(pollv, poll_need, tag)) spin(c.ws, poll_off, poll_need, tag);
    }
    if constexpr (g % 4 == 3 && g < 27) { STAMP2(16 + 2 * (g / 4)); asm volatile("s_waitcnt lgkmcnt(0)" ::: "memory"); sync_vm<sync_cntB(g)>(); STAMP2(17 + 2 * (g / 4)); }
    if constexpr (g == GB_CHK) STAMP(9);
    if constexpr (g == GB_POLL) pollv = ld64_sc1(w == 0 ? poll_off : OOB, c.rws, 0);
    SCHED();
    static_for<0, 4>([&](auto I) {
      constexpr int i = I;
      mfma_at(qa[i], wf[b][i], xf[b]);
      SCHED();
      if constexpr (i == 0) read_x(IC<g + 1>{});
      read_w(IC<g + 1>{}, I);
      if constexpr (i < 2) {
        wf[b][2][2 * i] = r == 28 ? gf[b][2 * i] : wf[b][2][2 * i];
        wf[b][2][2 * i + 1] = r == 28 ? gf[b][2 * i + 1] : wf[b][2][2 * i + 1];
        pin(wf[b][2]);
      }
      if constexpr (i >= 2 && cumB(g - 1) + i - 2 < cumB(g)) issueB<cumB(g - 1) + i - 2>(c);
      if constexpr (g >= GB_CHK && i < KV_RATE) if constexpr ((g - GB_CHK) * KV_RATE + i < 22) load_kv(IC<(g - GB_CHK) * KV_RATE + i>{});
      SCHED();
    });
  });
  static_assert(cumB(GB_CHK - 1) == 42, "no DMA after the first K/V load");
  static_for<(28 - GB_CHK) * KV_RATE, 22>(load_kv);
  STAMP(3);
  const float z = __uint_as_float(__builtin_amdgcn_permlane32_swap(__float_as_uint(qa[2][12]), __float_as_uint(qa[2][12]), false, false)[1]);
  const float gate = __builtin_amdgcn_rcpf(1.f + __builtin_amdgcn_exp2f(z * -1.4426950408889634f));
  const u32 omg = __float_as_uint(__builtin_amdgcn_rcpf(1.f + __builtin_amdgcn_exp2f(z * 1.4426950408889634f)));
  int ab[4];
  u32 msk[5][2];
#pragma unroll
  for (int b = 0; b < 4; ++b) ab[b] = (r >> 3) - ((r & 7) < 4 * hi + b ? 1 : 0);
#pragma unroll
  for (int a = 0; a < 5; ++a)
#pragma unroll
    for (int e = 0; e < 2; ++e) msk[a][e] = (ab[2 * e] == a - 1 ? 0xFFFFu : 0u) | (ab[2 * e + 1] == a - 1 ? 0xFFFF0000u : 0u);
  const float csc = 0.18898223650461362f * 1.4426950408889634f;
  __builtin_amdgcn_raw_buffer_store_b32(omg, c.rws, omg_off, WS_OMG + T * 128, SC1);
  asm volatile("" ::: "memory");
  u32 p01[4], p23[4];
  wait_vm<13>();
  STAMP(4);
  pin(kp[0]); pin(kp[1]);
  static_for<0, 4>([&](auto I) { pin(kf[I][0]); pin(kf[I][1]); });
  static_for<0, 4>([&](auto I) {
    constexpr int i = I;
    u32x4 qf[2] = {pk8(qa[i], 0), pk8(qa[i], 1)};
    if (i & 1) { qf[0][0] = hi ? qf[0][0] : 0u; qf[0][1] = hi ? qf[0][1] : 0u; }
    else       { qf[1][2] = hi ? 0u : qf[1][2]; qf[1][3] = hi ? 0u : qf[1][3]; }
    f32x16 so = {}, sv = {};
    so = mfma(kf[i][0], qf[0], so); sv = mfma(kp[0], qf[0], sv);
    so = mfma(kf[i][1], qf[1], so); sv = mfma(kp[1], qf[1], sv);
    float v[4];
#pragma unroll
    for (int b = 0; b < 4; ++b) {
      float t = first ? -1e30f : sv[4 * i + b];
#pragma unroll
      for (int a = 0; a < 4; ++a) t = ab[b] == a ? so[4 * a + b] : t;
      v[b] = t;
    }
    const float mx = xhalf_max(fmaxf(fmaxf(v[0], v[1]), fmaxf(v[2], v[3])));
#pragma unroll
    for (int b = 0; b < 4; ++b) v[b] = __builtin_amdgcn_exp2f((v[b] - mx) * csc);
    const float sc = gate * __builtin_amdgcn_rcpf(xhalf_sum((v[0] + v[1]) + (v[2] + v[3])));
    p01[i] = pk(v[0] * sc, v[1] * sc); p23[i] = pk(v[2] * sc, v[3] * sc);
  });
  static_for<0, 4>([&](auto I) { pin(p01[I]); pin(p23[I]); });
  STAMP(10);
  static_for<0, 2>([&](auto J) {
    constexpr int j = J;
    wait_vm<(j == 0 ? 7 : 5)>();
    u32x4 op[2][2];
#pragma unroll
    for (int e = 0; e < 2; ++e) {
      const int i = 2 * j + e;
      pin(vp[i]); pin(vf[i][0]); pin(vf[i][1]);
      f32x16 oa = {};
      oa = mfma(vp[i], u32x4{0u, 0u, p01[i] & msk[0][0], p23[i] & msk[0][1]}, oa);
      oa = mfma(vf[i][0], u32x4{p01[i] & msk[1][0], p23[i] & msk[1][1], p01[i] & msk[2][0], p23[i] & msk[2][1]}, oa);
      oa = mfma(vf[i][1], u32x4{p01[i] & msk[3][0], p23[i] & msk[3][1], p01[i] & msk[4][0], p23[i] & msk[4][1]}, oa);
      op[e][0] = pk8(oa, 0); op[e][1] = pk8(oa, 1);
    }
#pragma unroll
    for (int e = 0; e < 2; ++e) { auto t = __builtin_amdgcn_permlane32_swap(op[0][1][e], op[1][0][e], false, false); op[0][1][e] = t[0]; op[1][0][e] = t[1]; }
    const u32 o2 = ob + j * 3584;
    st128(op[0][0], c.rws, lane16, o2);
    st128(op[0][1], c.rws, lo16, o2 + 1024);
    st128(op[1][0], c.rws, lane16, o2 + 1536);
    st128(op[1][1], c.rws, lane16, o2 + 2560);
  });
  STAMP(5);
  sync_vm<0>();
  if (w == 0) __builtin_amdgcn_raw_buffer_store_b64(u32x2{(u32)tag, (u32)(tag >> 32)}, c.rws, lane == 0 ? (14 + hf) * 8 : OOB, WS_FLAG + 8192 + mt * 256, SC1);
  STAMP(6);
  if (mt == 0 && hf == 0 && w == 0) {
#pragma unroll 1
    for (int i = 0; i < 4; ++i) spin(c.ws, WS_FLAG + 8192 + (i * 4 + (lane >> 4)) * 256 + (lane & 15) * 8, true, tag);
    __builtin_amdgcn_raw_buffer_store_b64(u32x2{(u32)tag, (u32)(tag >> 32)}, c.rws, lane == 0 ? 0 : OOB, WS_E, SC1);
  }
}

__global__ __launch_bounds__(512, 2) void fused_kernel(const void* __restrict__ x, const void* __restrict__ wg, const void* __restrict__ wu,
                                                       const void* __restrict__ wd, const void* __restrict__ wo, const void* __restrict__ wqkv,
                                                       const void* __restrict__ gw, void* __restrict__ out, void* __restrict__ ws) {
  __shared__ __attribute__((aligned(1024))) char smem[163840];
  const int lane = threadIdx.x & 63, w = __builtin_amdgcn_readfirstlane(threadIdx.x >> 6);
  const void* tp = ((const void* const __attribute__((address_space(4)))*)__builtin_amdgcn_kernarg_segment_ptr())[w];
  const int b = blockIdx.x, xc = b & 7, ix = b >> 3;
  const int mq = xc >> 1, mt = mq * 4 + (ix & 3), rl = ix >> 2, rh = xc & 1;
  const bool isa = rl < 7;
  const int role = rh * 7 + rl;
  Ctx c;
  c.sm = (LAS char*)smem;
  c.m0w = (u32)(size_t)c.sm + w * 1024;
  c.rx = mk(x, 2048 * 896); c.rqkv = mk(wqkv, 448 * 2688); c.rwg = mk(wg, 448 * 3584); c.rwu = mk(wu, 448 * 3584);
  c.rwd = mk(wd, 1792 * 896); c.rwo = mk(wo, 448 * 896); c.rws = mk(ws, WS_END);
  c.ws = (const char*)ws;
  const int r8 = lane >> 3, c8 = lane & 7, R = 8 * w + r8;
  c.vx = (mt * 128 + 2 * R) * 896 + ((c8 ^ ((R >> 1) & 7)) << 4);
  const int sw = (c8 ^ ((r8 & 2) << 1)) << 4;
  const int isai = (rl - 7) >> 31;
  c.vkv = R * 2688 + ((896 + role * 128) & isai) + ((rh * 384) & ~isai) + sw;
  auto first3 = [&] { static_for<0, 3>([&](auto N) { issueA<N>(c); }); };
  if constexpr (G_TOUCH != -1) first3();
  {
    auto tab = [&](u64 t) { return (int)((t >> (8 * w)) & 255); };
    const int run = (lane * 37) >> 8, l = lane - 7 * run;
    const int gu = tab(0x010100ull), bb = tab(0x010000010100ull), stride = tab(0x1507071C1C00ull) * 128;
    const int row0 = tab(0x0208000000ull) * b + bb * (mq * 112 + ix * 4);
    const int n = tab(0x1C0E38383800ull) & isai;
    const u32 o = (row0 + (run >> gu)) * stride + ((run & gu) * 14 + bb * rh * 7 + tab(0x070000000000ull) + l) * 128;
    c.toff = lane < n ? o : OOB;
    c.rt = mk(tp, (448 << tab(0x02000000ull)) * stride);
    if constexpr (G_TOUCH < 0) c.tw = touch(c.toff, c.rt);
  }
  if constexpr (G_TOUCH == -1) first3();
  if (__builtin_expect(isa, 1)) {
    c.vg = R * 3584 + role * 128 + sw;
    c.vu = R * 3584 + role * 128 + (sw ^ 32);
    c.vw2 = R * 896 + rl * 128 + sw;
    type_a(c, w, lane, mt, role, xc * 4 + (ix & 3), rl, out DIAGA);
  } else {
    type_b(c, w, lane, mt, rh, gw DIAGA);
  }
}

extern "C" void kernel_launch(void* const* d_in, const int* in_sizes, int n_in, void* d_out, int out_size, void* d_ws, size_t ws_size,
                              hipStream_t stream) {
  fused_kernel<<<256, 512, 0, stream>>>(d_in[0], d_in[4], d_in[5], d_in[6], d_in[2], d_in[1], d_in[7], d_out, d_ws);
}
```

```cpp
#include <hip/hip_runtime.h>
#include <hip/hip_bf16.h>

typedef unsigned long long u64;
typedef unsigned u32;
typedef __bf16 bf16x8 __attribute__((ext_vector_type(8)));
typedef __bf16 bf16x2 __attribute__((ext_vector_type(2)));
typedef short s16x4 __attribute__((ext_vector_type(4)));
typedef u32 u32x4 __attribute__((ext_vector_type(4)));
typedef u32 u32x2 __attribute__((ext_vector_type(2)));
typedef float f32x16 __attribute__((ext_vector_type(16)));
typedef float f32x4 __attribute__((ext_vector_type(4)));
typedef __amdgpu_buffer_rsrc_t rsrc_t;
#define LAS __attribute__((address_space(3)))
#define GAS __attribute__((address_space(1)))
#define DI __device__ __forceinline__
#define SCHED() __builtin_amdgcn_sched_barrier(0)
template <int V> using IC = std::integral_constant<int, V>;

constexpr u32 WS_E = 0;
constexpr u32 WS_FLAG = 4096;
constexpr u32 WS_OMG = 32768;
constexpr u32 WS_K = 65536;
constexpr u32 KV_T = 28672;
constexpr u32 WS_V = WS_K + 64 * KV_T;
constexpr u32 WS_A2 = WS_V + 64 * KV_T;
constexpr u32 A2_T = 143360;
constexpr u32 WS_END = WS_A2 + 64 * A2_T;
constexpr u32 OOB = 0xFFFFFF00u;
constexpr int SC1 = 16;
constexpr int SLOT = 8192;
constexpr int SPIN_CAP = 1 << 15;

DI rsrc_t mk(const void* p, u32 bytes) { return __builtin_amdgcn_make_buffer_rsrc((void*)p, (short)0, (int)bytes, 0x00020000); }
DI void dma(u32 m0v, u32 voff, rsrc_t rs, u32 soff) {
  asm volatile("s_mov_b32 m0, %0\n\ts_nop 0\n\tbuffer_load_dwordx4 %1, %2, %3 offen lds" ::"s"(m0v), "v"(voff), "s"(rs), "s"(soff) : "memory");
}
DI u32x4 ld128_sc1(u32 voff, rsrc_t rs, u32 soff) {
  u32x4 v;
  asm volatile("buffer_load_dwordx4 %0, %1, %2, %3 offen sc1" : "=v"(v) : "v"(voff), "s"(rs), "s"(soff) : "memory");
  return v;
}
DI u32x2 ld64_sc1(u32 voff, rsrc_t rs, u32 soff) {
  u32x2 v;
  asm volatile("buffer_load_dwordx2 %0, %1, %2, %3 offen sc1" : "=v"(v) : "v"(voff), "s"(rs), "s"(soff) : "memory");
  return v;
}
DI u32 ld32_sc1(u32 voff, rsrc_t rs, u32 soff) {
  u32 v;
  asm volatile("buffer_load_dword %0, %1, %2, %3 offen sc1" : "=v"(v) : "v"(voff), "s"(rs), "s"(soff) : "memory");
  return v;
}
DI u32 touch(u32 voff, rsrc_t rs) {
  u32 v;
  asm volatile("buffer_load_dword %0, %1, %2, 0 offen" : "=v"(v) : "v"(voff), "s"(rs) : "memory");
  return v;
}
DI void st128(u32x4 v, rsrc_t rs, u32 voff, u32 soff) {
  asm volatile("buffer_store_dwordx4 %0, %1, %2, %3 offen sc1\n\ts_nop 1" ::"v"(v), "v"(voff), "s"(rs), "s"(soff) : "memory");
}
constexpr int clampvm(int n) { return n > 63 ? 63 : n; }
template <int N> DI void wait_vm() { static_assert(N >= 0, ""); asm volatile("s_waitcnt vmcnt(%0)" ::"n"(clampvm(N)) : "memory"); }
template <class T> DI void pin(T& v) { asm volatile("" : "+v"(v)); }

DI u32x4 trfrag(LAS char* p) {
  s16x4 lo = __builtin_amdgcn_ds_read_tr16_b64_v4i16((LAS s16x4*)p);
  s16x4 hi = __builtin_amdgcn_ds_read_tr16_b64_v4i16((LAS s16x4*)(p + 512));
  u32x2 a = __builtin_bit_cast(u32x2, lo), b = __builtin_bit_cast(u32x2, hi);
  return u32x4{a.x, a.y, b.x, b.y};
}
DI f32x16 mfma(u32x4 a, u32x4 b, f32x16 c) {
  return __builtin_amdgcn_mfma_f32_32x32x16_bf16(__builtin_bit_cast(bf16x8, a), __builtin_bit_cast(bf16x8, b), c, 0, 0, 0);
}
DI void mfma_at(f32x16& c, u32x4 a, u32x4 b) { c = mfma(a, b, c); asm volatile("" : "+v"(c)); }
DI u32 pk(float a, float b) { bf16x2 c; c[0] = (__bf16)a; c[1] = (__bf16)b; return __builtin_bit_cast(u32, c); }
DI u32x4 pk8(const f32x16& v, int s) { return u32x4{pk(v[8 * s], v[8 * s + 1]), pk(v[8 * s + 2], v[8 * s + 3]), pk(v[8 * s + 4], v[8 * s + 5]), pk(v[8 * s + 6], v[8 * s + 7])}; }
DI float glu(float g, float u) {
  return u * __builtin_amdgcn_rcpf(1.f + __builtin_amdgcn_exp2f(g * -1.4426950408889634f)); }
DI float xhalf_max(float v) { auto r = __builtin_amdgcn_permlane32_swap(__float_as_uint(v), __float_as_uint(v), false, false); return fmaxf(__uint_as_float(r[0]), __uint_as_float(r[1])); }
DI float xhalf_sum(float v) { auto r = __builtin_amdgcn_permlane32_swap(__float_as_uint(v), __float_as_uint(v), false, false); return __uint_as_float(r[0]) + __uint_as_float(r[1]); }

DI void spin(const char* ws, u32 off, bool need, u64 tag) {
  const GAS u64* p = (const GAS u64*)(ws + off);
#pragma unroll 1
  for (int i = 0; i < SPIN_CAP; ++i) {
    u64 v = __hip_atomic_load(p, __ATOMIC_RELAXED, __HIP_MEMORY_SCOPE_AGENT);
    if (__all(!need || v == tag)) break;
    __builtin_amdgcn_s_sleep(2);
  }
  asm volatile("" ::: "memory");
}
DI bool match(u32x2 v, bool need, u64 tag) {
  return __all(!need || (v.x == (u32)tag && v.y == (u32)(tag >> 32))); }
DI u64 tag_of(u32x2 ev) {
  u64 e = ((u64)__builtin_amdgcn_readfirstlane(ev.y) << 32) | (u32)__builtin_amdgcn_readfirstlane(ev.x);
  return e * 6364136223846793005ull + 1442695040888963407ull;
}

#define STAMP(i)
#define DIAGP
#define DIAGA
#define STAMP2(i)
template <int N> DI void sync_vm() { static_assert(N >= 0, ""); asm volatile("s_waitcnt vmcnt(%0)\n\ts_barrier" ::"n"(clampvm(N)) : "memory"); }

template <int I, int N, class Fn> DI void static_for(Fn&& f) {
  if constexpr (I < N) { f(IC<I>{}); static_for<I + 1, N>(f); }
}

#define HEAD "KKGKKGKKGK"
#define HEAD_BARS 0x1011414145ull
#define HEAD_N 26
#define CFG 9,20,-2,22,40,57,52,56,64,68,70,76,2,6,6,2,6,6,2,0,1,2,16,16,0
constexpr int kCfg[] = {CFG};
constexpr int PRO_DMA = kCfg[0], HEAD_RATE = kCfg[1], G_TOUCH = kCfg[2], U_KVPUB = kCfg[3], U_H1PUB = kCfg[4], U_H2PUB = kCfg[5];
constexpr int U_POLL1 = kCfg[6], U_CHK1 = kCfg[7], U_POLL2 = kCfg[8], U_CHK2 = kCfg[9], U_POLL3 = kCfg[10], U_CHK3 = kCfg[11];
constexpr int EP1 = kCfg[12];
constexpr int ld_rel(int cl, int k) { int f = kCfg[13 + 3 * cl], a = kCfg[14 + 3 * cl], b = kCfg[15 + 3 * cl]; return k < f ? k / a : (f + a - 1) / a + (k - f) / b; }
#define TAIL 0
#define ORDER 1
constexpr int N2 = ORDER ? 14 : 8, IT_OMG = ORDER ? 28 : 14;
constexpr int NV = 105, U0 = 14;
constexpr int U_H2ST = 56;
#define TPOS 3
constexpr int PRO_OPS = PRO_DMA + 1 + (G_TOUCH == -2);
constexpr int imin(int a, int b) { return a < b ? a : b; }
constexpr int item_of(int i) { return i < IT_OMG ? i : i + 1; }
constexpr int blk_of(int i) { return ORDER || i < 14 ? i : (i < 21 ? i + 14 : i - 7); }
constexpr int ld_iter(int it) { return U0 + (it < 14 ? U_CHK1 + ld_rel(0, it) : (it < 14 + N2 ? U_CHK2 + ld_rel(1, it - 14) : U_CHK3 + ld_rel(2, it - 14 - N2))); }
struct Sched {
  int nhead = 0, vkvst = 0, vg1 = 0;
  int xl[NV + 2] = {}, wl[NV + 2] = {};
  int kind[NV + 2] = {}, idx[NV + 2] = {}, lo[NV + 2] = {}, hi[NV + 2] = {};
  int pk[84] = {}, pi[84] = {}, pj[84] = {};
  bool bar[NV] = {};
  int cum[NV + 1] = {}, base[NV + 1] = {}, nst[NV] = {}, ldlo[NV + 1] = {}, cnt[NV] = {}, pdma[84] = {}, pld[36] = {}, freed[NV] = {};
  int cnt0 = 0; bool ok = true;
  constexpr Sched() {
    const char* h = HEAD;
    int v = 0, n = 0, k = 0, g = 0;
    int nx = 0;
    auto add = [&](int kd, int st, int nit, int nw) {
      const int first = n; int x = -1;
      if (st < 7 && st == nx) { x = n; for (int e = 0; e < 2; ++e) { pk[n] = 0; pi[n] = st; pj[n] = e; ++n; } ++nx; }
      for (int e = 0; e < nit; ++e) { kind[v] = kd; idx[v] = nit * st + e; lo[v] = first; hi[v] = n + nw - 1; xl[v] = x; wl[v] = n; ++v; }
      for (int e = 0; e < nw; ++e) { pk[n] = kd; pi[n] = st; pj[n] = kd ? e : 2; ++n; }
    };
    for (int i = 0; h[i]; ++i) { if (h[i] == 'K') { add(0, k++, 2, 1); nhead = v; } else { add(1, g++, 4, 2); if (g == 7) vg1 = v; } }
    if (k != 7 || g > 7) ok = false;
    vkvst = nhead + 1;
    while (g < 14) { add(1, g++, 4, 2); if (g == 7) vg1 = v; }
    for (int i = 0; i < 35; ++i) { kind[v] = 2; idx[v] = 56 + i; lo[v] = hi[v] = wl[v] = n; pk[n] = 2; pi[n] = i; ++n; ++v; }
    kind[NV] = kind[NV + 1] = 3; hi[NV] = hi[NV + 1] = 83;
    for (v = 0; v < NV; ++v) {
      const int u = v - U0, t = u + 2;
      bar[v] = v < 42 ? (((unsigned long long)(HEAD_BARS) >> v) & 1) : (t < 56 ? t % 8 == 0 : (t < 91 && (t - 56) % 5 == 0));
      if ((u == U_KVPUB || u == U_H1PUB || u == U_H2PUB || (u == U_CHK1 && u != 56) || u == U_CHK2 || u == U_CHK3)) bar[v] = true;
    }
    if (U0 + U_KVPUB <= vkvst || U0 + U_H1PUB <= vg1 + 2 * EP1) ok = false;
    int fr = 0, cc = PRO_DMA, cr = 0, it = 0;
    cum[0] = cc; base[0] = PRO_OPS;
    for (n = 0; n < PRO_DMA; ++n) pdma[n] = n + (G_TOUCH == -2 && n >= TPOS);
    for (v = 0; v < NV; ++v) {
      const int u = v - U0;
      if (bar[v] || (u == 56 && U_CHK1 == 56)) fr = lo[v];
      freed[v] = fr;
      const int pay = 2 * (v == vkvst) + (v == vg1 + EP1) + (v == vg1 + 2 * EP1) + 2 * (u == U_H2ST);
      nst[v] = pay + (v == G_TOUCH) + (u == U_POLL1) + (u == U_POLL2) + (u == U_POLL3);
      ldlo[v] = it;
      while (it < 36 && ld_iter(it) == v) { pld[it] = base[v] + nst[v] + (it - ldlo[v]); ++it; }
      cr += (v < HEAD_N ? HEAD_RATE : (u < 56 ? kCfg[22] : kCfg[23])) - kCfg[24] * pay;
      int t = imin(imin(cc + imin(cr > 0 ? cr / 16 : 0, 2), fr + 20), 84);
      cr -= 16 * (t - cc); if (cr > 16) cr = 16;
      for (n = cc; n < t; ++n) pdma[n] = base[v] + nst[v] + (it - ldlo[v]) + (n - cc);
      base[v + 1] = base[v] + nst[v] + (it - ldlo[v]) + (t - cc);
      cc = t; cum[v + 1] = cc;
    }
    ldlo[NV] = it;
    if (it != 36 || cc != 84) ok = false;
    for (v = -1; v < NV; ++v) {
      if (v >= 0 && !bar[v]) continue;
      int nx = v + 1; while (nx < NV && !bar[nx]) ++nx;
      const int need = hi[nx + 1], at = v < 0 ? 0 : v;
      if (need >= cum[at]) ok = false;
      int c = base[at] - pdma[need] - 1;
      const int u = v - U0;
      if (u == U_KVPUB) c = imin(c, base[v] - (base[vkvst] + nst[vkvst]));
      if (u == U_H1PUB) c = imin(c, base[v] - (base[vg1 + 2 * EP1] + nst[vg1 + 2 * EP1]));
      if (u == U_H2PUB) c = imin(c, base[v] - (base[U0 + U_H2ST] + nst[U0 + U_H2ST]));
      if (v < 0) cnt0 = c; else cnt[v] = c;
    }
    for (int i = 0; i < 35; ++i) if (ld_iter(item_of(i)) > U0 + 56 + i) ok = false;
  }
};
constexpr Sched SA{};
static_assert(SA.ok, "schedule: a piece or operand is used before it is requested, or HEAD is malformed");
static_assert(U_POLL1 < U_CHK1 && U_CHK1 < U_POLL2 && U_POLL2 < U_CHK2 && U_CHK2 < U_POLL3 && U_POLL3 < U_CHK3, "");
static_assert(U_KVPUB < U_CHK1 && U_H2PUB > U_H2ST && U_H1PUB < U_CHK1 && U_H2PUB < U_CHK2, "a workgroup publishes before it waits");
static_assert(SA.freed[NV - 1] >= 72, "the reduction scratch (slots 4..11 = pieces 64..71) must be free at the end");

struct Ctx {
  LAS char* sm; u32 m0w;
  rsrc_t rx, rqkv, rwg, rwu, rwd, rwo, rws;
  const char* ws;
  rsrc_t rt; u32 toff, tw;
  u32 vx, vkv, vg, vu, vw2;
};

template <int N> DI void issueA(const Ctx& c) {
  constexpr u32 dst = (N % 20) * SLOT;
  constexpr int k = SA.pk[N], st = SA.pi[N], j = SA.pj[N];
  if constexpr (k == 0) {
    if constexpr (j < 2) dma(c.m0w + dst, c.vx, c.rx, j * 896 + st * 128);
    else dma(c.m0w + dst, c.vkv, c.rqkv, st * 64 * 2688);
  } else if constexpr (k == 1) {
    if constexpr (j == 0) dma(c.m0w + dst, c.vg, c.rwg, (st % 7) * 64 * 3584 + (st / 7) * 1792);
    else dma(c.m0w + dst, c.vu, c.rwu, (st % 7) * 64 * 3584 + (st / 7) * 1792);
  } else if constexpr (blk_of(st) < 28) dma(c.m0w + dst, c.vw2, c.rwd, blk_of(st) * 64 * 896);
  else dma(c.m0w + dst, c.vw2, c.rwo, (blk_of(st) - 28) * 64 * 896);
}

DI void type_a(Ctx& c, int w, int lane, int mt, int role, int R2, int n2, void* out DIAGP) {
  const int r = lane & 31, hi = lane >> 5, blk = (lane >> 4) & 1, q = (lane & 15) >> 2, p = lane & 3;
  const int sp = ((p & 1) << 1) | (p >> 1);
  const int rg = w & 3, cg = w >> 2, T = mt * 4 + rg;
  const bool isv = role >= 7;
  LAS char* sm = c.sm;
  u32 td = 0;
  static_for<2, TPOS>([&](auto N) { issueA<N>(c); });
  if constexpr (G_TOUCH == -2) c.tw = touch(c.toff, c.rt);
  static_for<TPOS, PRO_DMA>([&](auto N) { issueA<N>(c); });
  u32x2 ev = ld64_sc1(w == 0 ? WS_E : OOB, c.rws, 0);
  const int R = 32 * (rg & 1) + r, xm = (R >> 1) & 7;
  int xa[4];
#pragma unroll
  for (int s = 0; s < 4; ++s) xa[s] = (rg >> 1) * SLOT + R * 128 + (((2 * s + hi) ^ xm) << 4);
  const int rowb = (8 * hi + q) * 128, q2 = q & 2;
  const int akv = rowb + (((2 * cg + blk) ^ q2) << 5) + (isv ? p : sp) * 8;
  int agu[2];
#pragma unroll
  for (int i = 0; i < 2; ++i) agu[i] = blk * SLOT + rowb + (((2 * cg + i) ^ q2 ^ blk) << 5) + sp * 8;
  int aguw[2][3];
#pragma unroll
  for (int i = 0; i < 2; ++i)
#pragma unroll
    for (int k = 0; k < 3; ++k) { aguw[i][k] = agu[i] + k * 65536; pin(aguw[i][k]); }
  const int rt = w & 1, kq = w >> 1;
  int aw2[2], aw2w[2][3];
#pragma unroll
  for (int i = 0; i < 2; ++i) aw2[i] = kq * 2048 + rowb + (((2 * i + blk) ^ q2) << 5) + (TAIL ? sp : p) * 8;
  const int T2 = (R2 >> 1) * 4 + 2 * rt + (R2 & 1);

  u32x4 xf[28], wf[2][2], af[35], hp[2];
  f32x16 akvacc = {}, g1[2] = {}, g2[2] = {}, ah[2] = {}, ao[2] = {};
  u32x2 pollv; u32 omg; u64 tag;
  const u32 lane16 = lane * 16;
  const u32 poll_off = WS_FLAG + (R2 >> 1) * 256 + (lane & 15) * 8;
  const bool need_h = (lane & 15) < 14;
  const u32 a2_off = WS_A2 + T2 * A2_T + kq * 1024 + lane16;
  const u32 h_off = WS_A2 + T * A2_T + (role * 4 + 2 * cg) * 1024 + lane16;

  auto read_w = [&](auto V, auto I) {
    constexpr int v = V, i = I, k = SA.kind[v], u = SA.idx[v], sl = SA.wl[v] % 20;
    if constexpr (k < 2 && SA.xl[v] >= 0 && (k == 0 || i == 0)) {
      constexpr int ks = k == 0 ? 2 * u + i : u, xs = SA.xl[v] % 20;
      const int wrap = xs == 19 ? (rg >> 1) * (-20 * SLOT) : 0;
      xf[ks] = *(LAS u32x4*)(sm + xa[ks % 4] + wrap + xs * SLOT);
    }
    if constexpr (k == 0) {
      wf[v & 1][i] = trfrag(sm + akv + sl * SLOT + ((2 * u + i) % 4) * 2048);
    } else if constexpr (k == 1) {
      const int wrap = sl == 19 ? blk * (-20 * SLOT) : 0;
      wf[v & 1][i] = trfrag(sm + aguw[i][sl / 8] + wrap + (sl % 8) * SLOT + (u % 4) * 2048);
    } else if constexpr (k == 2) {
      if constexpr (u == 56) {
#pragma unroll
        for (int e = 0; e < 3; ++e) { aw2w[i][e] = aw2[i] + e * 65536; pin(aw2w[i][e]); }
      }
      wf[v & 1][i] = trfrag(sm + aw2w[i][sl / 8] + (sl % 8) * SLOT);
    }
  };
  auto publish = [&](int cls) {
    if (w == 0) __builtin_amdgcn_raw_buffer_store_b64(u32x2{(u32)tag, (u32)(tag >> 32)}, c.rws, lane == 0 ? role * 8 : OOB, WS_FLAG + cls * 4096 + mt * 256, SC1);
  };
  auto load_item = [&](auto IT) {
    constexpr int it = IT;
    if constexpr (it == IT_OMG) omg = ld32_sc1(r * 4, c.rws, WS_OMG + T2 * 128);
    else { constexpr int i = it < IT_OMG ? it : it - 1; af[i] = ld128_sc1(a2_off, c.rws, blk_of(i) * 4096); }
  };

  STAMP(1);
  sync_vm<SA.cnt0>();
  STAMP(2);
  static_for<0, 4>([&](auto S) { read_w(IC<S / 2>{}, IC<S % 2>{}); });
  SCHED();
  auto check = [&](auto CNT, int cls, bool need) {
    wait_vm<CNT>();
    pin(pollv);
    if (w == 0 && !match(pollv, need, tag)) spin(c.ws, poll_off + cls * 4096, need, tag);
  };
  auto iter = [&](auto V, auto ISV) {
    constexpr int v = V, b = v & 1, kd = SA.kind[v], ui = SA.idx[v], u = v - U0;
    if constexpr (v == SA.nhead) STAMP(3);
    if constexpr (v == SA.vg1) STAMP(4);
    if constexpr (u == 56) STAMP(5);
    if constexpr (u == 70) STAMP(8);
    if constexpr (u == 77) STAMP(9);
    if constexpr (u == U_CHK2) STAMP2(56);
    if constexpr (u == U_CHK3) STAMP2(58);
    if constexpr (u == U_KVPUB) STAMP2(50);
    if constexpr (u == U_H1PUB) STAMP2(52);
    if constexpr (u == U_H2PUB) STAMP2(54);
    if constexpr (v % 4 == 0) STAMP2(16 + v / 4);
    if constexpr (u == U_CHK1 && u != 56) check(IC<SA.base[v] - SA.base[U0 + U_POLL1] - 1>{}, 1, need_h);
    if constexpr (u == U_CHK2) check(IC<SA.base[v] - SA.base[U0 + U_POLL2] - 1>{}, 2, ORDER ? need_h : !need_h);
    if constexpr (u == U_CHK3) check(IC<SA.base[v] - SA.base[U0 + U_POLL3] - 1>{}, 2, ORDER ? !need_h : need_h);
    if constexpr (SA.bar[v]) sync_vm<SA.cnt[v]>();
    if constexpr (u == U_CHK2) STAMP2(57);
    if constexpr (u == U_CHK3) STAMP2(59);
    if constexpr (u == imin(U_KVPUB, U_H1PUB)) {
      pin(ev); if constexpr (G_TOUCH < 0) pin(c.tw);
      tag = tag_of(ev);
    }
    if constexpr (u == U_KVPUB) { publish(0); STAMP2(51); }
    if constexpr (u == U_H1PUB) { publish(1); STAMP2(53); }
    if constexpr (u == U_H2PUB) { publish(2); STAMP2(55); }
    if constexpr (v == G_TOUCH) td = touch(c.toff, c.rt);
    if constexpr (u == U_H1PUB) pin(td);
    if constexpr (u == U_POLL1) pollv = ld64_sc1(w == 0 ? poll_off : OOB, c.rws, 4096);
    if constexpr (u == U_POLL2 || u == U_POLL3) pollv = ld64_sc1(w == 0 ? poll_off : OOB, c.rws, 8192);
    if constexpr (v == SA.vkvst) {
#pragma unroll
      for (int s = 0; s < 2; ++s) {
        const u32 off = isv ? WS_V + T * KV_T + (s * 2 + hi) * 7168 + ((role - 7) * 64 + cg * 32 + r) * 16
                            : WS_K + T * KV_T + (role * 8 + cg * 4 + 2 * s) * 512 + lane16;
        st128(pk8(akvacc, s), c.rws, off, 0);
      }
    }
    if constexpr (v == SA.vg1 + EP1) st128(hp[0], c.rws, h_off, 0);
    if constexpr (v == SA.vg1 + 2 * EP1) st128(hp[1], c.rws, h_off, 1024);
    if constexpr (u == U_H2ST) {
#pragma unroll
      for (int t = 0; t < 2; ++t) {
#pragma unroll
        for (int j = 0; j < 8; j += 2) hp[t][j / 2] = pk(glu(g2[t][j], g2[t][8 + j]), glu(g2[t][j + 1], g2[t][9 + j]));
        st128(hp[t], c.rws, h_off, (56 + t) * 1024);
        SCHED();
      }
    }
    asm volatile("" ::: "memory");
    if constexpr (u == 56) STAMP2(44);
    if constexpr (u == 56 && U_CHK1 == 56) {
      check(IC<SA.base[v] + SA.nst[v] - SA.base[U0 + U_POLL1] - 1>{}, 1, need_h);
      asm volatile("s_barrier" ::: "memory");
      STAMP2(45);
    }
    constexpr bool ld_first = u >= 56 && ld_iter(item_of(u < 56 ? 0 : u - 56)) == v;
    constexpr int nld = SA.ldlo[v + 1] - SA.ldlo[v], nd = SA.cum[v + 1] - SA.cum[v];
    if constexpr (ld_first) static_for<SA.ldlo[v], SA.ldlo[v + 1]>(load_item);
    if constexpr (u >= 56) {
      wait_vm<SA.base[v] + SA.nst[v] + (ld_first ? nld : 0) - SA.pld[item_of(u - 56)] - 1>();
      pin(af[u - 56]);
      if constexpr (u == 56) STAMP2(46);
      if constexpr (u == 70) STAMP2(47);
      if constexpr (u == 77) STAMP2(48);
    }
    SCHED();
    static_for<0, 2>([&](auto I) {
      constexpr int i = I;
      if constexpr (kd == 0) { if constexpr (ISV) mfma_at(akvacc, xf[2 * ui + i], wf[b][i]); else mfma_at(akvacc, wf[b][i], xf[2 * ui + i]); }
      else if constexpr (ui < 28) mfma_at(g1[i], wf[b][i], xf[ui]);
      else if constexpr (ui < 56) mfma_at(g2[i], wf[b][i], xf[ui - 28]);
      else if constexpr (blk_of(ui - 56) < 28) mfma_at(ah[i], wf[b][i], af[ui - 56]);
      else mfma_at(ao[i], wf[b][i], af[ui - 56]);
      SCHED();
      read_w(IC<v + 2>{}, I);
      if constexpr (v >= SA.vg1 && v < SA.vg1 + 2 * EP1) {
        constexpr int t = (v - SA.vg1) / EP1, per = 4 / EP1, k0 = ((v - SA.vg1) % EP1) * per;
#pragma unroll
        for (int k = k0 + (i * per + 1) / 2; k < k0 + ((i + 1) * per + 1) / 2; ++k) {
          u32 hv = pk(glu(g1[t][2 * k], g1[t][8 + 2 * k]), glu(g1[t][2 * k + 1], g1[t][9 + 2 * k]));
          pin(hv);
          hp[t][k] = hv;
        }
      }
      if constexpr (i == 0 && !ld_first) static_for<SA.ldlo[v], SA.ldlo[v + 1]>(load_item);
      if constexpr (i == 0 && nd == 2) issueA<SA.cum[v]>(c);
      if constexpr (i == 1 && nd >= 1) issueA<SA.cum[v + 1] - 1>(c);
      SCHED();
    });
  };
  constexpr int vk0 = [] { int v = 0; while (SA.kind[v] != 0) ++v; return v; }();
  static_for<0, vk0>([&](auto V) { iter(V, IC<0>{}); });
  if (isv) static_for<vk0, SA.nhead>([&](auto V) { iter(V, IC<1>{}); });
  else static_for<vk0, SA.nhead>([&](auto V) { iter(V, IC<0>{}); });
  static_for<SA.nhead, NV>([&](auto V) { iter(V, IC<0>{}); });
  pin(omg);
  STAMP(6);
  const float om = __uint_as_float(omg);
  LAS char* red = sm + 4 * SLOT;
#pragma unroll
  for (int t = 0; t < 2; ++t)
#pragma unroll
    for (int a = 0; a < 4; ++a) {
      f32x4 v;
#pragma unroll
      for (int e = 0; e < 4; ++e) v[e] = ah[t][4 * a + e] * om + ao[t][4 * a + e];
      *(LAS f32x4*)(red + kq * 16384 + (2 * r + rt) * 256 + (((8 * t + 2 * a + hi) ^ (r & 15)) << 4)) = v;
    }
  asm volatile("s_waitcnt lgkmcnt(0)\n\ts_barrier" ::: "memory");
  STAMP(7);
  {
    const int row = w * 8 + (lane >> 3), c8 = lane & 7, m = (row >> 1) & 15;
    f32x4 s0 = {}, s1 = {};
#pragma unroll
    for (int k = 0; k < 4; ++k) {
      s0 += *(LAS f32x4*)(red + k * 16384 + row * 256 + (((2 * c8) ^ m) << 4));
      s1 += *(LAS f32x4*)(red + k * 16384 + row * 256 + (((2 * c8 + 1) ^ m) << 4));
    }
    u32x4 o = {pk(s0[0], s0[1]), pk(s0[2], s0[3]), pk(s1[0], s1[1]), pk(s1[2], s1[3])};
    u32x4* op = (u32x4*)((char*)out + (size_t)(R2 * 64 + row) * 896 + n2 * 128 + c8 * 16);
    *op = o;
  }
}

constexpr int PRO_B = 12;
constexpr int freedB(int g) { return 6 * ((g + 1) / 4); }
constexpr int cumB(int g) {
  int cc = PRO_B;
  for (int j = 0; j <= g; ++j) { int t = cc + 2, cap = freedB(j) + 18; t = t < cap ? t : cap; cc = t < 42 ? t : 42; }
  return cc;
}
constexpr int n_stB(int g) { return 0; }
constexpr int baseB(int g) { int t = PRO_B; for (int j = 0; j < g; ++j) t += n_stB(j) + cumB(j) - cumB(j - 1); return t; }
constexpr int pos_dmaB(int n) {
  if (n < PRO_B) return n;
  for (int g = 0; g < 28; ++g) if (n < cumB(g)) return baseB(g) + n_stB(g) + (n - cumB(g - 1));
  return -1000;
}
constexpr int sync_cntB(int g) {
  int n = baseB(g) - pos_dmaB(6 * (g / 4 + 2) - 1) - 1;
  return n;
}
template <int N> DI void issueB(const Ctx& c) {
  constexpr int kc = N / 6, j = N % 6;
  constexpr u32 dst = (N % 18) * SLOT;
  if constexpr (j < 2) dma(c.m0w + dst, c.vx, c.rx, j * 896 + kc * 128);
  else dma(c.m0w + dst, c.vkv, c.rqkv, kc * 64 * 2688 + (j - 2) * 128);
}

DI void type_b(Ctx& c, int w, int lane, int mt, int hf, const void* gw DIAGP) {
  const int r = lane & 31, hi = lane >> 5, blk = (lane >> 4) & 1, q = (lane & 15) >> 2, p = lane & 3;
  const int sp = ((p & 1) << 1) | (p >> 1);
  const int rg = w & 3, cg = w >> 2, T = mt * 4 + rg;
  LAS char* sm = c.sm;
  if (w == 0) dma(c.m0w + 18 * SLOT, lane * 16, mk(gw, 896), 0);
  u32x2 ev = ld64_sc1(w == 0 ? WS_E : OOB, c.rws, 0);
  static_for<2, PRO_B>([&](auto N) { issueB<N>(c); });
  const int R = 32 * (rg & 1) + r, xm = (R >> 1) & 7;
  int xa[4], aq[4];
#pragma unroll
  for (int s = 0; s < 4; ++s) xa[s] = (rg >> 1) * SLOT + R * 128 + (((2 * s + hi) ^ xm) << 4);
  const int h0 = 8 * hf + 4 * cg;
#pragma unroll
  for (int i = 0; i < 4; ++i) {
    const int col = 28 * (h0 + i) - 4 * (i & 1) - 192 * hf + 16 * blk + 4 * sp;
    aq[i] = (2 + (col >> 6)) * SLOT + (8 * hi + q) * 128 + (((col & 63) * 2) ^ ((q & 2) << 5));
  }
  const bool first = (mt == 0) && !(rg & 1);
  const int Tp = first ? T : ((rg & 1) ? T - 1 : T - 3);
  const u32 lane16 = lane * 16, lo16 = hi ? OOB : lane16;
  const u32 ob = WS_A2 + T * A2_T + (1792 + 28 * h0) * 64;
  const u32 omg_off = (hf == 0 && cg == 0 && hi == 0) ? r * 4 : OOB;
  const u32 poll_off = WS_FLAG + ((lane & 16) && mt > 0 ? mt - 1 : mt) * 256 + (lane & 15) * 8;
  const bool poll_need = (lane & 15) < 14;
  u32x4 xf[2], gf[2], wf[2][4];
  u32x4 kf[4][2], kp[2], vf[4][2], vp[4];
  f32x16 qa[4] = {};
  const int sr = (r & 19) | ((r & 4) << 1) | ((r & 8) >> 1);
  const u32 kp_off = (28 * (r >> 3) - 4 * ((r >> 3) & 1)) * 64 + (hi * 32 + 24 + (r & 7)) * 16;
  const u32 v_off = hi * 7168 + sr * 16;
  auto load_kv = [&](auto IT) {
    constexpr int it = IT;
    if constexpr (it < 2) kp[it] = ld128_sc1(kp_off, c.rws, WS_K + Tp * KV_T + 28 * h0 * 64 + it * 1024);
    else if constexpr (it < 10) {
      constexpr int i = (it - 2) / 2, k = it & 1;
      kf[i][k] = ld128_sc1(lane16, c.rws, WS_K + T * KV_T + (28 * (h0 + i) - 4 * (i & 1)) * 64 + k * 1024);
    } else {
      constexpr int i = (it - 10) / 3, k = (it - 10) % 3;
      const int wh = 28 * (h0 + i) - 4 * (i & 1);
      if constexpr (k == 0) vp[i] = ld128_sc1(v_off, c.rws, WS_V + Tp * KV_T + 14336 + wh * 16);
      else vf[i][k - 1] = ld128_sc1(v_off, c.rws, WS_V + T * KV_T + (k - 1) * 14336 + wh * 16);
    }
  };
  auto read_x = [&](auto G) {
    constexpr int g = G;
    if constexpr (g < 28) {
      xf[g & 1] = *(LAS u32x4*)(sm + xa[g % 4] + ((6 * (g / 4)) % 18) * SLOT);
      gf[g & 1] = *(LAS u32x4*)(sm + 18 * SLOT + g * 32 + hi * 16);
    }
  };
  auto read_w = [&](auto G, auto I) {
    constexpr int g = G;
    if constexpr (g < 28) wf[g & 1][I] = trfrag(sm + aq[I] + ((6 * (g / 4)) % 18) * SLOT + (g % 4) * 2048);
  };
  STAMP(1);
  sync_vm<PRO_B - 6>();
  if constexpr (G_TOUCH == -1) pin(c.tw);
  STAMP(2);
  read_x(IC<0>{});
  static_for<0, 4>([&](auto I) { read_w(IC<0>{}, I); });
  SCHED();
  static_for<0, 28>([&](auto G) {
    constexpr int g = G, b = g & 1;
    if constexpr (g % 4 == 3 && g < 27) { STAMP2(16 + 2 * (g / 4)); asm volatile("s_waitcnt lgkmcnt(0)" ::: "memory"); sync_vm<sync_cntB(g)>(); STAMP2(17 + 2 * (g / 4)); }
    SCHED();
    static_for<0, 4>([&](auto I) {
      constexpr int i = I;
      mfma_at(qa[i], wf[b][i], xf[b]);
      SCHED();
      if constexpr (i == 0) read_x(IC<g + 1>{});
      read_w(IC<g + 1>{}, I);
      if constexpr (i < 2) {
        wf[b][2][2 * i] = r == 28 ? gf[b][2 * i] : wf[b][2][2 * i];
        wf[b][2][2 * i + 1] = r == 28 ? gf[b][2 * i + 1] : wf[b][2][2 * i + 1];
        pin(wf[b][2]);
      }
      if constexpr (i >= 2 && cumB(g - 1) + i - 2 < cumB(g)) issueB<cumB(g - 1) + i - 2>(c);
      SCHED();
    });
  });
  STAMP(3);
  const float z = __uint_as_float(__builtin_amdgcn_permlane32_swap(__float_as_uint(qa[2][12]), __float_as_uint(qa[2][12]), false, false)[1]);
  const float gate = __builtin_amdgcn_rcpf(1.f + __builtin_amdgcn_exp2f(z * -1.4426950408889634f));
  const u32 omg = __float_as_uint(__builtin_amdgcn_rcpf(1.f + __builtin_amdgcn_exp2f(z * 1.4426950408889634f)));
  int ab[4];
  u32 msk[5][2];
#pragma unroll
  for (int b = 0; b < 4; ++b) ab[b] = (r >> 3) - ((r & 7) < 4 * hi + b ? 1 : 0);
#pragma unroll
  for (int a = 0; a < 5; ++a)
#pragma unroll
    for (int e = 0; e < 2; ++e) msk[a][e] = (ab[2 * e] == a - 1 ? 0xFFFFu : 0u) | (ab[2 * e + 1] == a - 1 ? 0xFFFF0000u : 0u);
  const float csc = 0.18898223650461362f * 1.4426950408889634f;
  __builtin_amdgcn_raw_buffer_store_b32(omg, c.rws, omg_off, WS_OMG + T * 128, SC1);
  asm volatile("" ::: "memory");
  u32 p01[4], p23[4];
  u32x4 qf[4][2];
  static_for<0, 4>([&](auto I) {
    constexpr int i = I;
    qf[i][0] = pk8(qa[i], 0); qf[i][1] = pk8(qa[i], 1);
    if (i & 1) { qf[i][0][0] = hi ? qf[i][0][0] : 0u; qf[i][0][1] = hi ? qf[i][0][1] : 0u; }
    else       { qf[i][1][2] = hi ? 0u : qf[i][1][2]; qf[i][1][3] = hi ? 0u : qf[i][1][3]; }
    pin(qf[i][0]); pin(qf[i][1]);
  });
#pragma unroll
  for (int a = 0; a < 5; ++a) { pin(msk[a][0]); pin(msk[a][1]); }
  pin(ev);
  const u64 tag = tag_of(ev);
  STAMP(8);
  if (w == 0) spin(c.ws, poll_off, poll_need, tag);
  asm volatile("s_barrier" ::: "memory");
  STAMP(9);
  static_for<0, 10>(load_kv);
  f32x16 so[2], sv[2];
  static_for<0, 5>([&](auto I) {
    constexpr int i = I;
    if constexpr (i < 4) {
      static_for<10 + 3 * i, 13 + 3 * i>(load_kv);
      wait_vm<9 + i>();
      if constexpr (i == 0) { pin(kp[0]); pin(kp[1]); STAMP(4); }
      pin(kf[i][0]); pin(kf[i][1]);
      so[i & 1] = mfma(kf[i][0], qf[i][0], f32x16{}); sv[i & 1] = mfma(kp[0], qf[i][0], f32x16{});
      mfma_at(so[i & 1], kf[i][1], qf[i][1]); mfma_at(sv[i & 1], kp[1], qf[i][1]);
    }
    if constexpr (i > 0) {
      constexpr int j = i - 1;
      float v[4];
#pragma unroll
      for (int b = 0; b < 4; ++b) {
        float t = first ? -1e30f : sv[j & 1][4 * j + b];
#pragma unroll
        for (int a = 0; a < 4; ++a) t = ab[b] == a ? so[j & 1][4 * a + b] : t;
        v[b] = t;
      }
      const float mx = xhalf_max(fmaxf(fmaxf(v[0], v[1]), fmaxf(v[2], v[3])));
#pragma unroll
      for (int b = 0; b < 4; ++b) v[b] = __builtin_amdgcn_exp2f((v[b] - mx) * csc);
      const float sc = gate * __builtin_amdgcn_rcpf(xhalf_sum((v[0] + v[1]) + (v[2] + v[3])));
      p01[j] = pk(v[0] * sc, v[1] * sc); p23[j] = pk(v[2] * sc, v[3] * sc);
      pin(p01[j]); pin(p23[j]);
    }
    SCHED();
  });
  STAMP(10);
  static_for<0, 2>([&](auto J) {
    constexpr int j = J;
    wait_vm<(j == 0 ? 6 : 4)>();
    u32x4 op[2][2];
#pragma unroll
    for (int e = 0; e < 2; ++e) {
      const int i = 2 * j + e;
      pin(vp[i]); pin(vf[i][0]); pin(vf[i][1]);
      f32x16 oa = {};
      oa = mfma(vp[i], u32x4{0u, 0u, p01[i] & msk[0][0], p23[i] & msk[0][1]}, oa);
      oa = mfma(vf[i][0], u32x4{p01[i] & msk[1][0], p23[i] & msk[1][1], p01[i] & msk[2][0], p23[i] & msk[2][1]}, oa);
      oa = mfma(vf[i][1], u32x4{p01[i] & msk[3][0], p23[i] & msk[3][1], p01[i] & msk[4][0], p23[i] & msk[4][1]}, oa);
      op[e][0] = pk8(oa, 0); op[e][1] = pk8(oa, 1);
    }
#pragma unroll
    for (int e = 0; e < 2; ++e) { auto t = __builtin_amdgcn_permlane32_swap(op[0][1][e], op[1][0][e], false, false); op[0][1][e] = t[0]; op[1][0][e] = t[1]; }
    const u32 o2 = ob + j * 3584;
    st128(op[0][0], c.rws, lane16, o2);
    st128(op[0][1], c.rws, lo16, o2 + 1024);
    st128(op[1][0], c.rws, lane16, o2 + 1536);
    st128(op[1][1], c.rws, lane16, o2 + 2560);
  });
  STAMP(5);
  sync_vm<0>();
  if (w == 0) __builtin_amdgcn_raw_buffer_store_b64(u32x2{(u32)tag, (u32)(tag >> 32)}, c.rws, lane == 0 ? (14 + hf) * 8 : OOB, WS_FLAG + 8192 + mt * 256, SC1);
  STAMP(6);
  if (mt == 0 && hf == 0 && w == 0) {
#pragma unroll 1
    for (int i = 0; i < 4; ++i) spin(c.ws, WS_FLAG + 8192 + (i * 4 + (lane >> 4)) * 256 + (lane & 15) * 8, true, tag);
    __builtin_amdgcn_raw_buffer_store_b64(u32x2{(u32)tag, (u32)(tag >> 32)}, c.rws, lane == 0 ? 0 : OOB, WS_E, SC1);
  }
}

__global__ __launch_bounds__(512, 2) void fused_kernel(const void* __restrict__ x, const void* __restrict__ wg, const void* __restrict__ wu,
                                                       const void* __restrict__ wd, const void* __restrict__ wo, const void* __restrict__ wqkv,
                                                       const void* __restrict__ gw, void* __restrict__ out, void* __restrict__ ws) {
  __shared__ __attribute__((aligned(1024))) char smem[163840];
  const int lane = threadIdx.x & 63, w = __builtin_amdgcn_readfirstlane(threadIdx.x >> 6);
  const void* tp = ((const void* const __attribute__((address_space(4)))*)__builtin_amdgcn_kernarg_segment_ptr())[w];
  const int b = blockIdx.x, xc = b & 7, ix = b >> 3;
  const int mq = xc >> 1, mt = mq * 4 + (ix & 3), rl = ix >> 2, rh = xc & 1;
  const bool isa = rl < 7;
  const int role = rh * 7 + rl;
  Ctx c;
  c.sm = (LAS char*)smem;
  c.m0w = (u32)(size_t)c.sm + w * 1024;
  c.rx = mk(x, 2048 * 896); c.rqkv = mk(wqkv, 448 * 2688); c.rwg = mk(wg, 448 * 3584); c.rwu = mk(wu, 448 * 3584);
  c.rwd = mk(wd, 1792 * 896); c.rwo = mk(wo, 448 * 896); c.rws = mk(ws, WS_END);
  c.ws = (const char*)ws;
  const int r8 = lane >> 3, c8 = lane & 7, R = 8 * w + r8;
  c.vx = (mt * 128 + 2 * R) * 896 + ((c8 ^ ((R >> 1) & 7)) << 4);
  const int sw = (c8 ^ ((r8 & 2) << 1)) << 4;
  const int isai = (rl - 7) >> 31;
  c.vkv = R * 2688 + ((896 + role * 128) & isai) + ((rh * 384) & ~isai) + sw;
  auto first3 = [&] { static_for<0, 2>([&](auto N) { issueA<N>(c); }); };
  if constexpr (G_TOUCH != -1) first3();
  {
    auto tab = [&](u64 t) { return (int)((t >> (8 * w)) & 255); };
    const int run = (lane * 37) >> 8, l = lane - 7 * run;
    const int gu = tab(0x010100ull), bb = tab(0x010000010100ull), stride = tab(0x1507071C1C00ull) * 128;
    const int row0 = tab(0x0208000000ull) * b + bb * (mq * 112 + ix * 4);
    const int n = tab(0x1C0E38383800ull) & isai;
    const u32 o = (row0 + (run >> gu)) * stride + ((run & gu) * 14 + bb * rh * 7 + tab(0x070000000000ull) + l) * 128;
    c.toff = lane < n ? o : OOB;
    c.rt = mk(tp, (448 << tab(0x02000000ull)) * stride);
    if constexpr (G_TOUCH == -1) c.tw = touch(c.toff, c.rt);
  }
  if constexpr (G_TOUCH == -1) first3();
  if (__builtin_expect(isa, 1)) {
    c.vg = R * 3584 + role * 128 + sw;
    c.vu = R * 3584 + role * 128 + (sw ^ 32);
    c.vw2 = R * 896 + rl * 128 + sw;
    type_a(c, w, lane, mt, role, xc * 4 + (ix & 3), rl, out DIAGA);
  } else {
    type_b(c, w, lane, mt, rh, gw DIAGA);
  }
}

extern "C" void kernel_launch(void* const* d_in, const int* in_sizes, int n_in, void* d_out, int out_size, void* d_ws, size_t ws_size,
                              hipStream_t stream) {
  fused_kernel<<<256, 512, 0, stream>>>(d_in[0], d_in[4], d_in[5], d_in[6], d_in[2], d_in[1], d_in[7], d_out, d_ws);
}
```

```cpp
#define TAIL 1
#include <hip/hip_runtime.h>
#include <hip/hip_bf16.h>

typedef unsigned long long u64;
typedef unsigned u32;
typedef __bf16 bf16x8 __attribute__((ext_vector_type(8)));
typedef __bf16 bf16x2 __attribute__((ext_vector_type(2)));
typedef short s16x4 __attribute__((ext_vector_type(4)));
typedef u32 u32x4 __attribute__((ext_vector_type(4)));
typedef u32 u32x2 __attribute__((ext_vector_type(2)));
typedef float f32x16 __attribute__((ext_vector_type(16)));
typedef float f32x4 __attribute__((ext_vector_type(4)));
typedef __amdgpu_buffer_rsrc_t rsrc_t;
#define LAS __attribute__((address_space(3)))
#define GAS __attribute__((address_space(1)))
#define DI __device__ __forceinline__
#define SCHED() __builtin_amdgcn_sched_barrier(0)
template <int V> using IC = std::integral_constant<int, V>;

constexpr u32 WS_E = 0;
constexpr u32 WS_FLAG = 4096;
constexpr u32 WS_OMG = 32768;
constexpr u32 WS_K = 65536;
constexpr u32 KV_T = 28672;
constexpr u32 WS_V = WS_K + 64 * KV_T;
constexpr u32 WS_A2 = WS_V + 64 * KV_T;
constexpr u32 A2_T = 143360;
constexpr u32 WS_END = WS_A2 + 64 * A2_T;
constexpr u32 OOB = 0xFFFFFF00u;
constexpr int SC1 = 16;
constexpr int SLOT = 8192;
constexpr int SPIN_CAP = 1 << 15;

DI rsrc_t mk(const void* p, u32 bytes) { return __builtin_amdgcn_make_buffer_rsrc((void*)p, (short)0, (int)bytes, 0x00020000); }
DI void dma(u32 m0v, u32 voff, rsrc_t rs, u32 soff) {
  asm volatile("s_mov_b32 m0, %0\n\ts_nop 0\n\tbuffer_load_dwordx4 %1, %2, %3 offen lds" ::"s"(m0v), "v"(voff), "s"(rs), "s"(soff) : "memory");
}
DI u32x4 ld128_sc1(u32 voff, rsrc_t rs, u32 soff) {
  u32x4 v;
  asm volatile("buffer_load_dwordx4 %0, %1, %2, %3 offen sc1" : "=v"(v) : "v"(voff), "s"(rs), "s"(soff) : "memory");
  return v;
}
DI u32x2 ld64_sc1(u32 voff, rsrc_t rs, u32 soff) {
  u32x2 v;
  asm volatile("buffer_load_dwordx2 %0, %1, %2, %3 offen sc1" : "=v"(v) : "v"(voff), "s"(rs), "s"(soff) : "memory");
  return v;
}
DI u32 ld32_sc1(u32 voff, rsrc_t rs, u32 soff) {
  u32 v;
  asm volatile("buffer_load_dword %0, %1, %2, %3 offen sc1" : "=v"(v) : "v"(voff), "s"(rs), "s"(soff) : "memory");
  return v;
}
DI u32 touch(u32 voff, rsrc_t rs) {
  u32 v;
  asm volatile("buffer_load_dword %0, %1, %2, 0 offen" : "=v"(v) : "v"(voff), "s"(rs) : "memory");
  return v;
}
DI void st128(u32x4 v, rsrc_t rs, u32 voff, u32 soff) {
  asm volatile("buffer_store_dwordx4 %0, %1, %2, %3 offen sc1\n\ts_nop 1" ::"v"(v), "v"(voff), "s"(rs), "s"(soff) : "memory");
}
constexpr int clampvm(int n) { return n > 63 ? 63 : n; }
template <int N> DI void wait_vm() { static_assert(N >= 0, ""); asm volatile("s_waitcnt vmcnt(%0)" ::"n"(clampvm(N)) : "memory"); }
template <class T> DI void pin(T& v) { asm volatile("" : "+v"(v)); }

DI u32x4 trfrag(LAS char* p) {
  s16x4 lo = __builtin_amdgcn_ds_read_tr16_b64_v4i16((LAS s16x4*)p);
  s16x4 hi = __builtin_amdgcn_ds_read_tr16_b64_v4i16((LAS s16x4*)(p + 512));
  u32x2 a = __builtin_bit_cast(u32x2, lo), b = __builtin_bit_cast(u32x2, hi);
  return u32x4{a.x, a.y, b.x, b.y};
}
DI f32x16 mfma(u32x4 a, u32x4 b, f32x16 c) {
  return __builtin_amdgcn_mfma_f32_32x32x16_bf16(__builtin_bit_cast(bf16x8, a), __builtin_bit_cast(bf16x8, b), c, 0, 0, 0);
}
DI void mfma_at(f32x16& c, u32x4 a, u32x4 b) { c = mfma(a, b, c); asm volatile("" : "+v"(c)); }
DI u32 pk(float a, float b) { bf16x2 c; c[0] = (__bf16)a; c[1] = (__bf16)b; return __builtin_bit_cast(u32, c); }
DI u32x4 pk8(const f32x16& v, int s) { return u32x4{pk(v[8 * s], v[8 * s + 1]), pk(v[8 * s + 2], v[8 * s + 3]), pk(v[8 * s + 4], v[8 * s + 5]), pk(v[8 * s + 6], v[8 * s + 7])}; }
DI float glu(float g, float u) {
  return u * __builtin_amdgcn_rcpf(1.f + __builtin_amdgcn_exp2f(g * -1.4426950408889634f)); }
DI float xhalf_max(float v) { auto r = __builtin_amdgcn_permlane32_swap(__float_as_uint(v), __float_as_uint(v), false, false); return fmaxf(__uint_as_float(r[0]), __uint_as_float(r[1])); }
DI float xhalf_sum(float v) { auto r = __builtin_amdgcn_permlane32_swap(__float_as_uint(v), __float_as_uint(v), false, false); return __uint_as_float(r[0]) + __uint_as_float(r[1]); }

DI void spin(const char* ws, u32 off, bool need, u64 tag) {
  const GAS u64* p = (const GAS u64*)(ws + off);
#pragma unroll 1
  for (int i = 0; i < SPIN_CAP; ++i) {
    u64 v = __hip_atomic_load(p, __ATOMIC_RELAXED, __HIP_MEMORY_SCOPE_AGENT);
    if (__all(!need || v == tag)) break;
    __builtin_amdgcn_s_sleep(2);
  }
  asm volatile("" ::: "memory");
}
DI bool match(u32x2 v, bool need, u64 tag) {
  return __all(!need || (v.x == (u32)tag && v.y == (u32)(tag >> 32))); }
DI u64 tag_of(u32x2 ev) {
  u64 e = ((u64)__builtin_amdgcn_readfirstlane(ev.y) << 32) | (u32)__builtin_amdgcn_readfirstlane(ev.x);
  return e * 6364136223846793005ull + 1442695040888963407ull;
}

#define STAMP(i)
#define DIAGP
#define DIAGA
#define STAMP2(i)
template <int N> DI void sync_vm() { static_assert(N >= 0, ""); asm volatile("s_waitcnt vmcnt(%0)\n\ts_barrier" ::"n"(clampvm(N)) : "memory"); }

template <int I, int N, class Fn> DI void static_for(Fn&& f) {
  if constexpr (I < N) { f(IC<I>{}); static_for<I + 1, N>(f); }
}

#define HEAD "KKGKKGKKGK"
#define HEAD_BARS 0x1011414145ull
#define HEAD_N 26
#define CFG 9,20,-2,22,40,57,52,56,64,68,70,76,2,6,6,2,6,6,2,0,1,2,16,16,0
constexpr int kCfg[] = {CFG};
constexpr int PRO_DMA = kCfg[0], HEAD_RATE = kCfg[1], G_TOUCH = kCfg[2], U_KVPUB = kCfg[3], U_H1PUB = kCfg[4], U_H2PUB = kCfg[5];
constexpr int U_POLL1 = kCfg[6], U_CHK1 = kCfg[7], U_POLL2 = kCfg[8], U_CHK2 = kCfg[9], U_POLL3 = kCfg[10], U_CHK3 = kCfg[11];
constexpr int EP1 = kCfg[12];
constexpr int ld_rel(int cl, int k) { int f = kCfg[13 + 3 * cl], a = kCfg[14 + 3 * cl], b = kCfg[15 + 3 * cl]; return k < f ? k / a : (f + a - 1) / a + (k - f) / b; }
#define ORDER 1
constexpr int N2 = ORDER ? 14 : 8, IT_OMG = ORDER ? 28 : 14;
constexpr int NV = 105, U0 = 14;
constexpr int U_H2ST = 56;
#define TPOS 3
constexpr int PRO_OPS = PRO_DMA + 1 + (G_TOUCH == -2);
constexpr int imin(int a, int b) { return a < b ? a : b; }
constexpr int item_of(int i) { return i < IT_OMG ? i : i + 1; }
constexpr int blk_of(int i) { return ORDER || i < 14 ? i : (i < 21 ? i + 14 : i - 7); }
constexpr int ld_iter(int it) { return U0 + (it < 14 ? U_CHK1 + ld_rel(0, it) : (it < 14 + N2 ? U_CHK2 + ld_rel(1, it - 14) : U_CHK3 + ld_rel(2, it - 14 - N2))); }
struct Sched {
  int nhead = 0, vkvst = 0, vg1 = 0;
  int xl[NV + 2] = {}, wl[NV + 2] = {};
  int kind[NV + 2] = {}, idx[NV + 2] = {}, lo[NV + 2] = {}, hi[NV + 2] = {};
  int pk[84] = {}, pi[84] = {}, pj[84] = {};
  bool bar[NV] = {};
  int cum[NV + 1] = {}, base[NV + 1] = {}, nst[NV] = {}, ldlo[NV + 1] = {}, cnt[NV] = {}, pdma[84] = {}, pld[36] = {}, freed[NV] = {};
  int cnt0 = 0; bool ok = true;
  constexpr Sched() {
    const char* h = HEAD;
    int v = 0, n = 0, k = 0, g = 0;
    int nx = 0;
    auto add = [&](int kd, int st, int nit, int nw) {
      const int first = n; int x = -1;
      if (st < 7 && st == nx) { x = n; for (int e = 0; e < 2; ++e) { pk[n] = 0; pi[n] = st; pj[n] = e; ++n; } ++nx; }
      for (int e = 0; e < nit; ++e) { kind[v] = kd; idx[v] = nit * st + e; lo[v] = first; hi[v] = n + nw - 1; xl[v] = x; wl[v] = n; ++v; }
      for (int e = 0; e < nw; ++e) { pk[n] = kd; pi[n] = st; pj[n] = kd ? e : 2; ++n; }
    };
    for (int i = 0; h[i]; ++i) { if (h[i] == 'K') { add(0, k++, 2, 1); nhead = v; } else { add(1, g++, 4, 2); if (g == 7) vg1 = v; } }
    if (k != 7 || g > 7) ok = false;
    vkvst = nhead + 1;
    while (g < 14) { add(1, g++, 4, 2); if (g == 7) vg1 = v; }
    for (int i = 0; i < 35; ++i) { kind[v] = 2; idx[v] = 56 + i; lo[v] = hi[v] = wl[v] = n; pk[n] = 2; pi[n] = i; ++n; ++v; }
    kind[NV] = kind[NV + 1] = 3; hi[NV] = hi[NV + 1] = 83;
    for (v = 0; v < NV; ++v) {
      const int u = v - U0, t = u + 2;
      bar[v] = v < 42 ? (((unsigned long long)(HEAD_BARS) >> v) & 1) : (t < 56 ? t % 8 == 0 : (t < 91 && (t - 56) % 5 == 0));
      if ((u == U_KVPUB || u == U_H1PUB || u == U_H2PUB || (u == U_CHK1 && u != 56) || u == U_CHK2 || u == U_CHK3)) bar[v] = true;
    }
    if (U0 + U_KVPUB <= vkvst || U0 + U_H1PUB <= vg1 + 2 * EP1) ok = false;
    int fr = 0, cc = PRO_DMA, cr = 0, it = 0;
    cum[0] = cc; base[0] = PRO_OPS;
    for (n = 0; n < PRO_DMA; ++n) pdma[n] = n + (G_TOUCH == -2 && n >= TPOS);
    for (v = 0; v < NV; ++v) {
      const int u = v - U0;
      if (bar[v] || (u == 56 && U_CHK1 == 56)) fr = lo[v];
      freed[v] = fr;
      const int pay = 2 * (v == vkvst) + (v == vg1 + EP1) + (v == vg1 + 2 * EP1) + 2 * (u == U_H2ST);
      nst[v] = pay + (v == G_TOUCH) + (u == U_POLL1) + (u == U_POLL2) + (u == U_POLL3);
      ldlo[v] = it;
      while (it < 36 && ld_iter(it) == v) { pld[it] = base[v] + nst[v] + (it - ldlo[v]); ++it; }
      cr += (v < HEAD_N ? HEAD_RATE : (u < 56 ? kCfg[22] : kCfg[23])) - kCfg[24] * pay;
      int t = imin(imin(cc + imin(cr > 0 ? cr / 16 : 0, 2), fr + 20), 84);
      cr -= 16 * (t - cc); if (cr > 16) cr = 16;
      for (n = cc; n < t; ++n) pdma[n] = base[v] + nst[v] + (it - ldlo[v]) + (n - cc);
      base[v + 1] = base[v] + nst[v] + (it - ldlo[v]) + (t - cc);
      cc = t; cum[v + 1] = cc;
    }
    ldlo[NV] = it;
    if (it != 36 || cc != 84) ok = false;
    for (v = -1; v < NV; ++v) {
      if (v >= 0 && !bar[v]) continue;
      int nx = v + 1; while (nx < NV && !bar[nx]) ++nx;
      const int need = hi[nx + 1], at = v < 0 ? 0 : v;
      if (need >= cum[at]) ok = false;
      int c = base[at] - pdma[need] - 1;
      const int u = v - U0;
      if (u == U_KVPUB) c = imin(c, base[v] - (base[vkvst] + nst[vkvst]));
      if (u == U_H1PUB) c = imin(c, base[v] - (base[vg1 + 2 * EP1] + nst[vg1 + 2 * EP1]));
      if (u == U_H2PUB) c = imin(c, base[v] - (base[U0 + U_H2ST] + nst[U0 + U_H2ST]));
      if (v < 0) cnt0 = c; else cnt[v] = c;
    }
    for (int i = 0; i < 35; ++i) if (ld_iter(item_of(i)) > U0 + 56 + i) ok = false;
  }
};
constexpr Sched SA{};
static_assert(SA.ok, "schedule: a piece or operand is used before it is requested, or HEAD is malformed");
static_assert(U_POLL1 < U_CHK1 && U_CHK1 < U_POLL2 && U_POLL2 < U_CHK2 && U_CHK2 < U_POLL3 && U_POLL3 < U_CHK3, "");
static_assert(U_KVPUB < U_CHK1 && U_H2PUB > U_H2ST && U_H1PUB < U_CHK1 && U_H2PUB < U_CHK2, "a workgroup publishes before it waits");
static_assert(SA.freed[NV - 1] >= 72, "the reduction scratch (slots 4..11 = pieces 64..71) must be free at the end");

struct Ctx {
  LAS char* sm; u32 m0w;
  rsrc_t rx, rqkv, rwg, rwu, rwd, rwo, rws;
  const char* ws;
  rsrc_t rt; u32 toff, tw;
  u32 vx, vkv, vg, vu, vw2;
};

template <int N> DI void issueA(const Ctx& c) {
  constexpr u32 dst = (N % 20) * SLOT;
  constexpr int k = SA.pk[N], st = SA.pi[N], j = SA.pj[N];
  if constexpr (k == 0) {
    if constexpr (j < 2) dma(c.m0w + dst, c.vx, c.rx, j * 896 + st * 128);
    else dma(c.m0w + dst, c.vkv, c.rqkv, st * 64 * 2688);
  } else if constexpr (k == 1) {
    if constexpr (j == 0) dma(c.m0w + dst, c.vg, c.rwg, (st % 7) * 64 * 3584 + (st / 7) * 1792);
    else dma(c.m0w + dst, c.vu, c.rwu, (st % 7) * 64 * 3584 + (st / 7) * 1792);
  } else if constexpr (blk_of(st) < 28) dma(c.m0w + dst, c.vw2, c.rwd, blk_of(st) * 64 * 896);
  else dma(c.m0w + dst, c.vw2, c.rwo, (blk_of(st) - 28) * 64 * 896);
}

DI void type_a(Ctx& c, int w, int lane, int mt, int role, int R2, int n2, void* out DIAGP) {
  const int r = lane & 31, hi = lane >> 5, blk = (lane >> 4) & 1, q = (lane & 15) >> 2, p = lane & 3;
  const int sp = ((p & 1) << 1) | (p >> 1);
  const int rg = w & 3, cg = w >> 2, T = mt * 4 + rg;
  const bool isv = role >= 7;
  LAS char* sm = c.sm;
  u32 td = 0;
  static_for<2, TPOS>([&](auto N) { issueA<N>(c); });
  if constexpr (G_TOUCH == -2) c.tw = touch(c.toff, c.rt);
  static_for<TPOS, PRO_DMA>([&](auto N) { issueA<N>(c); });
  u32x2 ev = ld64_sc1(w == 0 ? WS_E : OOB, c.rws, 0);
  const int R = 32 * (rg & 1) + r, xm = (R >> 1) & 7;
  int xa[4];
#pragma unroll
  for (int s = 0; s < 4; ++s) xa[s] = (rg >> 1) * SLOT + R * 128 + (((2 * s + hi) ^ xm) << 4);
  const int rowb = (8 * hi + q) * 128, q2 = q & 2;
  const int akv = rowb + (((2 * cg + blk) ^ q2) << 5) + (isv ? p : sp) * 8;
  int agu[2];
#pragma unroll
  for (int i = 0; i < 2; ++i) agu[i] = blk * SLOT + rowb + (((2 * cg + i) ^ q2 ^ blk) << 5) + sp * 8;
  int aguw[2][3];
#pragma unroll
  for (int i = 0; i < 2; ++i)
#pragma unroll
    for (int k = 0; k < 3; ++k) { aguw[i][k] = agu[i] + k * 65536; pin(aguw[i][k]); }
  const int rt = w & 1, kq = w >> 1;
  int aw2[2], aw2w[2][3];
#pragma unroll
  for (int i = 0; i < 2; ++i) aw2[i] = kq * 2048 + rowb + (((2 * i + blk) ^ q2) << 5) + (TAIL ? sp : p) * 8;
  const int T2 = (R2 >> 1) * 4 + 2 * rt + (R2 & 1);

  u32x4 xf[28], wf[2][2], af[35], hp[2];
  f32x16 akvacc = {}, g1[2] = {}, g2[2] = {}, ah[2] = {}, ao[2] = {};
  u32x2 pollv; u32 omg; u64 tag;
  const u32 lane16 = lane * 16;
  const u32 poll_off = WS_FLAG + (R2 >> 1) * 256 + (lane & 15) * 8;
  const bool need_h = (lane & 15) < 14;
  const u32 a2_off = WS_A2 + T2 * A2_T + kq * 1024 + lane16;
  const u32 h_off = WS_A2 + T * A2_T + (role * 4 + 2 * cg) * 1024 + lane16;

  auto read_w = [&](auto V, auto I) {
    constexpr int v = V, i = I, k = SA.kind[v], u = SA.idx[v], sl = SA.wl[v] % 20;
    if constexpr (k < 2 && SA.xl[v] >= 0 && (k == 0 || i == 0)) {
      constexpr int ks = k == 0 ? 2 * u + i : u, xs = SA.xl[v] % 20;
      const int wrap = xs == 19 ? (rg >> 1) * (-20 * SLOT) : 0;
      xf[ks] = *(LAS u32x4*)(sm + xa[ks % 4] + wrap + xs * SLOT);
    }
    if constexpr (k == 0) {
      wf[v & 1][i] = trfrag(sm + akv + sl * SLOT + ((2 * u + i) % 4) * 2048);
    } else if constexpr (k == 1) {
      const int wrap = sl == 19 ? blk * (-20 * SLOT) : 0;
      wf[v & 1][i] = trfrag(sm + aguw[i][sl / 8] + wrap + (sl % 8) * SLOT + (u % 4) * 2048);
    } else if constexpr (k == 2) {
      if constexpr (u == 56) {
#pragma unroll
        for (int e = 0; e < 3; ++e) { aw2w[i][e] = aw2[i] + e * 65536; pin(aw2w[i][e]); }
      }
      wf[v & 1][i] = trfrag(sm + aw2w[i][sl / 8] + (sl % 8) * SLOT);
    }
  };
  auto publish = [&](int cls) {
    if (w == 0) __builtin_amdgcn_raw_buffer_store_b64(u32x2{(u32)tag, (u32)(tag >> 32)}, c.rws, lane == 0 ? role * 8 : OOB, WS_FLAG + cls * 4096 + mt * 256, SC1);
  };
  auto load_item = [&](auto IT) {
    constexpr int it = IT;
    if constexpr (it == IT_OMG) omg = ld32_sc1(r * 4, c.rws, WS_OMG + T2 * 128);
    else { constexpr int i = it < IT_OMG ? it : it - 1; af[i] = ld128_sc1(a2_off, c.rws, blk_of(i) * 4096); }
  };

  STAMP(1);
  sync_vm<SA.cnt0>();
  STAMP(2);
  static_for<0, 4>([&](auto S) { read_w(IC<S / 2>{}, IC<S % 2>{}); });
  SCHED();
  auto check = [&](auto CNT, int cls, bool need) {
    wait_vm<CNT>();
    pin(pollv);
    if (w == 0 && !match(pollv, need, tag)) spin(c.ws, poll_off + cls * 4096, need, tag);
  };
  auto iter = [&](auto V, auto ISV) {
    constexpr int v = V, b = v & 1, kd = SA.kind[v], ui = SA.idx[v], u = v - U0;
    if constexpr (v == SA.nhead) STAMP(3);
    if constexpr (v == SA.vg1) STAMP(4);
    if constexpr (u == 56) STAMP(5);
    if constexpr (u == 70) STAMP(8);
    if constexpr (u == 77) STAMP(9);
    if constexpr (u == U_CHK2) STAMP2(56);
    if constexpr (u == U_CHK3) STAMP2(58);
    if constexpr (u == U_KVPUB) STAMP2(50);
    if constexpr (u == U_H1PUB) STAMP2(52);
    if constexpr (u == U_H2PUB) STAMP2(54);
    if constexpr (v % 4 == 0) STAMP2(16 + v / 4);
    if constexpr (u == U_CHK1 && u != 56) check(IC<SA.base[v] - SA.base[U0 + U_POLL1] - 1>{}, 1, need_h);
    if constexpr (u == U_CHK2) check(IC<SA.base[v] - SA.base[U0 + U_POLL2] - 1>{}, 2, ORDER ? need_h : !need_h);
    if constexpr (u == U_CHK3) check(IC<SA.base[v] - SA.base[U0 + U_POLL3] - 1>{}, 2, ORDER ? !need_h : need_h);
    if constexpr (SA.bar[v]) sync_vm<SA.cnt[v]>();
    if constexpr (u == U_CHK2) STAMP2(57);
    if constexpr (u == U_CHK3) STAMP2(59);
    if constexpr (u == imin(U_KVPUB, U_H1PUB)) {
      pin(ev); if constexpr (G_TOUCH < 0) pin(c.tw);
      tag = tag_of(ev);
    }
    if constexpr (u == U_KVPUB) { publish(0); STAMP2(51); }
    if constexpr (u == U_H1PUB) { publish(1); STAMP2(53); }
    if constexpr (u == U_H2PUB) { publish(2); STAMP2(55); }
    if constexpr (v == G_TOUCH) td = touch(c.toff, c.rt);
    if constexpr (u == U_H1PUB) pin(td);
    if constexpr (u == U_POLL1) pollv = ld64_sc1(w == 0 ? poll_off : OOB, c.rws, 4096);
    if constexpr (u == U_POLL2 || u == U_POLL3) pollv = ld64_sc1(w == 0 ? poll_off : OOB, c.rws, 8192);
    if constexpr (v == SA.vkvst) {
#pragma unroll
      for (int s = 0; s < 2; ++s) {
        const u32 off = isv ? WS_V + T * KV_T + (s * 2 + hi) * 7168 + ((role - 7) * 64 + cg * 32 + r) * 16
                            : WS_K + T * KV_T + (role * 8 + cg * 4 + 2 * s) * 512 + lane16;
        st128(pk8(akvacc, s), c.rws, off, 0);
      }
    }
    if constexpr (v == SA.vg1 + EP1) st128(hp[0], c.rws, h_off, 0);
    if constexpr (v == SA.vg1 + 2 * EP1) st128(hp[1], c.rws, h_off, 1024);
    if constexpr (u == U_H2ST) {
#pragma unroll
      for (int t = 0; t < 2; ++t) {
#pragma unroll
        for (int j = 0; j < 8; j += 2) hp[t][j / 2] = pk(glu(g2[t][j], g2[t][8 + j]), glu(g2[t][j + 1], g2[t][9 + j]));
        st128(hp[t], c.rws, h_off, (56 + t) * 1024);
        SCHED();
      }
    }
    asm volatile("" ::: "memory");
    if constexpr (u == 56) STAMP2(44);
    if constexpr (u == 56 && U_CHK1 == 56) {
      check(IC<SA.base[v] + SA.nst[v] - SA.base[U0 + U_POLL1] - 1>{}, 1, need_h);
      asm volatile("s_barrier" ::: "memory");
      STAMP2(45);
    }
    constexpr bool ld_first = u >= 56 && ld_iter(item_of(u < 56 ? 0 : u - 56)) == v;
    constexpr int nld = SA.ldlo[v + 1] - SA.ldlo[v], nd = SA.cum[v + 1] - SA.cum[v];
    if constexpr (ld_first) static_for<SA.ldlo[v], SA.ldlo[v + 1]>(load_item);
    if constexpr (u >= 56) {
      wait_vm<SA.base[v] + SA.nst[v] + (ld_first ? nld : 0) - SA.pld[item_of(u - 56)] - 1>();
      pin(af[u - 56]);
      if constexpr (u == 56) STAMP2(46);
      if constexpr (u == 70) STAMP2(47);
      if constexpr (u == 77) STAMP2(48);
    }
    SCHED();
    static_for<0, 2>([&](auto I) {
      constexpr int i = I;
      if constexpr (kd == 0) { if constexpr (ISV) mfma_at(akvacc, xf[2 * ui + i], wf[b][i]); else mfma_at(akvacc, wf[b][i], xf[2 * ui + i]); }
      else if constexpr (ui < 28) mfma_at(g1[i], wf[b][i], xf[ui]);
      else if constexpr (ui < 56) mfma_at(g2[i], wf[b][i], xf[ui - 28]);
      else if constexpr (blk_of(ui - 56) < 28) mfma_at(ah[i], wf[b][i], af[ui - 56]);
      else mfma_at(ao[i], wf[b][i], af[ui - 56]);
      SCHED();
      read_w(IC<v + 2>{}, I);
      if constexpr (v >= SA.vg1 && v < SA.vg1 + 2 * EP1) {
        constexpr int t = (v - SA.vg1) / EP1, per = 4 / EP1, k0 = ((v - SA.vg1) % EP1) * per;
#pragma unroll
        for (int k = k0 + (i * per + 1) / 2; k < k0 + ((i + 1) * per + 1) / 2; ++k) {
          u32 hv = pk(glu(g1[t][2 * k], g1[t][8 + 2 * k]), glu(g1[t][2 * k + 1], g1[t][9 + 2 * k]));
          pin(hv);
          hp[t][k] = hv;
        }
      }
      if constexpr (i == 0 && !ld_first) static_for<SA.ldlo[v], SA.ldlo[v + 1]>(load_item);
      if constexpr (i == 0 && nd == 2) issueA<SA.cum[v]>(c);
      if constexpr (i == 1 && nd >= 1) issueA<SA.cum[v + 1] - 1>(c);
      SCHED();
    });
  };
  constexpr int vk0 = [] { int v = 0; while (SA.kind[v] != 0) ++v; return v; }();
  static_for<0, vk0>([&](auto V) { iter(V, IC<0>{}); });
  if (isv) static_for<vk0, SA.nhead>([&](auto V) { iter(V, IC<1>{}); });
  else static_for<vk0, SA.nhead>([&](auto V) { iter(V, IC<0>{}); });
  static_for<SA.nhead, NV>([&](auto V) { iter(V, IC<0>{}); });
  pin(omg);
  STAMP(6);
  const float om = __uint_as_float(omg);
  LAS char* red = sm + 4 * SLOT;
  {
    f32x4 own[2];
#pragma unroll
    for (int gq = 0; gq < 4; ++gq)
#pragma unroll
      for (int e2 = 0; e2 < 2; ++e2) {
        f32x4 v;
#pragma unroll
        for (int e = 0; e < 4; ++e) v[e] = ah[gq >> 1][8 * (gq & 1) + 4 * e2 + e] * om + ao[gq >> 1][8 * (gq & 1) + 4 * e2 + e];
        if (gq == kq) own[e2] = v;
        else *(LAS f32x4*)(red + (((gq * 2 + rt) * 4 + kq) * 2 + e2) * 1024 + lane16) = v;
      }
    asm volatile("s_waitcnt lgkmcnt(0)\n\ts_barrier" ::: "memory");
    STAMP(7);
#pragma unroll
    for (int k = 1; k < 4; ++k)
#pragma unroll
      for (int e2 = 0; e2 < 2; ++e2) own[e2] += *(LAS f32x4*)(red + (((kq * 2 + rt) * 4 + ((kq + k) & 3)) * 2 + e2) * 1024 + lane16);
    u32x4 o = {pk(own[0][0], own[0][1]), pk(own[0][2], own[0][3]), pk(own[1][0], own[1][1]), pk(own[1][2], own[1][3])};
    u32x4* op = (u32x4*)((char*)out + (size_t)(R2 * 64 + 2 * r + rt) * 896 + n2 * 128 + kq * 32 + hi * 16);
    *op = o;
  }
}

constexpr int PRO_B = 12;
constexpr int freedB(int g) { return 6 * ((g + 1) / 4); }
constexpr int cumB(int g) {
  int cc = PRO_B;
  for (int j = 0; j <= g; ++j) { int t = cc + 2, cap = freedB(j) + 18; t = t < cap ? t : cap; cc = t < 42 ? t : 42; }
  return cc;
}
constexpr int n_stB(int g) { return 0; }
constexpr int baseB(int g) { int t = PRO_B; for (int j = 0; j < g; ++j) t += n_stB(j) + cumB(j) - cumB(j - 1); return t; }
constexpr int pos_dmaB(int n) {
  if (n < PRO_B) return n;
  for (int g = 0; g < 28; ++g) if (n < cumB(g)) return baseB(g) + n_stB(g) + (n - cumB(g - 1));
  return -1000;
}
constexpr int sync_cntB(int g) {
  int n = baseB(g) - pos_dmaB(6 * (g / 4 + 2) - 1) - 1;
  return n;
}
template <int N> DI void issueB(const Ctx& c) {
  constexpr int kc = N / 6, j = N % 6;
  constexpr u32 dst = (N % 18) * SLOT;
  if constexpr (j < 2) dma(c.m0w + dst, c.vx, c.rx, j * 896 + kc * 128);
  else dma(c.m0w + dst, c.vkv, c.rqkv, kc * 64 * 2688 + (j - 2) * 128);
}

DI void type_b(Ctx& c, int w, int lane, int mt, int hf, const void* gw DIAGP) {
  const int r = lane & 31, hi = lane >> 5, blk = (lane >> 4) & 1, q = (lane & 15) >> 2, p = lane & 3;
  const int sp = ((p & 1) << 1) | (p >> 1);
  const int rg = w & 3, cg = w >> 2, T = mt * 4 + rg;
  LAS char* sm = c.sm;
  if (w == 0) dma(c.m0w + 18 * SLOT, lane * 16, mk(gw, 896), 0);
  u32x2 ev = ld64_sc1(w == 0 ? WS_E : OOB, c.rws, 0);
  static_for<2, PRO_B>([&](auto N) { issueB<N>(c); });
  const int R = 32 * (rg & 1) + r, xm = (R >> 1) & 7;
  int xa[4], aq[4];
#pragma unroll
  for (int s = 0; s < 4; ++s) xa[s] = (rg >> 1) * SLOT + R * 128 + (((2 * s + hi) ^ xm) << 4);
  const int h0 = 8 * hf + 4 * cg;
#pragma unroll
  for (int i = 0; i < 4; ++i) {
    const int col = 28 * (h0 + i) - 4 * (i & 1) - 192 * hf + 16 * blk + 4 * sp;
    aq[i] = (2 + (col >> 6)) * SLOT + (8 * hi + q) * 128 + (((col & 63) * 2) ^ ((q & 2) << 5));
  }
  const bool first = (mt == 0) && !(rg & 1);
  const int Tp = first ? T : ((rg & 1) ? T - 1 : T - 3);
  const u32 lane16 = lane * 16, lo16 = hi ? OOB : lane16;
  const u32 ob = WS_A2 + T * A2_T + (1792 + 28 * h0) * 64;
  const u32 omg_off = (hf == 0 && cg == 0 && hi == 0) ? r * 4 : OOB;
  const u32 poll_off = WS_FLAG + ((lane & 16) && mt > 0 ? mt - 1 : mt) * 256 + (lane & 15) * 8;
  const bool poll_need = (lane & 15) < 14;
  u32x4 xf[2], gf[2], wf[2][4];
  u32x4 kf[4][2], kp[2], vf[4][2], vp[4];
  f32x16 qa[4] = {};
  const int sr = (r & 19) | ((r & 4) << 1) | ((r & 8) >> 1);
  const u32 kp_off = (28 * (r >> 3) - 4 * ((r >> 3) & 1)) * 64 + (hi * 32 + 24 + (r & 7)) * 16;
  const u32 v_off = hi * 7168 + sr * 16;
  auto load_kv = [&](auto IT) {
    constexpr int it = IT;
    if constexpr (it < 2) kp[it] = ld128_sc1(kp_off, c.rws, WS_K + Tp * KV_T + 28 * h0 * 64 + it * 1024);
    else if constexpr (it < 10) {
      constexpr int i = (it - 2) / 2, k = it & 1;
      kf[i][k] = ld128_sc1(lane16, c.rws, WS_K + T * KV_T + (28 * (h0 + i) - 4 * (i & 1)) * 64 + k * 1024);
    } else {
      constexpr int i = (it - 10) / 3, k = (it - 10) % 3;
      const int wh = 28 * (h0 + i) - 4 * (i & 1);
      if constexpr (k == 0) vp[i] = ld128_sc1(v_off, c.rws, WS_V + Tp * KV_T + 14336 + wh * 16);
      else vf[i][k - 1] = ld128_sc1(v_off, c.rws, WS_V + T * KV_T + (k - 1) * 14336 + wh * 16);
    }
  };
  auto read_x = [&](auto G) {
    constexpr int g = G;
    if constexpr (g < 28) {
      xf[g & 1] = *(LAS u32x4*)(sm + xa[g % 4] + ((6 * (g / 4)) % 18) * SLOT);
      gf[g & 1] = *(LAS u32x4*)(sm + 18 * SLOT + g * 32 + hi * 16);
    }
  };
  auto read_w = [&](auto G, auto I) {
    constexpr int g = G;
    if constexpr (g < 28) wf[g & 1][I] = trfrag(sm + aq[I] + ((6 * (g / 4)) % 18) * SLOT + (g % 4) * 2048);
  };
  STAMP(1);
  sync_vm<PRO_B - 6>();
  if constexpr (G_TOUCH == -1) pin(c.tw);
  STAMP(2);
  read_x(IC<0>{});
  static_for<0, 4>([&](auto I) { read_w(IC<0>{}, I); });
  SCHED();
  static_for<0, 28>([&](auto G) {
    constexpr int g = G, b = g & 1;
    if constexpr (g % 4 == 3 && g < 27) { STAMP2(16 + 2 * (g / 4)); asm volatile("s_waitcnt lgkmcnt(0)" ::: "memory"); sync_vm<sync_cntB(g)>(); STAMP2(17 + 2 * (g / 4)); }
    SCHED();
    static_for<0, 4>([&](auto I) {
      constexpr int i = I;
      mfma_at(qa[i], wf[b][i], xf[b]);
      SCHED();
      if constexpr (i == 0) read_x(IC<g + 1>{});
      read_w(IC<g + 1>{}, I);
      if constexpr (i < 2) {
        wf[b][2][2 * i] = r == 28 ? gf[b][2 * i] : wf[b][2][2 * i];
        wf[b][2][2 * i + 1] = r == 28 ? gf[b][2 * i + 1] : wf[b][2][2 * i + 1];
        pin(wf[b][2]);
      }
      if constexpr (i >= 2 && cumB(g - 1) + i - 2 < cumB(g)) issueB<cumB(g - 1) + i - 2>(c);
      SCHED();
    });
  });
  STAMP(3);
  const float z = __uint_as_float(__builtin_amdgcn_permlane32_swap(__float_as_uint(qa[2][12]), __float_as_uint(qa[2][12]), false, false)[1]);
  const float gate = __builtin_amdgcn_rcpf(1.f + __builtin_amdgcn_exp2f(z * -1.4426950408889634f));
  const u32 omg = __float_as_uint(__builtin_amdgcn_rcpf(1.f + __builtin_amdgcn_exp2f(z * 1.4426950408889634f)));
  int ab[4];
  u32 msk[5][2];
#pragma unroll
  for (int b = 0; b < 4; ++b) ab[b] = (r >> 3) - ((r & 7) < 4 * hi + b ? 1 : 0);
#pragma unroll
  for (int a = 0; a < 5; ++a)
#pragma unroll
    for (int e = 0; e < 2; ++e) msk[a][e] = (ab[2 * e] == a - 1 ? 0xFFFFu : 0u) | (ab[2 * e + 1] == a - 1 ? 0xFFFF0000u : 0u);
  const float csc = 0.18898223650461362f * 1.4426950408889634f;
  __builtin_amdgcn_raw_buffer_store_b32(omg, c.rws, omg_off, WS_OMG + T * 128, SC1);
  asm volatile("" ::: "memory");
  u32 p01[4], p23[4];
  u32x4 qf[4][2];
  static_for<0, 4>([&](auto I) {
    constexpr int i = I;
    qf[i][0] = pk8(qa[i], 0); qf[i][1] = pk8(qa[i], 1);
    if (i & 1) { qf[i][0][0] = hi ? qf[i][0][0] : 0u; qf[i][0][1] = hi ? qf[i][0][1] : 0u; }
    else       { qf[i][1][2] = hi ? 0u : qf[i][1][2]; qf[i][1][3] = hi ? 0u : qf[i][1][3]; }
    pin(qf[i][0]); pin(qf[i][1]);
  });
#pragma unroll
  for (int a = 0; a < 5; ++a) { pin(msk[a][0]); pin(msk[a][1]); }
  pin(ev);
  const u64 tag = tag_of(ev);
  STAMP(8);
  if (w == 0) spin(c.ws, poll_off, poll_need, tag);
  asm volatile("s_barrier" ::: "memory");
  STAMP(9);
  static_for<0, 10>(load_kv);
  f32x16 so[2], sv[2];
  static_for<0, 5>([&](auto I) {
    constexpr int i = I;
    if constexpr (i < 4) {
      static_for<10 + 3 * i, 13 + 3 * i>(load_kv);
      wait_vm<9 + i>();
      if constexpr (i == 0) { pin(kp[0]); pin(kp[1]); STAMP(4); }
      pin(kf[i][0]); pin(kf[i][1]);
      so[i & 1] = mfma(kf[i][0], qf[i][0], f32x16{}); sv[i & 1] = mfma(kp[0], qf[i][0], f32x16{});
      mfma_at(so[i & 1], kf[i][1], qf[i][1]); mfma_at(sv[i & 1], kp[1], qf[i][1]);
    }
    if constexpr (i > 0) {
      constexpr int j = i - 1;
      float v[4];
#pragma unroll
      for (int b = 0; b < 4; ++b) {
        float t = first ? -1e30f : sv[j & 1][4 * j + b];
#pragma unroll
        for (int a = 0; a < 4; ++a) t = ab[b] == a ? so[j & 1][4 * a + b] : t;
        v[b] = t;
      }
      const float mx = xhalf_max(fmaxf(fmaxf(v[0], v[1]), fmaxf(v[2], v[3])));
#pragma unroll
      for (int b = 0; b < 4; ++b) v[b] = __builtin_amdgcn_exp2f((v[b] - mx) * csc);
      const float sc = gate * __builtin_amdgcn_rcpf(xhalf_sum((v[0] + v[1]) + (v[2] + v[3])));
      p01[j] = pk(v[0] * sc, v[1] * sc); p23[j] = pk(v[2] * sc, v[3] * sc);
      pin(p01[j]); pin(p23[j]);
    }
    SCHED();
  });
  STAMP(10);
  static_for<0, 2>([&](auto J) {
    constexpr int j = J;
    wait_vm<(j == 0 ? 6 : 4)>();
    u32x4 op[2][2];
#pragma unroll
    for (int e = 0; e < 2; ++e) {
      const int i = 2 * j + e;
      pin(vp[i]); pin(vf[i][0]); pin(vf[i][1]);
      f32x16 oa = {};
      oa = mfma(vp[i], u32x4{0u, 0u, p01[i] & msk[0][0], p23[i] & msk[0][1]}, oa);
      oa = mfma(vf[i][0], u32x4{p01[i] & msk[1][0], p23[i] & msk[1][1], p01[i] & msk[2][0], p23[i] & msk[2][1]}, oa);
      oa = mfma(vf[i][1], u32x4{p01[i] & msk[3][0], p23[i] & msk[3][1], p01[i] & msk[4][0], p23[i] & msk[4][1]}, oa);
      op[e][0] = pk8(oa, 0); op[e][1] = pk8(oa, 1);
    }
#pragma unroll
    for (int e = 0; e < 2; ++e) { auto t = __builtin_amdgcn_permlane32_swap(op[0][1][e], op[1][0][e], false, false); op[0][1][e] = t[0]; op[1][0][e] = t[1]; }
    const u32 o2 = ob + j * 3584;
    st128(op[0][0], c.rws, lane16, o2);
    st128(op[0][1], c.rws, lo16, o2 + 1024);
    st128(op[1][0], c.rws, lane16, o2 + 1536);
    st128(op[1][1], c.rws, lane16, o2 + 2560);
  });
  STAMP(5);
  sync_vm<0>();
  if (w == 0) __builtin_amdgcn_raw_buffer_store_b64(u32x2{(u32)tag, (u32)(tag >> 32)}, c.rws, lane == 0 ? (14 + hf) * 8 : OOB, WS_FLAG + 8192 + mt * 256, SC1);
  STAMP(6);
  if (mt == 0 && hf == 0 && w == 0) {
#pragma unroll 1
    for (int i = 0; i < 4; ++i) spin(c.ws, WS_FLAG + 8192 + (i * 4 + (lane >> 4)) * 256 + (lane & 15) * 8, true, tag);
    __builtin_amdgcn_raw_buffer_store_b64(u32x2{(u32)tag, (u32)(tag >> 32)}, c.rws, lane == 0 ? 0 : OOB, WS_E, SC1);
  }
}

__global__ __launch_bounds__(512, 2) void fused_kernel(const void* __restrict__ x, const void* __restrict__ wg, const void* __restrict__ wu,
                                                       const void* __restrict__ wd, const void* __restrict__ wo, const void* __restrict__ wqkv,
                                                       const void* __restrict__ gw, void* __restrict__ out, void* __restrict__ ws) {
  __shared__ __attribute__((aligned(1024))) char smem[163840];
  const int lane = threadIdx.x & 63, w = __builtin_amdgcn_readfirstlane(threadIdx.x >> 6);
  const void* tp = ((const void* const __attribute__((address_space(4)))*)__builtin_amdgcn_kernarg_segment_ptr())[w];
  const int b = blockIdx.x, xc = b & 7, ix = b >> 3;
  const int mq = xc >> 1, mt = mq * 4 + (ix & 3), rl = ix >> 2, rh = xc & 1;
  const bool isa = rl < 7;
  const int role = rh * 7 + rl;
  Ctx c;
  c.sm = (LAS char*)smem;
  c.m0w = (u32)(size_t)c.sm + w * 1024;
  c.rx = mk(x, 2048 * 896); c.rqkv = mk(wqkv, 448 * 2688); c.rwg = mk(wg, 448 * 3584); c.rwu = mk(wu, 448 * 3584);
  c.rwd = mk(wd, 1792 * 896); c.rwo = mk(wo, 448 * 896); c.rws = mk(ws, WS_END);
  c.ws = (const char*)ws;
  const int r8 = lane >> 3, c8 = lane & 7, R = 8 * w + r8;
  c.vx = (mt * 128 + 2 * R) * 896 + ((c8 ^ ((R >> 1) & 7)) << 4);
  const int sw = (c8 ^ ((r8 & 2) << 1)) << 4;
  const int isai = (rl - 7) >> 31;
  c.vkv = R * 2688 + ((896 + role * 128) & isai) + ((rh * 384) & ~isai) + sw;
  auto first3 = [&] { static_for<0, 2>([&](auto N) { issueA<N>(c); }); };
  if constexpr (G_TOUCH != -1) first3();
  {
    auto tab = [&](u64 t) { return (int)((t >> (8 * w)) & 255); };
    const int run = (lane * 37) >> 8, l = lane - 7 * run;
    const int gu = tab(0x010100ull), bb = tab(0x010000010100ull), stride = tab(0x1507071C1C00ull) * 128;
    const int row0 = tab(0x0208000000ull) * b + bb * (mq * 112 + ix * 4);
    const int n = tab(0x1C0E38383800ull) & isai;
    const u32 o = (row0 + (run >> gu)) * stride + ((run & gu) * 14 + bb * rh * 7 + tab(0x070000000000ull) + l) * 128;
    c.toff = lane < n ? o : OOB;
    c.rt = mk(tp, (448 << tab(0x02000000ull)) * stride);
    if constexpr (G_TOUCH == -1) c.tw = touch(c.toff, c.rt);
  }
  if constexpr (G_TOUCH == -1) first3();
  if (__builtin_expect(isa, 1)) {
    c.vg = R * 3584 + role * 128 + sw;
    c.vu = R * 3584 + role * 128 + (sw ^ 32);
    c.vw2 = R * 896 + rl * 128 + sw;
    type_a(c, w, lane, mt, role, xc * 4 + (ix & 3), rl, out DIAGA);
  } else {
    type_b(c, w, lane, mt, rh, gw DIAGA);
  }
}

extern "C" void kernel_launch(void* const* d_in, const int* in_sizes, int n_in, void* d_out, int out_size, void* d_ws, size_t ws_size,
                              hipStream_t stream) {
  fused_kernel<<<256, 512, 0, stream>>>(d_in[0], d_in[4], d_in[5], d_in[6], d_in[2], d_in[1], d_in[7], d_out, d_ws);
}
```

```cpp
#include <hip/hip_runtime.h>
#include <hip/hip_bf16.h>

typedef unsigned long long u64;
typedef unsigned u32;
typedef __bf16 bf16x8 __attribute__((ext_vector_type(8)));
typedef __bf16 bf16x2 __attribute__((ext_vector_type(2)));
typedef short s16x4 __attribute__((ext_vector_type(4)));
typedef u32 u32x4 __attribute__((ext_vector_type(4)));
typedef u32 u32x2 __attribute__((ext_vector_type(2)));
typedef float f32x16 __attribute__((ext_vector_type(16)));
typedef float f32x4 __attribute__((ext_vector_type(4)));
typedef __amdgpu_buffer_rsrc_t rsrc_t;
#define LAS __attribute__((address_space(3)))
#define GAS __attribute__((address_space(1)))
#define DI __device__ __forceinline__
#define SCHED() __builtin_amdgcn_sched_barrier(0)
template <int V> using IC = std::integral_constant<int, V>;

constexpr u32 WS_E = 0;
constexpr u32 WS_FLAG = 4096;
constexpr u32 WS_OMG = 32768;
constexpr u32 WS_K = 65536;
constexpr u32 KV_T = 28672;
constexpr u32 WS_V = WS_K + 64 * KV_T;
constexpr u32 WS_A2 = WS_V + 64 * KV_T;
constexpr u32 A2_T = 143360;
constexpr u32 WS_END = WS_A2 + 64 * A2_T;
constexpr u32 OOB = 0xFFFFFF00u;
constexpr u32 CODE_BYTES = 32768;
constexpr int SC1 = 16;
constexpr int SLOT = 8192;
constexpr int SPIN_CAP = 1 << 15;

DI rsrc_t mk(const void* p, u32 bytes) { return __builtin_amdgcn_make_buffer_rsrc((void*)p, (short)0, (int)bytes, 0x00020000); }
template <u32 DST> DI void dma(u32 m0w, u32 voff, rsrc_t rs, u32 soff) {
  asm volatile("s_add_u32 m0, %0, %4\n\ts_nop 0\n\tbuffer_load_dwordx4 %1, %2, %3 offen lds" ::"s"(m0w), "v"(voff), "s"(rs), "s"(soff), "n"(DST) : "memory", "scc");
}
DI u32x4 ld128_sc1(u32 voff, rsrc_t rs, u32 soff) {
  u32x4 v;
  asm volatile("buffer_load_dwordx4 %0, %1, %2, %3 offen sc1" : "=v"(v) : "v"(voff), "s"(rs), "s"(soff) : "memory");
  return v;
}
DI u32x2 ld64_sc1(u32 voff, rsrc_t rs, u32 soff) {
  u32x2 v;
  asm volatile("buffer_load_dwordx2 %0, %1, %2, %3 offen sc1" : "=v"(v) : "v"(voff), "s"(rs), "s"(soff) : "memory");
  return v;
}
DI u32 ld32_sc1(u32 voff, rsrc_t rs, u32 soff) {
  u32 v;
  asm volatile("buffer_load_dword %0, %1, %2, %3 offen sc1" : "=v"(v) : "v"(voff), "s"(rs), "s"(soff) : "memory");
  return v;
}
DI u32 touch(u32 voff, rsrc_t rs) {
  u32 v;
  asm volatile("buffer_load_dword %0, %1, %2, 0 offen" : "=v"(v) : "v"(voff), "s"(rs) : "memory");
  return v;
}
DI void st128(u32x4 v, rsrc_t rs, u32 voff, u32 soff) {
  asm volatile("buffer_store_dwordx4 %0, %1, %2, %3 offen sc1\n\ts_nop 1" ::"v"(v), "v"(voff), "s"(rs), "s"(soff) : "memory");
}
constexpr int clampvm(int n) { return n > 63 ? 63 : n; }
template <int N> DI void wait_vm() { static_assert(N >= 0, ""); asm volatile("s_waitcnt vmcnt(%0)" ::"n"(clampvm(N)) : "memory"); }
template <int N> DI void sync_vm() { static_assert(N >= 0, ""); asm volatile("s_waitcnt vmcnt(%0)\n\ts_barrier" ::"n"(clampvm(N)) : "memory"); }
template <class T> DI void pin(T& v) { asm volatile("" : "+v"(v)); }
template <int I, int N, class Fn> DI void static_for(Fn&& f) {
  if constexpr (I < N) { f(IC<I>{}); static_for<I + 1, N>(f); }
}

DI u32x4 trfrag(LAS char* p) {
  s16x4 lo = __builtin_amdgcn_ds_read_tr16_b64_v4i16((LAS s16x4*)p);
  s16x4 hi = __builtin_amdgcn_ds_read_tr16_b64_v4i16((LAS s16x4*)(p + 512));
  u32x2 a = __builtin_bit_cast(u32x2, lo), b = __builtin_bit_cast(u32x2, hi);
  return u32x4{a.x, a.y, b.x, b.y};
}
DI f32x16 mfma(u32x4 a, u32x4 b, f32x16 c) {
  return __builtin_amdgcn_mfma_f32_32x32x16_bf16(__builtin_bit_cast(bf16x8, a), __builtin_bit_cast(bf16x8, b), c, 0, 0, 0);
}
DI void mfma_at(f32x16& c, u32x4 a, u32x4 b) { c = mfma(a, b, c); asm volatile("" : "+v"(c)); }
DI u32 pk(float a, float b) { bf16x2 c; c[0] = (__bf16)a; c[1] = (__bf16)b; return __builtin_bit_cast(u32, c); }
DI u32x4 pk8(const f32x16& v, int s) { return u32x4{pk(v[8 * s], v[8 * s + 1]), pk(v[8 * s + 2], v[8 * s + 3]), pk(v[8 * s + 4], v[8 * s + 5]), pk(v[8 * s + 6], v[8 * s + 7])}; }
DI float sigmoidf(float z) { return __builtin_amdgcn_rcpf(1.f + __builtin_amdgcn_exp2f(z * -1.4426950408889634f)); }
DI float glu(float g, float u) { return u * sigmoidf(g); }
DI float xhalf_max(float v) { auto r = __builtin_amdgcn_permlane32_swap(__float_as_uint(v), __float_as_uint(v), false, false); return fmaxf(__uint_as_float(r[0]), __uint_as_float(r[1])); }
DI float xhalf_sum(float v) { auto r = __builtin_amdgcn_permlane32_swap(__float_as_uint(v), __float_as_uint(v), false, false); return __uint_as_float(r[0]) + __uint_as_float(r[1]); }

DI void spin(const char* ws, u32 off, bool need, u64 tag) {
  const GAS u64* p = (const GAS u64*)(ws + off);
#pragma unroll 1
  for (int i = 0; i < SPIN_CAP; ++i) {
    u64 v = __hip_atomic_load(p, __ATOMIC_RELAXED, __HIP_MEMORY_SCOPE_AGENT);
    if (__all(!need || v == tag)) break;
    __builtin_amdgcn_s_sleep(2);
  }
  asm volatile("" ::: "memory");
}
DI bool match(u32x2 v, bool need, u64 tag) { return __all(!need || (v.x == (u32)tag && v.y == (u32)(tag >> 32))); }
DI u64 tag_of(u32x2 ev) {
  u64 e = ((u64)__builtin_amdgcn_readfirstlane(ev.y) << 32) | (u32)__builtin_amdgcn_readfirstlane(ev.x);
  return e * 6364136223846793005ull + 1442695040888963407ull;
}
DI void put_tag(u64 tag, rsrc_t rs, u32 voff, u32 soff) { __builtin_amdgcn_raw_buffer_store_b64(u32x2{(u32)tag, (u32)(tag >> 32)}, rs, voff, soff, SC1); }

constexpr char HEAD[] = "KKGKKGKKGK";
constexpr int BARS[] = {0, 2, 6, 8, 10, 14, 16, 18, 22, 24, 28, 46, 62, 68, 75, 79, 86, 96};
constexpr int PRO_DMA = 9, TPOS = 6;
constexpr int HEAD_N = 26, HEAD_RATE = 20;
constexpr int EP1 = 2;
constexpr int U_KVPUB = 22, U_H1PUB = 40, U_H2PUB = 57, U_POLL1 = 52, U_CHK1 = 56, U_POLL2 = 64, U_CHK2 = 68, U_POLL3 = 70, U_CHK3 = 76;
constexpr int NV = 105, U0 = 14, IT_OMG = 28;
constexpr int PRO_OPS = PRO_DMA + 2;
constexpr int imin(int a, int b) { return a < b ? a : b; }
constexpr int item_of(int i) { return i < IT_OMG ? i : i + 1; }
constexpr int ld_rel(int k) { return k < 6 ? 0 : 1 + (k - 6) / 2; }
constexpr int ld_iter(int it) { return U0 + (it < 14 ? U_CHK1 + ld_rel(it) : (it < 28 ? U_CHK2 + ld_rel(it - 14) : U_CHK3 + (it - 28) / 2)); }
struct Sched {
  int nhead = 0, vkvst = 0, vg1 = 0;
  int xl[NV + 2] = {}, wl[NV + 2] = {};
  int kind[NV + 2] = {}, idx[NV + 2] = {}, lo[NV + 2] = {}, hi[NV + 2] = {};
  int pk[84] = {}, pi[84] = {}, pj[84] = {};
  bool bar[NV] = {};
  int cum[NV + 1] = {}, base[NV + 1] = {}, nst[NV] = {}, ldlo[NV + 1] = {}, cnt[NV] = {}, pdma[84] = {}, pld[36] = {}, freed[NV] = {};
  int cnt0 = 0; bool ok = true;
  constexpr Sched() {
    int v = 0, n = 0, k = 0, g = 0;
    int nx = 0;
    auto add = [&](int kd, int st, int nit, int nw) {
      const int first = n; int x = -1;
      if (st < 7 && st == nx) { x = n; for (int e = 0; e < 2; ++e) { pk[n] = 0; pi[n] = st; pj[n] = e; ++n; } ++nx; }
      for (int e = 0; e < nit; ++e) { kind[v] = kd; idx[v] = nit * st + e; lo[v] = first; hi[v] = n + nw - 1; xl[v] = x; wl[v] = n; ++v; }
      for (int e = 0; e < nw; ++e) { pk[n] = kd; pi[n] = st; pj[n] = kd ? e : 2; ++n; }
    };
    for (int i = 0; HEAD[i]; ++i) { if (HEAD[i] == 'K') { add(0, k++, 2, 1); nhead = v; } else { add(1, g++, 4, 2); if (g == 7) vg1 = v; } }
    if (k != 7 || g > 7) ok = false;
    vkvst = nhead + 1;
    while (g < 14) { add(1, g++, 4, 2); if (g == 7) vg1 = v; }
    for (int i = 0; i < 35; ++i) { kind[v] = 2; idx[v] = 56 + i; lo[v] = hi[v] = wl[v] = n; pk[n] = 2; pi[n] = i; ++n; ++v; }
    kind[NV] = kind[NV + 1] = 3; hi[NV] = hi[NV + 1] = 83;
    for (int e : BARS) bar[e] = true;
    bar[U0 + U_KVPUB] = bar[U0 + U_H1PUB] = bar[U0 + U_H2PUB] = bar[U0 + U_CHK2] = bar[U0 + U_CHK3] = true;
    if (U0 + U_KVPUB <= vkvst || U0 + U_H1PUB <= vg1 + 2 * EP1 || bar[U0 + U_CHK1]) ok = false;
    int fr = 0, cc = PRO_DMA, cr = 0, it = 0;
    cum[0] = cc; base[0] = PRO_OPS;
    for (n = 0; n < PRO_DMA; ++n) pdma[n] = n + (n >= TPOS);
    for (v = 0; v < NV; ++v) {
      const int u = v - U0;
      if (bar[v] || u == U_CHK1) fr = lo[v];
      freed[v] = fr;
      nst[v] = 2 * (v == vkvst) + (v == vg1 + EP1) + (v == vg1 + 2 * EP1) + 2 * (u == 56) + (u == U_POLL1) + (u == U_POLL2) + (u == U_POLL3);
      ldlo[v] = it;
      while (it < 36 && ld_iter(it) == v) { pld[it] = base[v] + nst[v] + (it - ldlo[v]); ++it; }
      cr += v < HEAD_N ? HEAD_RATE : 16;
      const int t = imin(imin(cc + imin(cr / 16, 2), fr + 20), 84);
      cr -= 16 * (t - cc); if (cr > 16) cr = 16;
      for (n = cc; n < t; ++n) pdma[n] = base[v] + nst[v] + (it - ldlo[v]) + (n - cc);
      base[v + 1] = base[v] + nst[v] + (it - ldlo[v]) + (t - cc);
      cc = t; cum[v + 1] = cc;
    }
    ldlo[NV] = it;
    if (it != 36 || cc != 84) ok = false;
    for (v = -1; v < NV; ++v) {
      if (v >= 0 && !bar[v]) continue;
      int nb = v + 1; while (nb < NV && !bar[nb]) ++nb;
      const int need = hi[nb + 1], at = v < 0 ? 0 : v;
      if (need >= cum[at]) ok = false;
      int c = base[at] - pdma[need] - 1;
      const int u = v - U0;
      if (u == U_KVPUB) c = imin(c, base[v] - (base[vkvst] + nst[vkvst]));
      if (u == U_H1PUB) c = imin(c, base[v] - (base[vg1 + 2 * EP1] + nst[vg1 + 2 * EP1]));
      if (u == U_H2PUB) c = imin(c, base[v] - (base[U0 + 56] + nst[U0 + 56]));
      if (v < 0) cnt0 = c; else cnt[v] = c;
    }
    for (int i = 0; i < 35; ++i) if (ld_iter(item_of(i)) > U0 + 56 + i) ok = false;
  }
};
constexpr Sched SA{};
static_assert(SA.ok, "schedule: a piece or operand is used before it is requested, or HEAD is malformed");
static_assert(U_CHK1 == 56 && U_POLL1 < U_CHK1 && U_CHK1 < U_POLL2 && U_POLL2 < U_CHK2 && U_CHK2 < U_POLL3 && U_POLL3 < U_CHK3, "");
static_assert(U_KVPUB < U_H1PUB && U_H1PUB < U_CHK1 && U_H2PUB > 56 && U_H2PUB < U_CHK2, "a workgroup publishes a class before it waits for that class");
static_assert(SA.freed[NV - 1] >= 72, "the reduction scratch (slots 4..11 = pieces 64..71) must be free at the end");

struct Ctx {
  LAS char* sm; u32 m0w;
  rsrc_t rx, rqkv, rwg, rwu, rwd, rwo, rws, rt;
  const char* ws;
  u32 toff, pf;
  u32 vx, vkv, vg, vu, vw2;
};

template <int N> DI void issueA(const Ctx& c) {
  constexpr u32 dst = (N % 20) * SLOT;
  constexpr int k = SA.pk[N], st = SA.pi[N], j = SA.pj[N];
  if constexpr (k == 0) {
    if constexpr (j < 2) dma<dst>(c.m0w, c.vx, c.rx, j * 896 + st * 128);
    else dma<dst>(c.m0w, c.vkv, c.rqkv, st * 64 * 2688);
  } else if constexpr (k == 1) {
    if constexpr (j == 0) dma<dst>(c.m0w, c.vg, c.rwg, (st % 7) * 64 * 3584 + (st / 7) * 1792);
    else dma<dst>(c.m0w, c.vu, c.rwu, (st % 7) * 64 * 3584 + (st / 7) * 1792);
  } else if constexpr (st < 28) dma<dst>(c.m0w, c.vw2, c.rwd, st * 64 * 896);
  else dma<dst>(c.m0w, c.vw2, c.rwo, (st - 28) * 64 * 896);
}

DI void type_a(Ctx& c, int w, int lane, int mt, int role, int R2, int n2, void* out) {
  const int r = lane & 31, hi = lane >> 5, blk = (lane >> 4) & 1, q = (lane & 15) >> 2, p = lane & 3;
  const int sp = ((p & 1) << 1) | (p >> 1);
  const int rg = w & 3, cg = w >> 2, T = mt * 4 + rg;
  const bool isv = role >= 7;
  LAS char* sm = c.sm;
  static_for<2, TPOS>([&](auto N) { issueA<N>(c); });
  u32 tw = touch(c.toff, c.rt);
  static_for<TPOS, PRO_DMA>([&](auto N) { issueA<N>(c); });
  u32x2 ev = ld64_sc1(w == 0 ? WS_E : OOB, c.rws, 0);
  const int R = 32 * (rg & 1) + r, xm = (R >> 1) & 7;
  int xa[4];
#pragma unroll
  for (int s = 0; s < 4; ++s) xa[s] = (rg >> 1) * SLOT + R * 128 + (((2 * s + hi) ^ xm) << 4);
  const int rowb = (8 * hi + q) * 128, q2 = q & 2;
  const int akv = rowb + (((2 * cg + blk) ^ q2) << 5) + (isv ? p : sp) * 8;
  int agu[2];
#pragma unroll
  for (int i = 0; i < 2; ++i) agu[i] = blk * SLOT + rowb + (((2 * cg + i) ^ q2 ^ blk) << 5) + sp * 8;
  int aguw[2][3];
#pragma unroll
  for (int i = 0; i < 2; ++i)
#pragma unroll
    for (int k = 0; k < 3; ++k) { aguw[i][k] = agu[i] + k * 65536; pin(aguw[i][k]); }
  const int rt = w & 1, kq = w >> 1;
  int aw2[2], aw2w[2][3];
#pragma unroll
  for (int i = 0; i < 2; ++i) aw2[i] = kq * 2048 + rowb + (((2 * i + blk) ^ q2) << 5) + p * 8;
  const int T2 = (R2 >> 1) * 4 + 2 * rt + (R2 & 1);

  u32x4 xf[28], wf[2][2], af[35], hp[2];
  f32x16 akvacc = {}, g1[2] = {}, g2[2] = {}, ah[2] = {}, ao[2] = {};
  u32x2 pollv; u32 omg; u64 tag;
  const u32 lane16 = lane * 16;
  const u32 poll_off = WS_FLAG + (R2 >> 1) * 256 + (lane & 15) * 8;
  const bool need_h = (lane & 15) < 14;
  const u32 a2_off = WS_A2 + T2 * A2_T + kq * 1024 + lane16;
  const u32 h_off = WS_A2 + T * A2_T + (role * 4 + 2 * cg) * 1024 + lane16;

  auto read_w = [&](auto V, auto I) {
    constexpr int v = V, i = I, k = SA.kind[v], u = SA.idx[v], sl = SA.wl[v] % 20;
    if constexpr (k < 2 && SA.xl[v] >= 0 && (k == 0 || i == 0)) {
      constexpr int ks = k == 0 ? 2 * u + i : u, xs = SA.xl[v] % 20;
      const int wrap = xs == 19 ? (rg >> 1) * (-20 * SLOT) : 0;
      xf[ks] = *(LAS u32x4*)(sm + xa[ks % 4] + wrap + xs * SLOT);
    }
    if constexpr (k == 0) {
      wf[v & 1][i] = trfrag(sm + akv + sl * SLOT + ((2 * u + i) % 4) * 2048);
    } else if constexpr (k == 1) {
      const int wrap = sl == 19 ? blk * (-20 * SLOT) : 0;
      wf[v & 1][i] = trfrag(sm + aguw[i][sl / 8] + wrap + (sl % 8) * SLOT + (u % 4) * 2048);
    } else if constexpr (k == 2) {
      if constexpr (u == 56) {
#pragma unroll
        for (int e = 0; e < 3; ++e) { aw2w[i][e] = aw2[i] + e * 65536; pin(aw2w[i][e]); }
      }
      wf[v & 1][i] = trfrag(sm + aw2w[i][sl / 8] + (sl % 8) * SLOT);
    }
  };
  auto publish = [&](int cls) {
    if (w == 0) put_tag(tag, c.rws, lane == 0 ? role * 8 : OOB, WS_FLAG + cls * 4096 + mt * 256);
  };
  auto load_item = [&](auto IT) {
    constexpr int it = IT;
    if constexpr (it == IT_OMG) omg = ld32_sc1(r * 4, c.rws, WS_OMG + T2 * 128);
    else { constexpr int i = it < IT_OMG ? it : it - 1; af[i] = ld128_sc1(a2_off, c.rws, i * 4096); }
  };
  auto check = [&](auto CNT, int cls, bool need) {
    wait_vm<CNT>();
    pin(pollv);
    if (w == 0 && !match(pollv, need, tag)) spin(c.ws, poll_off + cls * 4096, need, tag);
  };

  sync_vm<SA.cnt0>();
  pin(c.pf);
  static_for<0, 4>([&](auto S) { read_w(IC<S / 2>{}, IC<S % 2>{}); });
  SCHED();
  auto iter = [&](auto V, auto ISV) {
    constexpr int v = V, b = v & 1, kd = SA.kind[v], ui = SA.idx[v], u = v - U0;
    if constexpr (u == U_CHK2) check(IC<SA.base[v] - SA.base[U0 + U_POLL2] - 1>{}, 2, need_h);
    if constexpr (u == U_CHK3) check(IC<SA.base[v] - SA.base[U0 + U_POLL3] - 1>{}, 2, !need_h);
    if constexpr (SA.bar[v]) sync_vm<SA.cnt[v]>();
    if constexpr (u == U_KVPUB) {
      pin(ev); pin(tw);
      tag = tag_of(ev);
      publish(0);
    }
    if constexpr (u == U_H1PUB) publish(1);
    if constexpr (u == U_H2PUB) publish(2);
    if constexpr (u == U_POLL1) pollv = ld64_sc1(w == 0 ? poll_off : OOB, c.rws, 4096);
    if constexpr (u == U_POLL2 || u == U_POLL3) pollv = ld64_sc1(w == 0 ? poll_off : OOB, c.rws, 8192);
    if constexpr (v == SA.vkvst) {
#pragma unroll
      for (int s = 0; s < 2; ++s) {
        const u32 off = isv ? WS_V + T * KV_T + (s * 2 + hi) * 7168 + ((role - 7) * 64 + cg * 32 + r) * 16
                            : WS_K + T * KV_T + (role * 8 + cg * 4 + 2 * s) * 512 + lane16;
        st128(pk8(akvacc, s), c.rws, off, 0);
      }
    }
    if constexpr (v == SA.vg1 + EP1) st128(hp[0], c.rws, h_off, 0);
    if constexpr (v == SA.vg1 + 2 * EP1) st128(hp[1], c.rws, h_off, 1024);
    if constexpr (u == 56) {
#pragma unroll
      for (int t = 0; t < 2; ++t) {
#pragma unroll
        for (int j = 0; j < 8; j += 2) hp[t][j / 2] = pk(glu(g2[t][j], g2[t][8 + j]), glu(g2[t][j + 1], g2[t][9 + j]));
        st128(hp[t], c.rws, h_off, (56 + t) * 1024);
        SCHED();
      }
    }
    asm volatile("" ::: "memory");
    if constexpr (u == U_CHK1) {
      check(IC<SA.base[v] + SA.nst[v] - SA.base[U0 + U_POLL1] - 1>{}, 1, need_h);
      asm volatile("s_barrier" ::: "memory");
    }
    constexpr bool ld_first = u >= 56 && ld_iter(item_of(u < 56 ? 0 : u - 56)) == v;
    constexpr int nld = SA.ldlo[v + 1] - SA.ldlo[v], nd = SA.cum[v + 1] - SA.cum[v];
    if constexpr (ld_first) static_for<SA.ldlo[v], SA.ldlo[v + 1]>(load_item);
    if constexpr (u >= 56) {
      wait_vm<SA.base[v] + SA.nst[v] + (ld_first ? nld : 0) - SA.pld[item_of(u - 56)] - 1>();
      pin(af[u - 56]);
    }
    SCHED();
    static_for<0, 2>([&](auto I) {
      constexpr int i = I;
      if constexpr (kd == 0) { if constexpr (ISV) mfma_at(akvacc, xf[2 * ui + i], wf[b][i]); else mfma_at(akvacc, wf[b][i], xf[2 * ui + i]); }
      else if constexpr (ui < 28) mfma_at(g1[i], wf[b][i], xf[ui]);
      else if constexpr (ui < 56) mfma_at(g2[i], wf[b][i], xf[ui - 28]);
      else if constexpr (ui < 84) mfma_at(ah[i], wf[b][i], af[ui - 56]);
      else mfma_at(ao[i], wf[b][i], af[ui - 56]);
      SCHED();
      read_w(IC<v + 2>{}, I);
      if constexpr (v >= SA.vg1 && v < SA.vg1 + 2 * EP1) {
        constexpr int t = (v - SA.vg1) / EP1, per = 4 / EP1, k0 = ((v - SA.vg1) % EP1) * per;
#pragma unroll
        for (int k = k0 + (i * per + 1) / 2; k < k0 + ((i + 1) * per + 1) / 2; ++k) {
          u32 hv = pk(glu(g1[t][2 * k], g1[t][8 + 2 * k]), glu(g1[t][2 * k + 1], g1[t][9 + 2 * k]));
          pin(hv);
          hp[t][k] = hv;
        }
      }
      if constexpr (i == 0 && !ld_first) static_for<SA.ldlo[v], SA.ldlo[v + 1]>(load_item);
      if constexpr (i == 0 && nd == 2) issueA<SA.cum[v]>(c);
      if constexpr (i == 1 && nd >= 1) issueA<SA.cum[v + 1] - 1>(c);
      SCHED();
    });
  };
  if (isv) static_for<0, SA.nhead>([&](auto V) { iter(V, IC<1>{}); });
  else static_for<0, SA.nhead>([&](auto V) { iter(V, IC<0>{}); });
  static_for<SA.nhead, NV>([&](auto V) { iter(V, IC<0>{}); });
  pin(omg);
  const float om = __uint_as_float(omg);
  LAS char* red = sm + 4 * SLOT;
#pragma unroll
  for (int t = 0; t < 2; ++t)
#pragma unroll
    for (int a = 0; a < 4; ++a) {
      f32x4 v;
#pragma unroll
      for (int e = 0; e < 4; ++e) v[e] = ah[t][4 * a + e] * om + ao[t][4 * a + e];
      *(LAS f32x4*)(red + kq * 16384 + (2 * r + rt) * 256 + (((8 * t + 2 * a + hi) ^ (r & 15)) << 4)) = v;
    }
  asm volatile("s_waitcnt lgkmcnt(0)\n\ts_barrier" ::: "memory");
  {
    const int row = w * 8 + (lane >> 3), c8 = lane & 7, m = (row >> 1) & 15;
    f32x4 s0 = {}, s1 = {};
#pragma unroll
    for (int k = 0; k < 4; ++k) {
      s0 += *(LAS f32x4*)(red + k * 16384 + row * 256 + (((2 * c8) ^ m) << 4));
      s1 += *(LAS f32x4*)(red + k * 16384 + row * 256 + (((2 * c8 + 1) ^ m) << 4));
    }
    const u32x4 o = {pk(s0[0], s0[1]), pk(s0[2], s0[3]), pk(s1[0], s1[1]), pk(s1[2], s1[3])};
    __builtin_amdgcn_raw_buffer_store_b128(o, mk(out, 2048 * 896), (R2 * 64 + row) * 896 + n2 * 128 + c8 * 16, 0, SC1);
  }
}

constexpr int PRO_B = 12, GB_POLL = 25;
constexpr int cumB(int g) {
  int cc = PRO_B;
  for (int j = 0; j <= g; ++j) cc = imin(imin(cc + 2, 6 * ((j + 1) / 4) + 18), 42);
  return cc;
}
template <int N> DI void issueB(const Ctx& c) {
  constexpr int kc = N / 6, j = N % 6;
  constexpr u32 dst = (N % 18) * SLOT;
  if constexpr (j < 2) dma<dst>(c.m0w, c.vx, c.rx, j * 896 + kc * 128);
  else dma<dst>(c.m0w, c.vkv, c.rqkv, kc * 64 * 2688 + (j - 2) * 128);
}

DI void type_b(Ctx& c, int w, int lane, int mt, int hf, const void* gw) {
  const int r = lane & 31, hi = lane >> 5, blk = (lane >> 4) & 1, q = (lane & 15) >> 2, p = lane & 3;
  const int sp = ((p & 1) << 1) | (p >> 1);
  const int rg = w & 3, cg = w >> 2, T = mt * 4 + rg;
  LAS char* sm = c.sm;
  if (w == 0) dma<18 * SLOT>(c.m0w, lane * 16, mk(gw, 896), 0);
  u32x2 ev = ld64_sc1(w == 0 ? WS_E : OOB, c.rws, 0);
  static_for<2, PRO_B>([&](auto N) { issueB<N>(c); });
  const int R = 32 * (rg & 1) + r, xm = (R >> 1) & 7;
  int xa[4], aq[4];
#pragma unroll
  for (int s = 0; s < 4; ++s) xa[s] = (rg >> 1) * SLOT + R * 128 + (((2 * s + hi) ^ xm) << 4);
  const int h0 = 8 * hf + 4 * cg;
#pragma unroll
  for (int i = 0; i < 4; ++i) {
    const int col = 28 * (h0 + i) - 4 * (i & 1) - 192 * hf + 16 * blk + 4 * sp;
    aq[i] = (2 + (col >> 6)) * SLOT + (8 * hi + q) * 128 + (((col & 63) * 2) ^ ((q & 2) << 5));
  }
  const bool first = (mt == 0) && !(rg & 1);
  const int Tp = first ? T : ((rg & 1) ? T - 1 : T - 3);
  const u32 lane16 = lane * 16, lo16 = hi ? OOB : lane16;
  const u32 ob = WS_A2 + T * A2_T + (1792 + 28 * h0) * 64;
  const u32 omg_off = (hf == 0 && cg == 0 && hi == 0) ? r * 4 : OOB;
  const u32 poll_off = WS_FLAG + ((lane & 16) && mt > 0 ? mt - 1 : mt) * 256 + (lane & 15) * 8;
  const bool poll_need = (lane & 15) < 14;
  u32x4 xf[2], gf[2], wf[2][4];
  u32x4 kf[4][2], kp[2], vf[4][2], vp[4];
  f32x16 qa[4] = {};
  u32x2 pollv;
  const int sr = (r & 19) | ((r & 4) << 1) | ((r & 8) >> 1);
  const u32 kp_off = (28 * (r >> 3) - 4 * ((r >> 3) & 1)) * 64 + (hi * 32 + 24 + (r & 7)) * 16;
  const u32 v_off = hi * 7168 + sr * 16;
  auto load_kv = [&](auto IT) {
    constexpr int it = IT;
    if constexpr (it < 2) kp[it] = ld128_sc1(kp_off, c.rws, WS_K + Tp * KV_T + 28 * h0 * 64 + it * 1024);
    else if constexpr (it < 10) {
      constexpr int i = (it - 2) / 2, k = it & 1;
      kf[i][k] = ld128_sc1(lane16, c.rws, WS_K + T * KV_T + (28 * (h0 + i) - 4 * (i & 1)) * 64 + k * 1024);
    } else {
      constexpr int i = (it - 10) / 3, k = (it - 10) % 3;
      const int wh = 28 * (h0 + i) - 4 * (i & 1);
      if constexpr (k == 0) vp[i] = ld128_sc1(v_off, c.rws, WS_V + Tp * KV_T + 14336 + wh * 16);
      else vf[i][k - 1] = ld128_sc1(v_off, c.rws, WS_V + T * KV_T + (k - 1) * 14336 + wh * 16);
    }
  };
  auto read_x = [&](auto G) {
    constexpr int g = G;
    if constexpr (g < 28) {
      xf[g & 1] = *(LAS u32x4*)(sm + xa[g % 4] + ((6 * (g / 4)) % 18) * SLOT);
      gf[g & 1] = *(LAS u32x4*)(sm + 18 * SLOT + g * 32 + hi * 16);
    }
  };
  auto read_w = [&](auto G, auto I) {
    constexpr int g = G;
    if constexpr (g < 28) wf[g & 1][I] = trfrag(sm + aq[I] + ((6 * (g / 4)) % 18) * SLOT + (g % 4) * 2048);
  };
  sync_vm<PRO_B - 6>();
  pin(c.pf);
  read_x(IC<0>{});
  static_for<0, 4>([&](auto I) { read_w(IC<0>{}, I); });
  SCHED();
  static_for<0, 28>([&](auto G) {
    constexpr int g = G, b = g & 1;
    if constexpr (g % 4 == 3 && g < 27) { asm volatile("s_waitcnt lgkmcnt(0)" ::: "memory"); sync_vm<cumB(g - 1) - 6 * (g / 4 + 2)>(); }
    if constexpr (g == GB_POLL) pollv = ld64_sc1(w == 0 ? poll_off : OOB, c.rws, 0);
    SCHED();
    static_for<0, 4>([&](auto I) {
      constexpr int i = I;
      mfma_at(qa[i], wf[b][i], xf[b]);
      SCHED();
      if constexpr (i == 0) read_x(IC<g + 1>{});
      read_w(IC<g + 1>{}, I);
      if constexpr (i < 2) {
        wf[b][2][2 * i] = r == 28 ? gf[b][2 * i] : wf[b][2][2 * i];
        wf[b][2][2 * i + 1] = r == 28 ? gf[b][2 * i + 1] : wf[b][2][2 * i + 1];
        pin(wf[b][2]);
      }
      if constexpr (i >= 2 && cumB(g - 1) + i - 2 < cumB(g)) issueB<cumB(g - 1) + i - 2>(c);
      SCHED();
    });
  });
  wait_vm<0>();
  pin(pollv); pin(ev);
  const u64 tag = tag_of(ev);
  if (w == 0 && !match(pollv, poll_need, tag)) spin(c.ws, poll_off, poll_need, tag);
  asm volatile("s_barrier" ::: "memory");
  static_for<0, 6>(load_kv);
  SCHED();
  const float z = __uint_as_float(__builtin_amdgcn_permlane32_swap(__float_as_uint(qa[2][12]), __float_as_uint(qa[2][12]), false, false)[1]);
  const float gate = sigmoidf(z);
  const u32 omg = __float_as_uint(sigmoidf(-z));
  int ab[4];
  u32 msk[5][2];
#pragma unroll
  for (int b = 0; b < 4; ++b) ab[b] = (r >> 3) - ((r & 7) < 4 * hi + b ? 1 : 0);
#pragma unroll
  for (int a = 0; a < 5; ++a)
#pragma unroll
    for (int e = 0; e < 2; ++e) msk[a][e] = (ab[2 * e] == a - 1 ? 0xFFFFu : 0u) | (ab[2 * e + 1] == a - 1 ? 0xFFFF0000u : 0u);
  const float csc = 0.18898223650461362f * 1.4426950408889634f;
  u32 p01[2], p23[2];
  u32x4 qf[4][2];
  static_for<0, 4>([&](auto I) {
    constexpr int i = I;
    qf[i][0] = pk8(qa[i], 0); qf[i][1] = pk8(qa[i], 1);
    if (i & 1) { qf[i][0][0] = hi ? qf[i][0][0] : 0u; qf[i][0][1] = hi ? qf[i][0][1] : 0u; }
    else       { qf[i][1][2] = hi ? 0u : qf[i][1][2]; qf[i][1][3] = hi ? 0u : qf[i][1][3]; }
    pin(qf[i][0]); pin(qf[i][1]);
  });
#pragma unroll
  for (int a = 0; a < 5; ++a) { pin(msk[a][0]); pin(msk[a][1]); }
  __builtin_amdgcn_raw_buffer_store_b32(omg, c.rws, omg_off, WS_OMG + T * 128, SC1);
  asm volatile("" ::: "memory");
  f32x16 so[2], sv[2], oa[2];
  auto scores = [&](auto J) {
    static_for<0, 2>([&](auto E) {
      constexpr int i = 2 * J + E;
      pin(kf[i][0]); pin(kf[i][1]);
      so[E] = mfma(kf[i][0], qf[i][0], f32x16{}); sv[E] = mfma(kp[0], qf[i][0], f32x16{});
      mfma_at(so[E], kf[i][1], qf[i][1]); mfma_at(sv[E], kp[1], qf[i][1]);
    });
  };
  auto softmax = [&](auto J) {
    static_for<0, 2>([&](auto E) {
      constexpr int i = 2 * J + E;
      float v[4];
#pragma unroll
      for (int b = 0; b < 4; ++b) {
        float t = first ? -1e30f : sv[E][4 * i + b];
#pragma unroll
        for (int a = 0; a < 4; ++a) t = ab[b] == a ? so[E][4 * a + b] : t;
        v[b] = t;
      }
      const float mx = xhalf_max(fmaxf(fmaxf(v[0], v[1]), fmaxf(v[2], v[3])));
#pragma unroll
      for (int b = 0; b < 4; ++b) v[b] = __builtin_amdgcn_exp2f((v[b] - mx) * csc);
      const float sc = gate * __builtin_amdgcn_rcpf(xhalf_sum((v[0] + v[1]) + (v[2] + v[3])));
      p01[E] = pk(v[0] * sc, v[1] * sc); p23[E] = pk(v[2] * sc, v[3] * sc);
    });
    pin(p01[0]); pin(p23[0]); pin(p01[1]); pin(p23[1]);
  };
  auto pv = [&](auto J) {
    static_for<0, 2>([&](auto E) {
      constexpr int i = 2 * J + E;
      pin(vp[i]); pin(vf[i][0]); pin(vf[i][1]);
      oa[E] = mfma(vp[i], u32x4{0u, 0u, p01[E] & msk[0][0], p23[E] & msk[0][1]}, f32x16{});
      oa[E] = mfma(vf[i][0], u32x4{p01[E] & msk[1][0], p23[E] & msk[1][1], p01[E] & msk[2][0], p23[E] & msk[2][1]}, oa[E]);
      mfma_at(oa[E], vf[i][1], u32x4{p01[E] & msk[3][0], p23[E] & msk[3][1], p01[E] & msk[4][0], p23[E] & msk[4][1]});
    });
  };
  auto store_o = [&](auto J) {
    u32x4 op[2][2] = {{pk8(oa[0], 0), pk8(oa[0], 1)}, {pk8(oa[1], 0), pk8(oa[1], 1)}};
#pragma unroll
    for (int e = 0; e < 2; ++e) { auto t = __builtin_amdgcn_permlane32_swap(op[0][1][e], op[1][0][e], false, false); op[0][1][e] = t[0]; op[1][0][e] = t[1]; }
    const u32 o2 = ob + J * 3584;
    st128(op[0][0], c.rws, lane16, o2);
    st128(op[0][1], c.rws, lo16, o2 + 1024);
    st128(op[1][0], c.rws, lane16, o2 + 1536);
    st128(op[1][1], c.rws, lane16, o2 + 2560);
  };
  static_for<10, 16>(load_kv);
  wait_vm<7>();
  pin(kp[0]); pin(kp[1]);
  scores(IC<0>{});
  static_for<6, 10>(load_kv);
  softmax(IC<0>{});
  static_for<16, 22>(load_kv);
  wait_vm<6>();
  pv(IC<0>{});
  scores(IC<1>{});
  store_o(IC<0>{});
  softmax(IC<1>{});
  wait_vm<4>();
  pv(IC<1>{});
  store_o(IC<1>{});
  sync_vm<0>();
  if (w == 0) put_tag(tag, c.rws, lane == 0 ? (14 + hf) * 8 : OOB, WS_FLAG + 8192 + mt * 256);
  if (mt == 0 && hf == 0 && w == 0) {
#pragma unroll 1
    for (int i = 0; i < 4; ++i) spin(c.ws, WS_FLAG + 8192 + (i * 4 + (lane >> 4)) * 256 + (lane & 15) * 8, true, tag);
    put_tag(tag, c.rws, lane == 0 ? 0 : OOB, WS_E);
  }
}

__global__ __launch_bounds__(512, 2) void fused_kernel(const void* __restrict__ x, const void* __restrict__ wg, const void* __restrict__ wu,
                                                       const void* __restrict__ wd, const void* __restrict__ wo, const void* __restrict__ wqkv,
                                                       const void* __restrict__ gw, void* __restrict__ out, void* __restrict__ ws) {
  __shared__ __attribute__((aligned(1024))) char smem[163840];
  const int lane = threadIdx.x & 63, w = __builtin_amdgcn_readfirstlane(threadIdx.x >> 6);
  Ctx c;
  {
    u64 pc;
    asm volatile("s_getpc_b64 %0" : "=s"(pc));
    c.pf = touch((w == 6 && lane < 8) ? ((blockIdx.x >> 3) * 8 + lane) * 128 : OOB, mk((const void*)(pc & ~255ull), CODE_BYTES));
  }
  const void* tp = ((const void* const __attribute__((address_space(4)))*)__builtin_amdgcn_kernarg_segment_ptr())[w];
  const int b = blockIdx.x, xc = b & 7, ix = b >> 3;
  const int mq = xc >> 1, mt = mq * 4 + (ix & 3), rl = ix >> 2, rh = xc & 1;
  const bool isa = rl < 7;
  const int role = rh * 7 + rl;
  c.sm = (LAS char*)smem;
  c.m0w = (u32)(size_t)c.sm + w * 1024;
  c.rx = mk(x, 2048 * 896); c.rqkv = mk(wqkv, 448 * 2688); c.rwg = mk(wg, 448 * 3584); c.rwu = mk(wu, 448 * 3584);
  c.rwd = mk(wd, 1792 * 896); c.rwo = mk(wo, 448 * 896); c.rws = mk(ws, WS_END);
  c.ws = (const char*)ws;
  const int r8 = lane >> 3, c8 = lane & 7, R = 8 * w + r8;
  c.vx = (mt * 128 + 2 * R) * 896 + ((c8 ^ ((R >> 1) & 7)) << 4);
  const int sw = (c8 ^ ((r8 & 2) << 1)) << 4;
  const int isai = (rl - 7) >> 31;
  c.vkv = R * 2688 + ((896 + role * 128) & isai) + ((rh * 384) & ~isai) + sw;
  static_for<0, 2>([&](auto N) { issueA<N>(c); });
  {
    auto tab = [&](u64 t) { return (int)((t >> (8 * w)) & 255); };
    const int run = (lane * 37) >> 8, l = lane - 7 * run;
    const int gu = tab(0x010100ull), bb = tab(0x010000010100ull), stride = tab(0x1507071C1C00ull) * 128;
    const int row0 = tab(0x0208000000ull) * b + bb * (mq * 112 + ix * 4);
    const int n = tab(0x1C0E38383800ull) & isai;
    const u32 o = (row0 + (run >> gu)) * stride + ((run & gu) * 14 + bb * rh * 7 + tab(0x070000000000ull) + l) * 128;
    c.toff = lane < n ? o : OOB;
    c.rt = mk(tp, (448 << tab(0x02000000ull)) * stride);
  }
  if (__builtin_expect(isa, 1)) {
    c.vg = R * 3584 + role * 128 + sw;
    c.vu = R * 3584 + role * 128 + (sw ^ 32);
    c.vw2 = R * 896 + rl * 128 + sw;
    type_a(c, w, lane, mt, role, xc * 4 + (ix & 3), rl, out);
  } else {
    type_b(c, w, lane, mt, rh, gw);
  }
}

extern "C" void kernel_launch(void* const* d_in, const int* in_sizes, int n_in, void* d_out, int out_size, void* d_ws, size_t ws_size,
                              hipStream_t stream) {
  fused_kernel<<<256, 512, 0, stream>>>(d_in[0], d_in[4], d_in[5], d_in[6], d_in[2], d_in[1], d_in[7], d_out, d_ws);
}
```

```cpp
#define OUT_NT
#include <hip/hip_runtime.h>
#include <hip/hip_bf16.h>

typedef unsigned long long u64;
typedef unsigned u32;
typedef __bf16 bf16x8 __attribute__((ext_vector_type(8)));
typedef __bf16 bf16x2 __attribute__((ext_vector_type(2)));
typedef short s16x4 __attribute__((ext_vector_type(4)));
typedef u32 u32x4 __attribute__((ext_vector_type(4)));
typedef u32 u32x2 __attribute__((ext_vector_type(2)));
typedef float f32x16 __attribute__((ext_vector_type(16)));
typedef float f32x4 __attribute__((ext_vector_type(4)));
typedef __amdgpu_buffer_rsrc_t rsrc_t;
#define LAS __attribute__((address_space(3)))
#define GAS __attribute__((address_space(1)))
#define DI __device__ __forceinline__
#define SCHED() __builtin_amdgcn_sched_barrier(0)
template <int V> using IC = std::integral_constant<int, V>;

constexpr u32 WS_E = 0;
constexpr u32 WS_FLAG = 4096;
constexpr u32 WS_OMG = 32768;
constexpr u32 WS_K = 65536;
constexpr u32 KV_T = 28672;
constexpr u32 WS_V = WS_K + 64 * KV_T;
constexpr u32 WS_A2 = WS_V + 64 * KV_T;
constexpr u32 A2_T = 143360;
constexpr u32 WS_END = WS_A2 + 64 * A2_T;
constexpr u32 OOB = 0xFFFFFF00u;
constexpr int SC1 = 16;
constexpr int SLOT = 8192;
constexpr int SPIN_CAP = 1 << 15;

DI rsrc_t mk(const void* p, u32 bytes) { return __builtin_amdgcn_make_buffer_rsrc((void*)p, (short)0, (int)bytes, 0x00020000); }
DI void dma(u32 m0v, u32 voff, rsrc_t rs, u32 soff) {
  asm volatile("s_mov_b32 m0, %0\n\ts_nop 0\n\tbuffer_load_dwordx4 %1, %2, %3 offen lds" ::"s"(m0v), "v"(voff), "s"(rs), "s"(soff) : "memory");
}
DI u32x4 ld128_sc1(u32 voff, rsrc_t rs, u32 soff) {
  u32x4 v;
  asm volatile("buffer_load_dwordx4 %0, %1, %2, %3 offen sc1" : "=v"(v) : "v"(voff), "s"(rs), "s"(soff) : "memory");
  return v;
}
DI u32x2 ld64_sc1(u32 voff, rsrc_t rs, u32 soff) {
  u32x2 v;
  asm volatile("buffer_load_dwordx2 %0, %1, %2, %3 offen sc1" : "=v"(v) : "v"(voff), "s"(rs), "s"(soff) : "memory");
  return v;
}
DI u32 ld32_sc1(u32 voff, rsrc_t rs, u32 soff) {
  u32 v;
  asm volatile("buffer_load_dword %0, %1, %2, %3 offen sc1" : "=v"(v) : "v"(voff), "s"(rs), "s"(soff) : "memory");
  return v;
}
DI u32 touch(u32 voff, rsrc_t rs) {
  u32 v;
  asm volatile("buffer_load_dword %0, %1, %2, 0 offen" : "=v"(v) : "v"(voff), "s"(rs) : "memory");
  return v;
}
DI void st128(u32x4 v, rsrc_t rs, u32 voff, u32 soff) {
  asm volatile("buffer_store_dwordx4 %0, %1, %2, %3 offen sc1\n\ts_nop 1" ::"v"(v), "v"(voff), "s"(rs), "s"(soff) : "memory");
}
constexpr int clampvm(int n) { return n > 63 ? 63 : n; }
template <int N> DI void wait_vm() { static_assert(N >= 0, ""); asm volatile("s_waitcnt vmcnt(%0)" ::"n"(clampvm(N)) : "memory"); }
template <class T> DI void pin(T& v) { asm volatile("" : "+v"(v)); }

DI u32x4 trfrag(LAS char* p) {
  s16x4 lo = __builtin_amdgcn_ds_read_tr16_b64_v4i16((LAS s16x4*)p);
  s16x4 hi = __builtin_amdgcn_ds_read_tr16_b64_v4i16((LAS s16x4*)(p + 512));
  u32x2 a = __builtin_bit_cast(u32x2, lo), b = __builtin_bit_cast(u32x2, hi);
  return u32x4{a.x, a.y, b.x, b.y};
}
DI f32x16 mfma(u32x4 a, u32x4 b, f32x16 c) {
  return __builtin_amdgcn_mfma_f32_32x32x16_bf16(__builtin_bit_cast(bf16x8, a), __builtin_bit_cast(bf16x8, b), c, 0, 0, 0);
}
DI void mfma_at(f32x16& c, u32x4 a, u32x4 b) { c = mfma(a, b, c); asm volatile("" : "+v"(c)); }
DI u32 pk(float a, float b) { bf16x2 c; c[0] = (__bf16)a; c[1] = (__bf16)b; return __builtin_bit_cast(u32, c); }
DI u32x4 pk8(const f32x16& v, int s) { return u32x4{pk(v[8 * s], v[8 * s + 1]), pk(v[8 * s + 2], v[8 * s + 3]), pk(v[8 * s + 4], v[8 * s + 5]), pk(v[8 * s + 6], v[8 * s + 7])}; }
DI float glu(float g, float u) {
  return u * __builtin_amdgcn_rcpf(1.f + __builtin_amdgcn_exp2f(g * -1.4426950408889634f)); }
DI float xhalf_max(float v) { auto r = __builtin_amdgcn_permlane32_swap(__float_as_uint(v), __float_as_uint(v), false, false); return fmaxf(__uint_as_float(r[0]), __uint_as_float(r[1])); }
DI float xhalf_sum(float v) { auto r = __builtin_amdgcn_permlane32_swap(__float_as_uint(v), __float_as_uint(v), false, false); return __uint_as_float(r[0]) + __uint_as_float(r[1]); }

DI void spin(const char* ws, u32 off, bool need, u64 tag) {
  const GAS u64* p = (const GAS u64*)(ws + off);
#pragma unroll 1
  for (int i = 0; i < SPIN_CAP; ++i) {
    u64 v = __hip_atomic_load(p, __ATOMIC_RELAXED, __HIP_MEMORY_SCOPE_AGENT);
    if (__all(!need || v == tag)) break;
    __builtin_amdgcn_s_sleep(2);
  }
  asm volatile("" ::: "memory");
}
DI bool match(u32x2 v, bool need, u64 tag) {
  return __all(!need || (v.x == (u32)tag && v.y == (u32)(tag >> 32))); }
DI u64 tag_of(u32x2 ev) {
  u64 e = ((u64)__builtin_amdgcn_readfirstlane(ev.y) << 32) | (u32)__builtin_amdgcn_readfirstlane(ev.x);
  return e * 6364136223846793005ull + 1442695040888963407ull;
}

#define STAMP(i)
#define DIAGP
#define DIAGA
#define STAMP2(i)
template <int N> DI void sync_vm() { static_assert(N >= 0, ""); asm volatile("s_waitcnt vmcnt(%0)\n\ts_barrier" ::"n"(clampvm(N)) : "memory"); }

template <int I, int N, class Fn> DI void static_for(Fn&& f) {
  if constexpr (I < N) { f(IC<I>{}); static_for<I + 1, N>(f); }
}

#define HEAD "KKGKKGKKGK"
#define HEAD_BARS 0x1011414145ull
#define HEAD_N 26
#define CFG 9,20,-2,22,40,57,52,56,64,68,70,76,2,6,6,2,6,6,2,0,1,2,16,16,0
constexpr int kCfg[] = {CFG};
constexpr int PRO_DMA = kCfg[0], HEAD_RATE = kCfg[1], G_TOUCH = kCfg[2], U_KVPUB = kCfg[3], U_H1PUB = kCfg[4], U_H2PUB = kCfg[5];
constexpr int U_POLL1 = kCfg[6], U_CHK1 = kCfg[7], U_POLL2 = kCfg[8], U_CHK2 = kCfg[9], U_POLL3 = kCfg[10], U_CHK3 = kCfg[11];
constexpr int EP1 = kCfg[12];
constexpr int ld_rel(int cl, int k) { int f = kCfg[13 + 3 * cl], a = kCfg[14 + 3 * cl], b = kCfg[15 + 3 * cl]; return k < f ? k / a : (f + a - 1) / a + (k - f) / b; }
#define TAIL 0
#define ORDER 1
constexpr int N2 = ORDER ? 14 : 8, IT_OMG = ORDER ? 28 : 14;
constexpr int NV = 105, U0 = 14;
constexpr int U_H2ST = 56;
#define TPOS 3
constexpr int PRO_OPS = PRO_DMA + 1 + (G_TOUCH == -2);
constexpr int imin(int a, int b) { return a < b ? a : b; }
constexpr int item_of(int i) { return i < IT_OMG ? i : i + 1; }
constexpr int blk_of(int i) { return ORDER || i < 14 ? i : (i < 21 ? i + 14 : i - 7); }
constexpr int ld_iter(int it) { return U0 + (it < 14 ? U_CHK1 + ld_rel(0, it) : (it < 14 + N2 ? U_CHK2 + ld_rel(1, it - 14) : U_CHK3 + ld_rel(2, it - 14 - N2))); }
struct Sched {
  int nhead = 0, vkvst = 0, vg1 = 0;
  int xl[NV + 2] = {}, wl[NV + 2] = {};
  int kind[NV + 2] = {}, idx[NV + 2] = {}, lo[NV + 2] = {}, hi[NV + 2] = {};
  int pk[84] = {}, pi[84] = {}, pj[84] = {};
  bool bar[NV] = {};
  int cum[NV + 1] = {}, base[NV + 1] = {}, nst[NV] = {}, ldlo[NV + 1] = {}, cnt[NV] = {}, pdma[84] = {}, pld[36] = {}, freed[NV] = {};
  int cnt0 = 0; bool ok = true;
  constexpr Sched() {
    const char* h = HEAD;
    int v = 0, n = 0, k = 0, g = 0;
    int nx = 0;
    auto add = [&](int kd, int st, int nit, int nw) {
      const int first = n; int x = -1;
      if (st < 7 && st == nx) { x = n; for (int e = 0; e < 2; ++e) { pk[n] = 0; pi[n] = st; pj[n] = e; ++n; } ++nx; }
      for (int e = 0; e < nit; ++e) { kind[v] = kd; idx[v] = nit * st + e; lo[v] = first; hi[v] = n + nw - 1; xl[v] = x; wl[v] = n; ++v; }
      for (int e = 0; e < nw; ++e) { pk[n] = kd; pi[n] = st; pj[n] = kd ? e : 2; ++n; }
    };
    for (int i = 0; h[i]; ++i) { if (h[i] == 'K') { add(0, k++, 2, 1); nhead = v; } else { add(1, g++, 4, 2); if (g == 7) vg1 = v; } }
    if (k != 7 || g > 7) ok = false;
    vkvst = nhead + 1;
    while (g < 14) { add(1, g++, 4, 2); if (g == 7) vg1 = v; }
    for (int i = 0; i < 35; ++i) { kind[v] = 2; idx[v] = 56 + i; lo[v] = hi[v] = wl[v] = n; pk[n] = 2; pi[n] = i; ++n; ++v; }
    kind[NV] = kind[NV + 1] = 3; hi[NV] = hi[NV + 1] = 83;
    for (v = 0; v < NV; ++v) {
      const int u = v - U0, t = u + 2;
      bar[v] = v < 42 ? (((unsigned long long)(HEAD_BARS) >> v) & 1) : (t < 56 ? t % 8 == 0 : (t < 91 && (t - 56) % 5 == 0));
      if ((u == U_KVPUB || u == U_H1PUB || u == U_H2PUB || (u == U_CHK1 && u != 56) || u == U_CHK2 || u == U_CHK3)) bar[v] = true;
    }
    if (U0 + U_KVPUB <= vkvst || U0 + U_H1PUB <= vg1 + 2 * EP1) ok = false;
    int fr = 0, cc = PRO_DMA, cr = 0, it = 0;
    cum[0] = cc; base[0] = PRO_OPS;
    for (n = 0; n < PRO_DMA; ++n) pdma[n] = n + (G_TOUCH == -2 && n >= TPOS);
    for (v = 0; v < NV; ++v) {
      const int u = v - U0;
      if (bar[v] || (u == 56 && U_CHK1 == 56)) fr = lo[v];
      freed[v] = fr;
      const int pay = 2 * (v == vkvst) + (v == vg1 + EP1) + (v == vg1 + 2 * EP1) + 2 * (u == U_H2ST);
      nst[v] = pay + (v == G_TOUCH) + (u == U_POLL1) + (u == U_POLL2) + (u == U_POLL3);
      ldlo[v] = it;
      while (it < 36 && ld_iter(it) == v) { pld[it] = base[v] + nst[v] + (it - ldlo[v]); ++it; }
      cr += (v < HEAD_N ? HEAD_RATE : (u < 56 ? kCfg[22] : kCfg[23])) - kCfg[24] * pay;
      int t = imin(imin(cc + imin(cr > 0 ? cr / 16 : 0, 2), fr + 20), 84);
      cr -= 16 * (t - cc); if (cr > 16) cr = 16;
      for (n = cc; n < t; ++n) pdma[n] = base[v] + nst[v] + (it - ldlo[v]) + (n - cc);
      base[v + 1] = base[v] + nst[v] + (it - ldlo[v]) + (t - cc);
      cc = t; cum[v + 1] = cc;
    }
    ldlo[NV] = it;
    if (it != 36 || cc != 84) ok = false;
    for (v = -1; v < NV; ++v) {
      if (v >= 0 && !bar[v]) continue;
      int nx = v + 1; while (nx < NV && !bar[nx]) ++nx;
      const int need = hi[nx + 1], at = v < 0 ? 0 : v;
      if (need >= cum[at]) ok = false;
      int c = base[at] - pdma[need] - 1;
      const int u = v - U0;
      if (u == U_KVPUB) c = imin(c, base[v] - (base[vkvst] + nst[vkvst]));
      if (u == U_H1PUB) c = imin(c, base[v] - (base[vg1 + 2 * EP1] + nst[vg1 + 2 * EP1]));
      if (u == U_H2PUB) c = imin(c, base[v] - (base[U0 + U_H2ST] + nst[U0 + U_H2ST]));
      if (v < 0) cnt0 = c; else cnt[v] = c;
    }
    for (int i = 0; i < 35; ++i) if (ld_iter(item_of(i)) > U0 + 56 + i) ok = false;
  }
};
constexpr Sched SA{};
static_assert(SA.ok, "schedule: a piece or operand is used before it is requested, or HEAD is malformed");
static_assert(U_POLL1 < U_CHK1 && U_CHK1 < U_POLL2 && U_POLL2 < U_CHK2 && U_CHK2 < U_POLL3 && U_POLL3 < U_CHK3, "");
static_assert(U_KVPUB < U_CHK1 && U_H2PUB > U_H2ST && U_H1PUB < U_CHK1 && U_H2PUB < U_CHK2, "a workgroup publishes before it waits");
static_assert(SA.freed[NV - 1] >= 72, "the reduction scratch (slots 4..11 = pieces 64..71) must be free at the end");

struct Ctx {
  LAS char* sm; u32 m0w;
  rsrc_t rx, rqkv, rwg, rwu, rwd, rwo, rws;
  const char* ws;
  rsrc_t rt; u32 toff, tw;
  u32 vx, vkv, vg, vu, vw2;
};

template <int N> DI void issueA(const Ctx& c) {
  constexpr u32 dst = (N % 20) * SLOT;
  constexpr int k = SA.pk[N], st = SA.pi[N], j = SA.pj[N];
  if constexpr (k == 0) {
    if constexpr (j < 2) dma(c.m0w + dst, c.vx, c.rx, j * 896 + st * 128);
    else dma(c.m0w + dst, c.vkv, c.rqkv, st * 64 * 2688);
  } else if constexpr (k == 1) {
    if constexpr (j == 0) dma(c.m0w + dst, c.vg, c.rwg, (st % 7) * 64 * 3584 + (st / 7) * 1792);
    else dma(c.m0w + dst, c.vu, c.rwu, (st % 7) * 64 * 3584 + (st / 7) * 1792);
  } else if constexpr (blk_of(st) < 28) dma(c.m0w + dst, c.vw2, c.rwd, blk_of(st) * 64 * 896);
  else dma(c.m0w + dst, c.vw2, c.rwo, (blk_of(st) - 28) * 64 * 896);
}

DI void type_a(Ctx& c, int w, int lane, int mt, int role, int R2, int n2, void* out DIAGP) {
  const int r = lane & 31, hi = lane >> 5, blk = (lane >> 4) & 1, q = (lane & 15) >> 2, p = lane & 3;
  const int sp = ((p & 1) << 1) | (p >> 1);
  const int rg = w & 3, cg = w >> 2, T = mt * 4 + rg;
  const bool isv = role >= 7;
  LAS char* sm = c.sm;
  u32 td = 0;
  static_for<2, TPOS>([&](auto N) { issueA<N>(c); });
  if constexpr (G_TOUCH == -2) c.tw = touch(c.toff, c.rt);
  static_for<TPOS, PRO_DMA>([&](auto N) { issueA<N>(c); });
  u32x2 ev = ld64_sc1(w == 0 ? WS_E : OOB, c.rws, 0);
  const int R = 32 * (rg & 1) + r, xm = (R >> 1) & 7;
  int xa[4];
#pragma unroll
  for (int s = 0; s < 4; ++s) xa[s] = (rg >> 1) * SLOT + R * 128 + (((2 * s + hi) ^ xm) << 4);
  const int rowb = (8 * hi + q) * 128, q2 = q & 2;
  const int akv = rowb + (((2 * cg + blk) ^ q2) << 5) + (isv ? p : sp) * 8;
  int agu[2];
#pragma unroll
  for (int i = 0; i < 2; ++i) agu[i] = blk * SLOT + rowb + (((2 * cg + i) ^ q2 ^ blk) << 5) + sp * 8;
  int aguw[2][3];
#pragma unroll
  for (int i = 0; i < 2; ++i)
#pragma unroll
    for (int k = 0; k < 3; ++k) { aguw[i][k] = agu[i] + k * 65536; pin(aguw[i][k]); }
  const int rt = w & 1, kq = w >> 1;
  int aw2[2], aw2w[2][3];
#pragma unroll
  for (int i = 0; i < 2; ++i) aw2[i] = kq * 2048 + rowb + (((2 * i + blk) ^ q2) << 5) + (TAIL ? sp : p) * 8;
  const int T2 = (R2 >> 1) * 4 + 2 * rt + (R2 & 1);

  u32x4 xf[28], wf[2][2], af[35], hp[2];
  f32x16 akvacc = {}, g1[2] = {}, g2[2] = {}, ah[2] = {}, ao[2] = {};
  u32x2 pollv; u32 omg; u64 tag;
  const u32 lane16 = lane * 16;
  const u32 poll_off = WS_FLAG + (R2 >> 1) * 256 + (lane & 15) * 8;
  const bool need_h = (lane & 15) < 14;
  const u32 a2_off = WS_A2 + T2 * A2_T + kq * 1024 + lane16;
  const u32 h_off = WS_A2 + T * A2_T + (role * 4 + 2 * cg) * 1024 + lane16;

  auto read_w = [&](auto V, auto I) {
    constexpr int v = V, i = I, k = SA.kind[v], u = SA.idx[v], sl = SA.wl[v] % 20;
    if constexpr (k < 2 && SA.xl[v] >= 0 && (k == 0 || i == 0)) {
      constexpr int ks = k == 0 ? 2 * u + i : u, xs = SA.xl[v] % 20;
      const int wrap = xs == 19 ? (rg >> 1) * (-20 * SLOT) : 0;
      xf[ks] = *(LAS u32x4*)(sm + xa[ks % 4] + wrap + xs * SLOT);
    }
    if constexpr (k == 0) {
      wf[v & 1][i] = trfrag(sm + akv + sl * SLOT + ((2 * u + i) % 4) * 2048);
    } else if constexpr (k == 1) {
      const int wrap = sl == 19 ? blk * (-20 * SLOT) : 0;
      wf[v & 1][i] = trfrag(sm + aguw[i][sl / 8] + wrap + (sl % 8) * SLOT + (u % 4) * 2048);
    } else if constexpr (k == 2) {
      if constexpr (u == 56) {
#pragma unroll
        for (int e = 0; e < 3; ++e) { aw2w[i][e] = aw2[i] + e * 65536; pin(aw2w[i][e]); }
      }
      wf[v & 1][i] = trfrag(sm + aw2w[i][sl / 8] + (sl % 8) * SLOT);
    }
  };
  auto publish = [&](int cls) {
    if (w == 0) __builtin_amdgcn_raw_buffer_store_b64(u32x2{(u32)tag, (u32)(tag >> 32)}, c.rws, lane == 0 ? role * 8 : OOB, WS_FLAG + cls * 4096 + mt * 256, SC1);
  };
  auto load_item = [&](auto IT) {
    constexpr int it = IT;
    if constexpr (it == IT_OMG) omg = ld32_sc1(r * 4, c.rws, WS_OMG + T2 * 128);
    else { constexpr int i = it < IT_OMG ? it : it - 1; af[i] = ld128_sc1(a2_off, c.rws, blk_of(i) * 4096); }
  };

  STAMP(1);
  sync_vm<SA.cnt0>();
  STAMP(2);
  static_for<0, 4>([&](auto S) { read_w(IC<S / 2>{}, IC<S % 2>{}); });
  SCHED();
  auto check = [&](auto CNT, int cls, bool need) {
    wait_vm<CNT>();
    pin(pollv);
    if (w == 0 && !match(pollv, need, tag)) spin(c.ws, poll_off + cls * 4096, need, tag);
  };
  auto iter = [&](auto V, auto ISV) {
    constexpr int v = V, b = v & 1, kd = SA.kind[v], ui = SA.idx[v], u = v - U0;
    if constexpr (v == SA.nhead) STAMP(3);
    if constexpr (v == SA.vg1) STAMP(4);
    if constexpr (u == 56) STAMP(5);
    if constexpr (u == 70) STAMP(8);
    if constexpr (u == 77) STAMP(9);
    if constexpr (u == U_CHK2) STAMP2(56);
    if constexpr (u == U_CHK3) STAMP2(58);
    if constexpr (u == U_KVPUB) STAMP2(50);
    if constexpr (u == U_H1PUB) STAMP2(52);
    if constexpr (u == U_H2PUB) STAMP2(54);
    if constexpr (v % 4 == 0) STAMP2(16 + v / 4);
    if constexpr (u == U_CHK1 && u != 56) check(IC<SA.base[v] - SA.base[U0 + U_POLL1] - 1>{}, 1, need_h);
    if constexpr (u == U_CHK2) check(IC<SA.base[v] - SA.base[U0 + U_POLL2] - 1>{}, 2, ORDER ? need_h : !need_h);
    if constexpr (u == U_CHK3) check(IC<SA.base[v] - SA.base[U0 + U_POLL3] - 1>{}, 2, ORDER ? !need_h : need_h);
    if constexpr (SA.bar[v]) sync_vm<SA.cnt[v]>();
    if constexpr (u == U_CHK2) STAMP2(57);
    if constexpr (u == U_CHK3) STAMP2(59);
    if constexpr (u == imin(U_KVPUB, U_H1PUB)) {
      pin(ev); if constexpr (G_TOUCH < 0) pin(c.tw);
      tag = tag_of(ev);
    }
    if constexpr (u == U_KVPUB) { publish(0); STAMP2(51); }
    if constexpr (u == U_H1PUB) { publish(1); STAMP2(53); }
    if constexpr (u == U_H2PUB) { publish(2); STAMP2(55); }
    if constexpr (v == G_TOUCH) td = touch(c.toff, c.rt);
    if constexpr (u == U_H1PUB) pin(td);
    if constexpr (u == U_POLL1) pollv = ld64_sc1(w == 0 ? poll_off : OOB, c.rws, 4096);
    if constexpr (u == U_POLL2 || u == U_POLL3) pollv = ld64_sc1(w == 0 ? poll_off : OOB, c.rws, 8192);
    if constexpr (v == SA.vkvst) {
#pragma unroll
      for (int s = 0; s < 2; ++s) {
        const u32 off = isv ? WS_V + T * KV_T + (s * 2 + hi) * 7168 + ((role - 7) * 64 + cg * 32 + r) * 16
                            : WS_K + T * KV_T + (role * 8 + cg * 4 + 2 * s) * 512 + lane16;
        st128(pk8(akvacc, s), c.rws, off, 0);
      }
    }
    if constexpr (v == SA.vg1 + EP1) st128(hp[0], c.rws, h_off, 0);
    if constexpr (v == SA.vg1 + 2 * EP1) st128(hp[1], c.rws, h_off, 1024);
    if constexpr (u == U_H2ST) {
#pragma unroll
      for (int t = 0; t < 2; ++t) {
#pragma unroll
        for (int j = 0; j < 8; j += 2) hp[t][j / 2] = pk(glu(g2[t][j], g2[t][8 + j]), glu(g2[t][j + 1], g2[t][9 + j]));
        st128(hp[t], c.rws, h_off, (56 + t) * 1024);
        SCHED();
      }
    }
    asm volatile("" ::: "memory");
    if constexpr (u == 56) STAMP2(44);
    if constexpr (u == 56 && U_CHK1 == 56) {
      check(IC<SA.base[v] + SA.nst[v] - SA.base[U0 + U_POLL1] - 1>{}, 1, need_h);
      asm volatile("s_barrier" ::: "memory");
      STAMP2(45);
    }
    constexpr bool ld_first = u >= 56 && ld_iter(item_of(u < 56 ? 0 : u - 56)) == v;
    constexpr int nld = SA.ldlo[v + 1] - SA.ldlo[v], nd = SA.cum[v + 1] - SA.cum[v];
    if constexpr (ld_first) static_for<SA.ldlo[v], SA.ldlo[v + 1]>(load_item);
    if constexpr (u >= 56) {
      wait_vm<SA.base[v] + SA.nst[v] + (ld_first ? nld : 0) - SA.pld[item_of(u - 56)] - 1>();
      pin(af[u - 56]);
      if constexpr (u == 56) STAMP2(46);
      if constexpr (u == 70) STAMP2(47);
      if constexpr (u == 77) STAMP2(48);
    }
    SCHED();
    static_for<0, 2>([&](auto I) {
      constexpr int i = I;
      if constexpr (kd == 0) { if constexpr (ISV) mfma_at(akvacc, xf[2 * ui + i], wf[b][i]); else mfma_at(akvacc, wf[b][i], xf[2 * ui + i]); }
      else if constexpr (ui < 28) mfma_at(g1[i], wf[b][i], xf[ui]);
      else if constexpr (ui < 56) mfma_at(g2[i], wf[b][i], xf[ui - 28]);
      else if constexpr (blk_of(ui - 56) < 28) mfma_at(ah[i], wf[b][i], af[ui - 56]);
      else mfma_at(ao[i], wf[b][i], af[ui - 56]);
      SCHED();
      read_w(IC<v + 2>{}, I);
      if constexpr (v >= SA.vg1 && v < SA.vg1 + 2 * EP1) {
        constexpr int t = (v - SA.vg1) / EP1, per = 4 / EP1, k0 = ((v - SA.vg1) % EP1) * per;
#pragma unroll
        for (int k = k0 + (i * per + 1) / 2; k < k0 + ((i + 1) * per + 1) / 2; ++k) {
          u32 hv = pk(glu(g1[t][2 * k], g1[t][8 + 2 * k]), glu(g1[t][2 * k + 1], g1[t][9 + 2 * k]));
          pin(hv);
          hp[t][k] = hv;
        }
      }
      if constexpr (i == 0 && !ld_first) static_for<SA.ldlo[v], SA.ldlo[v + 1]>(load_item);
      if constexpr (i == 0 && nd == 2) issueA<SA.cum[v]>(c);
      if constexpr (i == 1 && nd >= 1) issueA<SA.cum[v + 1] - 1>(c);
      SCHED();
    });
  };
  constexpr int vk0 = [] { int v = 0; while (SA.kind[v] != 0) ++v; return v; }();
  static_for<0, vk0>([&](auto V) { iter(V, IC<0>{}); });
  if (isv) static_for<vk0, SA.nhead>([&](auto V) { iter(V, IC<1>{}); });
  else static_for<vk0, SA.nhead>([&](auto V) { iter(V, IC<0>{}); });
  static_for<SA.nhead, NV>([&](auto V) { iter(V, IC<0>{}); });
  pin(omg);
  STAMP(6);
  const float om = __uint_as_float(omg);
  LAS char* red = sm + 4 * SLOT;
#pragma unroll
  for (int t = 0; t < 2; ++t)
#pragma unroll
    for (int a = 0; a < 4; ++a) {
      f32x4 v;
#pragma unroll
      for (int e = 0; e < 4; ++e) v[e] = ah[t][4 * a + e] * om + ao[t][4 * a + e];
      *(LAS f32x4*)(red + kq * 16384 + (2 * r + rt) * 256 + (((8 * t + 2 * a + hi) ^ (r & 15)) << 4)) = v;
    }
  asm volatile("s_waitcnt lgkmcnt(0)\n\ts_barrier" ::: "memory");
  STAMP(7);
  {
    const int row = w * 8 + (lane >> 3), c8 = lane & 7, m = (row >> 1) & 15;
    f32x4 s0 = {}, s1 = {};
#pragma unroll
    for (int k = 0; k < 4; ++k) {
      s0 += *(LAS f32x4*)(red + k * 16384 + row * 256 + (((2 * c8) ^ m) << 4));
      s1 += *(LAS f32x4*)(red + k * 16384 + row * 256 + (((2 * c8 + 1) ^ m) << 4));
    }
    u32x4 o = {pk(s0[0], s0[1]), pk(s0[2], s0[3]), pk(s1[0], s1[1]), pk(s1[2], s1[3])};
    u32x4* op = (u32x4*)((char*)out + (size_t)(R2 * 64 + row) * 896 + n2 * 128 + c8 * 16);
    __builtin_nontemporal_store(o, op);
  }
}

constexpr int PRO_B = 12;
constexpr int freedB(int g) { return 6 * ((g + 1) / 4); }
constexpr int cumB(int g) {
  int cc = PRO_B;
  for (int j = 0; j <= g; ++j) { int t = cc + 2, cap = freedB(j) + 18; t = t < cap ? t : cap; cc = t < 42 ? t : 42; }
  return cc;
}
constexpr int n_stB(int g) { return 0; }
constexpr int baseB(int g) { int t = PRO_B; for (int j = 0; j < g; ++j) t += n_stB(j) + cumB(j) - cumB(j - 1); return t; }
constexpr int pos_dmaB(int n) {
  if (n < PRO_B) return n;
  for (int g = 0; g < 28; ++g) if (n < cumB(g)) return baseB(g) + n_stB(g) + (n - cumB(g - 1));
  return -1000;
}
constexpr int sync_cntB(int g) {
  int n = baseB(g) - pos_dmaB(6 * (g / 4 + 2) - 1) - 1;
  return n;
}
template <int N> DI void issueB(const Ctx& c) {
  constexpr int kc = N / 6, j = N % 6;
  constexpr u32 dst = (N % 18) * SLOT;
  if constexpr (j < 2) dma(c.m0w + dst, c.vx, c.rx, j * 896 + kc * 128);
  else dma(c.m0w + dst, c.vkv, c.rqkv, kc * 64 * 2688 + (j - 2) * 128);
}

DI void type_b(Ctx& c, int w, int lane, int mt, int hf, const void* gw DIAGP) {
  const int r = lane & 31, hi = lane >> 5, blk = (lane >> 4) & 1, q = (lane & 15) >> 2, p = lane & 3;
  const int sp = ((p & 1) << 1) | (p >> 1);
  const int rg = w & 3, cg = w >> 2, T = mt * 4 + rg;
  LAS char* sm = c.sm;
  if (w == 0) dma(c.m0w + 18 * SLOT, lane * 16, mk(gw, 896), 0);
  u32x2 ev = ld64_sc1(w == 0 ? WS_E : OOB, c.rws, 0);
  static_for<2, PRO_B>([&](auto N) { issueB<N>(c); });
  const int R = 32 * (rg & 1) + r, xm = (R >> 1) & 7;
  int xa[4], aq[4];
#pragma unroll
  for (int s = 0; s < 4; ++s) xa[s] = (rg >> 1) * SLOT + R * 128 + (((2 * s + hi) ^ xm) << 4);
  const int h0 = 8 * hf + 4 * cg;
#pragma unroll
  for (int i = 0; i < 4; ++i) {
    const int col = 28 * (h0 + i) - 4 * (i & 1) - 192 * hf + 16 * blk + 4 * sp;
    aq[i] = (2 + (col >> 6)) * SLOT + (8 * hi + q) * 128 + (((col & 63) * 2) ^ ((q & 2) << 5));
  }
  const bool first = (mt == 0) && !(rg & 1);
  const int Tp = first ? T : ((rg & 1) ? T - 1 : T - 3);
  const u32 lane16 = lane * 16, lo16 = hi ? OOB : lane16;
  const u32 ob = WS_A2 + T * A2_T + (1792 + 28 * h0) * 64;
  const u32 omg_off = (hf == 0 && cg == 0 && hi == 0) ? r * 4 : OOB;
  const u32 poll_off = WS_FLAG + ((lane & 16) && mt > 0 ? mt - 1 : mt) * 256 + (lane & 15) * 8;
  const bool poll_need = (lane & 15) < 14;
  u32x4 xf[2], gf[2], wf[2][4];
  u32x4 kf[4][2], kp[2], vf[4][2], vp[4];
  f32x16 qa[4] = {};
  const int sr = (r & 19) | ((r & 4) << 1) | ((r & 8) >> 1);
  const u32 kp_off = (28 * (r >> 3) - 4 * ((r >> 3) & 1)) * 64 + (hi * 32 + 24 + (r & 7)) * 16;
  const u32 v_off = hi * 7168 + sr * 16;
  auto load_kv = [&](auto IT) {
    constexpr int it = IT;
    if constexpr (it < 2) kp[it] = ld128_sc1(kp_off, c.rws, WS_K + Tp * KV_T + 28 * h0 * 64 + it * 1024);
    else if constexpr (it < 10) {
      constexpr int i = (it - 2) / 2, k = it & 1;
      kf[i][k] = ld128_sc1(lane16, c.rws, WS_K + T * KV_T + (28 * (h0 + i) - 4 * (i & 1)) * 64 + k * 1024);
    } else {
      constexpr int i = (it - 10) / 3, k = (it - 10) % 3;
      const int wh = 28 * (h0 + i) - 4 * (i & 1);
      if constexpr (k == 0) vp[i] = ld128_sc1(v_off, c.rws, WS_V + Tp * KV_T + 14336 + wh * 16);
      else vf[i][k - 1] = ld128_sc1(v_off, c.rws, WS_V + T * KV_T + (k - 1) * 14336 + wh * 16);
    }
  };
  auto read_x = [&](auto G) {
    constexpr int g = G;
    if constexpr (g < 28) {
      xf[g & 1] = *(LAS u32x4*)(sm + xa[g % 4] + ((6 * (g / 4)) % 18) * SLOT);
      gf[g & 1] = *(LAS u32x4*)(sm + 18 * SLOT + g * 32 + hi * 16);
    }
  };
  auto read_w = [&](auto G, auto I) {
    constexpr int g = G;
    if constexpr (g < 28) wf[g & 1][I] = trfrag(sm + aq[I] + ((6 * (g / 4)) % 18) * SLOT + (g % 4) * 2048);
  };
  STAMP(1);
  sync_vm<PRO_B - 6>();
  if constexpr (G_TOUCH == -1) pin(c.tw);
  STAMP(2);
  read_x(IC<0>{});
  static_for<0, 4>([&](auto I) { read_w(IC<0>{}, I); });
  SCHED();
  static_for<0, 28>([&](auto G) {
    constexpr int g = G, b = g & 1;
    if constexpr (g % 4 == 3 && g < 27) { STAMP2(16 + 2 * (g / 4)); asm volatile("s_waitcnt lgkmcnt(0)" ::: "memory"); sync_vm<sync_cntB(g)>(); STAMP2(17 + 2 * (g / 4)); }
    SCHED();
    static_for<0, 4>([&](auto I) {
      constexpr int i = I;
      mfma_at(qa[i], wf[b][i], xf[b]);
      SCHED();
      if constexpr (i == 0) read_x(IC<g + 1>{});
      read_w(IC<g + 1>{}, I);
      if constexpr (i < 2) {
        wf[b][2][2 * i] = r == 28 ? gf[b][2 * i] : wf[b][2][2 * i];
        wf[b][2][2 * i + 1] = r == 28 ? gf[b][2 * i + 1] : wf[b][2][2 * i + 1];
        pin(wf[b][2]);
      }
      if constexpr (i >= 2 && cumB(g - 1) + i - 2 < cumB(g)) issueB<cumB(g - 1) + i - 2>(c);
      SCHED();
    });
  });
  STAMP(3);
  const float z = __uint_as_float(__builtin_amdgcn_permlane32_swap(__float_as_uint(qa[2][12]), __float_as_uint(qa[2][12]), false, false)[1]);
  const float gate = __builtin_amdgcn_rcpf(1.f + __builtin_amdgcn_exp2f(z * -1.4426950408889634f));
  const u32 omg = __float_as_uint(__builtin_amdgcn_rcpf(1.f + __builtin_amdgcn_exp2f(z * 1.4426950408889634f)));
  int ab[4];
  u32 msk[5][2];
#pragma unroll
  for (int b = 0; b < 4; ++b) ab[b] = (r >> 3) - ((r & 7) < 4 * hi + b ? 1 : 0);
#pragma unroll
  for (int a = 0; a < 5; ++a)
#pragma unroll
    for (int e = 0; e < 2; ++e) msk[a][e] = (ab[2 * e] == a - 1 ? 0xFFFFu : 0u) | (ab[2 * e + 1] == a - 1 ? 0xFFFF0000u : 0u);
  const float csc = 0.18898223650461362f * 1.4426950408889634f;
  __builtin_amdgcn_raw_buffer_store_b32(omg, c.rws, omg_off, WS_OMG + T * 128, SC1);
  asm volatile("" ::: "memory");
  u32 p01[4], p23[4];
  u32x4 qf[4][2];
  static_for<0, 4>([&](auto I) {
    constexpr int i = I;
    qf[i][0] = pk8(qa[i], 0); qf[i][1] = pk8(qa[i], 1);
    if (i & 1) { qf[i][0][0] = hi ? qf[i][0][0] : 0u; qf[i][0][1] = hi ? qf[i][0][1] : 0u; }
    else       { qf[i][1][2] = hi ? 0u : qf[i][1][2]; qf[i][1][3] = hi ? 0u : qf[i][1][3]; }
    pin(qf[i][0]); pin(qf[i][1]);
  });
#pragma unroll
  for (int a = 0; a < 5; ++a) { pin(msk[a][0]); pin(msk[a][1]); }
  pin(ev);
  const u64 tag = tag_of(ev);
  STAMP(8);
  if (w == 0) spin(c.ws, poll_off, poll_need, tag);
  asm volatile("s_barrier" ::: "memory");
  STAMP(9);
  static_for<0, 10>(load_kv);
  f32x16 so[2], sv[2];
  static_for<0, 5>([&](auto I) {
    constexpr int i = I;
    if constexpr (i < 4) {
      static_for<10 + 3 * i, 13 + 3 * i>(load_kv);
      wait_vm<9 + i>();
      if constexpr (i == 0) { pin(kp[0]); pin(kp[1]); STAMP(4); }
      pin(kf[i][0]); pin(kf[i][1]);
      so[i & 1] = mfma(kf[i][0], qf[i][0], f32x16{}); sv[i & 1] = mfma(kp[0], qf[i][0], f32x16{});
      mfma_at(so[i & 1], kf[i][1], qf[i][1]); mfma_at(sv[i & 1], kp[1], qf[i][1]);
    }
    if constexpr (i > 0) {
      constexpr int j = i - 1;
      float v[4];
#pragma unroll
      for (int b = 0; b < 4; ++b) {
        float t = first ? -1e30f : sv[j & 1][4 * j + b];
#pragma unroll
        for (int a = 0; a < 4; ++a) t = ab[b] == a ? so[j & 1][4 * a + b] : t;
        v[b] = t;
      }
      const float mx = xhalf_max(fmaxf(fmaxf(v[0], v[1]), fmaxf(v[2], v[3])));
#pragma unroll
      for (int b = 0; b < 4; ++b) v[b] = __builtin_amdgcn_exp2f((v[b] - mx) * csc);
      const float sc = gate * __builtin_amdgcn_rcpf(xhalf_sum((v[0] + v[1]) + (v[2] + v[3])));
      p01[j] = pk(v[0] * sc, v[1] * sc); p23[j] = pk(v[2] * sc, v[3] * sc);
      pin(p01[j]); pin(p23[j]);
    }
    SCHED();
  });
  STAMP(10);
  static_for<0, 2>([&](auto J) {
    constexpr int j = J;
    wait_vm<(j == 0 ? 6 : 4)>();
    u32x4 op[2][2];
#pragma unroll
    for (int e = 0; e < 2; ++e) {
      const int i = 2 * j + e;
      pin(vp[i]); pin(vf[i][0]); pin(vf[i][1]);
      f32x16 oa = {};
      oa = mfma(vp[i], u32x4{0u, 0u, p01[i] & msk[0][0], p23[i] & msk[0][1]}, oa);
      oa = mfma(vf[i][0], u32x4{p01[i] & msk[1][0], p23[i] & msk[1][1], p01[i] & msk[2][0], p23[i] & msk[2][1]}, oa);
      oa = mfma(vf[i][1], u32x4{p01[i] & msk[3][0], p23[i] & msk[3][1], p01[i] & msk[4][0], p23[i] & msk[4][1]}, oa);
      op[e][0] = pk8(oa, 0); op[e][1] = pk8(oa, 1);
    }
#pragma unroll
    for (int e = 0; e < 2; ++e) { auto t = __builtin_amdgcn_permlane32_swap(op[0][1][e], op[1][0][e], false, false); op[0][1][e] = t[0]; op[1][0][e] = t[1]; }
    const u32 o2 = ob + j * 3584;
    st128(op[0][0], c.rws, lane16, o2);
    st128(op[0][1], c.rws, lo16, o2 + 1024);
    st128(op[1][0], c.rws, lane16, o2 + 1536);
    st128(op[1][1], c.rws, lane16, o2 + 2560);
  });
  STAMP(5);
  sync_vm<0>();
  if (w == 0) __builtin_amdgcn_raw_buffer_store_b64(u32x2{(u32)tag, (u32)(tag >> 32)}, c.rws, lane == 0 ? (14 + hf) * 8 : OOB, WS_FLAG + 8192 + mt * 256, SC1);
  STAMP(6);
  if (mt == 0 && hf == 0 && w == 0) {
#pragma unroll 1
    for (int i = 0; i < 4; ++i) spin(c.ws, WS_FLAG + 8192 + (i * 4 + (lane >> 4)) * 256 + (lane & 15) * 8, true, tag);
    __builtin_amdgcn_raw_buffer_store_b64(u32x2{(u32)tag, (u32)(tag >> 32)}, c.rws, lane == 0 ? 0 : OOB, WS_E, SC1);
  }
}

__global__ __launch_bounds__(512, 2) void fused_kernel(const void* __restrict__ x, const void* __restrict__ wg, const void* __restrict__ wu,
                                                       const void* __restrict__ wd, const void* __restrict__ wo, const void* __restrict__ wqkv,
                                                       const void* __restrict__ gw, void* __restrict__ out, void* __restrict__ ws) {
  __shared__ __attribute__((aligned(1024))) char smem[163840];
  const int lane = threadIdx.x & 63, w = __builtin_amdgcn_readfirstlane(threadIdx.x >> 6);
  const void* tp = ((const void* const __attribute__((address_space(4)))*)__builtin_amdgcn_kernarg_segment_ptr())[w];
  const int b = blockIdx.x, xc = b & 7, ix = b >> 3;
  const int mq = xc >> 1, mt = mq * 4 + (ix & 3), rl = ix >> 2, rh = xc & 1;
  const bool isa = rl < 7;
  const int role = rh * 7 + rl;
  Ctx c;
  c.sm = (LAS char*)smem;
  c.m0w = (u32)(size_t)c.sm + w * 1024;
  c.rx = mk(x, 2048 * 896); c.rqkv = mk(wqkv, 448 * 2688); c.rwg = mk(wg, 448 * 3584); c.rwu = mk(wu, 448 * 3584);
  c.rwd = mk(wd, 1792 * 896); c.rwo = mk(wo, 448 * 896); c.rws = mk(ws, WS_END);
  c.ws = (const char*)ws;
  const int r8 = lane >> 3, c8 = lane & 7, R = 8 * w + r8;
  c.vx = (mt * 128 + 2 * R) * 896 + ((c8 ^ ((R >> 1) & 7)) << 4);
  const int sw = (c8 ^ ((r8 & 2) << 1)) << 4;
  const int isai = (rl - 7) >> 31;
  c.vkv = R * 2688 + ((896 + role * 128) & isai) + ((rh * 384) & ~isai) + sw;
  auto first3 = [&] { static_for<0, 2>([&](auto N) { issueA<N>(c); }); };
  if constexpr (G_TOUCH != -1) first3();
  {
    auto tab = [&](u64 t) { return (int)((t >> (8 * w)) & 255); };
    const int run = (lane * 37) >> 8, l = lane - 7 * run;
    const int gu = tab(0x010100ull), bb = tab(0x010000010100ull), stride = tab(0x1507071C1C00ull) * 128;
    const int row0 = tab(0x0208000000ull) * b + bb * (mq * 112 + ix * 4);
    const int n = tab(0x1C0E38383800ull) & isai;
    const u32 o = (row0 + (run >> gu)) * stride + ((run & gu) * 14 + bb * rh * 7 + tab(0x070000000000ull) + l) * 128;
    c.toff = lane < n ? o : OOB;
    c.rt = mk(tp, (448 << tab(0x02000000ull)) * stride);
    if constexpr (G_TOUCH == -1) c.tw = touch(c.toff, c.rt);
  }
  if constexpr (G_TOUCH == -1) first3();
  if (__builtin_expect(isa, 1)) {
    c.vg = R * 3584 + role * 128 + sw;
    c.vu = R * 3584 + role * 128 + (sw ^ 32);
    c.vw2 = R * 896 + rl * 128 + sw;
    type_a(c, w, lane, mt, role, xc * 4 + (ix & 3), rl, out DIAGA);
  } else {
    type_b(c, w, lane, mt, rh, gw DIAGA);
  }
}

extern "C" void kernel_launch(void* const* d_in, const int* in_sizes, int n_in, void* d_out, int out_size, void* d_ws, size_t ws_size,
                              hipStream_t stream) {
  fused_kernel<<<256, 512, 0, stream>>>(d_in[0], d_in[4], d_in[5], d_in[6], d_in[2], d_in[1], d_in[7], d_out, d_ws);
}
```

```cpp
#include <hip/hip_runtime.h>
#include <hip/hip_bf16.h>

typedef unsigned long long u64;
typedef unsigned u32;
typedef __bf16 bf16x8 __attribute__((ext_vector_type(8)));
typedef __bf16 bf16x2 __attribute__((ext_vector_type(2)));
typedef short s16x4 __attribute__((ext_vector_type(4)));
typedef u32 u32x4 __attribute__((ext_vector_type(4)));
typedef u32 u32x2 __attribute__((ext_vector_type(2)));
typedef float f32x16 __attribute__((ext_vector_type(16)));
typedef float f32x4 __attribute__((ext_vector_type(4)));
typedef __amdgpu_buffer_rsrc_t rsrc_t;
#define LAS __attribute__((address_space(3)))
#define GAS __attribute__((address_space(1)))
#define DI __device__ __forceinline__
#define SCHED() __builtin_amdgcn_sched_barrier(0)
template <int V> using IC = std::integral_constant<int, V>;

constexpr u32 WS_E = 0;
constexpr u32 WS_FLAG = 4096;
constexpr u32 WS_OMG = 32768;
constexpr u32 WS_K = 65536;
constexpr u32 KV_T = 28672;
constexpr u32 WS_V = WS_K + 64 * KV_T;
constexpr u32 WS_A2 = WS_V + 64 * KV_T;
constexpr u32 A2_T = 143360;
constexpr u32 WS_END = WS_A2 + 64 * A2_T;
constexpr u32 OOB = 0xFFFFFF00u;
constexpr u32 CODE_BYTES = 32768;
constexpr int SC1 = 16;
constexpr int SLOT = 8192;
constexpr int SPIN_CAP = 1 << 15;

DI rsrc_t mk(const void* p, u32 bytes) { return __builtin_amdgcn_make_buffer_rsrc((void*)p, (short)0, (int)bytes, 0x00020000); }
template <u32 DST> DI void dma(u32 m0w, u32 voff, rsrc_t rs, u32 soff) {
  asm volatile("s_add_u32 m0, %0, %4\n\ts_nop 0\n\tbuffer_load_dwordx4 %1, %2, %3 offen lds" ::"s"(m0w), "v"(voff), "s"(rs), "s"(soff), "n"(DST) : "memory", "scc");
}
template <u32 DST, u32 MUL> DI void dmam(u32 m0w, u32 voff, rsrc_t rs, u32 k) {
  u32 t;
  asm volatile("s_add_u32 m0, %1, %5\n\ts_mul_i32 %0, %4, %6\n\tbuffer_load_dwordx4 %2, %3, %0 offen lds" : "=&s"(t) : "s"(m0w), "v"(voff), "s"(rs), "s"(k), "n"(DST), "n"(MUL) : "memory", "scc");
}
DI u32x4 ld128_sc1(u32 voff, rsrc_t rs, u32 soff) {
  u32x4 v;
  asm volatile("buffer_load_dwordx4 %0, %1, %2, %3 offen sc1" : "=v"(v) : "v"(voff), "s"(rs), "s"(soff) : "memory");
  return v;
}
DI u32x2 ld64_sc1(u32 voff, rsrc_t rs, u32 soff) {
  u32x2 v;
  asm volatile("buffer_load_dwordx2 %0, %1, %2, %3 offen sc1" : "=v"(v) : "v"(voff), "s"(rs), "s"(soff) : "memory");
  return v;
}
DI u32 ld32_sc1(u32 voff, rsrc_t rs, u32 soff) {
  u32 v;
  asm volatile("buffer_load_dword %0, %1, %2, %3 offen sc1" : "=v"(v) : "v"(voff), "s"(rs), "s"(soff) : "memory");
  return v;
}
DI u32 touch(u32 voff, rsrc_t rs) {
  u32 v;
  asm volatile("buffer_load_dword %0, %1, %2, 0 offen" : "=v"(v) : "v"(voff), "s"(rs) : "memory");
  return v;
}
DI void st128(u32x4 v, rsrc_t rs, u32 voff, u32 soff) {
  asm volatile("buffer_store_dwordx4 %0, %1, %2, %3 offen sc1\n\ts_nop 1" ::"v"(v), "v"(voff), "s"(rs), "s"(soff) : "memory");
}
constexpr int clampvm(int n) { return n > 63 ? 63 : n; }
template <int N> DI void wait_vm() { static_assert(N >= 0, ""); asm volatile("s_waitcnt vmcnt(%0)" ::"n"(clampvm(N)) : "memory"); }
template <int N> DI void sync_vm() { static_assert(N >= 0, ""); asm volatile("s_waitcnt vmcnt(%0)\n\ts_barrier" ::"n"(clampvm(N)) : "memory"); }
template <class T> DI void pin(T& v) { asm volatile("" : "+v"(v)); }
template <int I, int N, class Fn> DI void static_for(Fn&& f) {
  if constexpr (I < N) { f(IC<I>{}); static_for<I + 1, N>(f); }
}

DI u32x4 trfrag(LAS char* p) {
  s16x4 lo = __builtin_amdgcn_ds_read_tr16_b64_v4i16((LAS s16x4*)p);
  s16x4 hi = __builtin_amdgcn_ds_read_tr16_b64_v4i16((LAS s16x4*)(p + 512));
  u32x2 a = __builtin_bit_cast(u32x2, lo), b = __builtin_bit_cast(u32x2, hi);
  return u32x4{a.x, a.y, b.x, b.y};
}
DI f32x16 mfma(u32x4 a, u32x4 b, f32x16 c) {
  return __builtin_amdgcn_mfma_f32_32x32x16_bf16(__builtin_bit_cast(bf16x8, a), __builtin_bit_cast(bf16x8, b), c, 0, 0, 0);
}
DI void mfma_at(f32x16& c, u32x4 a, u32x4 b) { c = mfma(a, b, c); asm volatile("" : "+v"(c)); }
DI u32 pk(float a, float b) { bf16x2 c; c[0] = (__bf16)a; c[1] = (__bf16)b; return __builtin_bit_cast(u32, c); }
DI u32x4 pk8(const f32x16& v, int s) { return u32x4{pk(v[8 * s], v[8 * s + 1]), pk(v[8 * s + 2], v[8 * s + 3]), pk(v[8 * s + 4], v[8 * s + 5]), pk(v[8 * s + 6], v[8 * s + 7])}; }
DI float sigmoidf(float z) { return __builtin_amdgcn_rcpf(1.f + __builtin_amdgcn_exp2f(z * -1.4426950408889634f)); }
DI float glu(float g, float u) { return u * sigmoidf(g); }
DI float xhalf_max(float v) { auto r = __builtin_amdgcn_permlane32_swap(__float_as_uint(v), __float_as_uint(v), false, false); return fmaxf(__uint_as_float(r[0]), __uint_as_float(r[1])); }
DI float xhalf_sum(float v) { auto r = __builtin_amdgcn_permlane32_swap(__float_as_uint(v), __float_as_uint(v), false, false); return __uint_as_float(r[0]) + __uint_as_float(r[1]); }

DI void spin(const char* ws, u32 off, bool need, u64 tag) {
  const GAS u64* p = (const GAS u64*)(ws + off);
#pragma unroll 1
  for (int i = 0; i < SPIN_CAP; ++i) {
    u64 v = __hip_atomic_load(p, __ATOMIC_RELAXED, __HIP_MEMORY_SCOPE_AGENT);
    if (__all(!need || v == tag)) break;
    __builtin_amdgcn_s_sleep(2);
  }
  asm volatile("" ::: "memory");
}
DI bool match(u32x2 v, bool need, u64 tag) { return __all(!need || (v.x == (u32)tag && v.y == (u32)(tag >> 32))); }
DI u64 tag_of(u32x2 ev) {
  u64 e = ((u64)__builtin_amdgcn_readfirstlane(ev.y) << 32) | (u32)__builtin_amdgcn_readfirstlane(ev.x);
  return e * 6364136223846793005ull + 1442695040888963407ull;
}
DI void put_tag(u64 tag, rsrc_t rs, u32 voff, u32 soff) { __builtin_amdgcn_raw_buffer_store_b64(u32x2{(u32)tag, (u32)(tag >> 32)}, rs, voff, soff, SC1); }

constexpr char HEAD[] = "KKGKKGKKGK";
constexpr int BARS[] = {0, 2, 6, 8, 10, 14, 16, 18, 22, 24, 28, 46, 62, 68, 75, 79, 86, 96};
constexpr int PRO_DMA = 9;
constexpr int HEAD_N = 26, HEAD_RATE = 20;
constexpr int EP1 = 2;
constexpr int U_KVPUB = 25, U_H1PUB = 41, U_H2PUB = 57, U_POLL1 = 52, U_CHK1 = 56, U_POLL2 = 64, U_CHK2 = 68, U_POLL3 = 70, U_CHK3 = 76;
constexpr int NV = 105, U0 = 14, IT_OMG = 28;
constexpr int imin(int a, int b) { return a < b ? a : b; }
constexpr int item_of(int i) { return i < IT_OMG ? i : i + 1; }
constexpr int ld_rel(int k) { return k < 6 ? 0 : 1 + (k - 6) / 2; }
constexpr int ld_iter(int it) { return U0 + (it < 14 ? U_CHK1 + ld_rel(it) : (it < 28 ? U_CHK2 + ld_rel(it - 14) : U_CHK3 + (it - 28) / 2)); }
struct Sched {
  int nhead = 0, vkvst = 0, vg1 = 0;
  int xl[NV + 2] = {}, wl[NV + 2] = {};
  int kind[NV + 2] = {}, idx[NV + 2] = {}, lo[NV + 2] = {}, hi[NV + 2] = {};
  int pk[84] = {}, pi[84] = {}, pj[84] = {};
  bool bar[NV] = {};
  int cum[NV + 1] = {}, base[NV + 1] = {}, nst[NV] = {}, ldlo[NV + 1] = {}, cnt[NV] = {}, pdma[84] = {}, pld[36] = {}, freed[NV] = {};
  int cnt0 = 0; bool ok = true;
  constexpr Sched() {
    int v = 0, n = 0, k = 0, g = 0;
    int nx = 0;
    auto add = [&](int kd, int st, int nit, int nw) {
      const int first = n; int x = -1;
      if (st < 7 && st == nx) { x = n; for (int e = 0; e < 2; ++e) { pk[n] = 0; pi[n] = st; pj[n] = e; ++n; } ++nx; }
      for (int e = 0; e < nit; ++e) { kind[v] = kd; idx[v] = nit * st + e; lo[v] = first; hi[v] = n + nw - 1; xl[v] = x; wl[v] = n; ++v; }
      for (int e = 0; e < nw; ++e) { pk[n] = kd; pi[n] = st; pj[n] = kd ? e : 2; ++n; }
    };
    for (int i = 0; HEAD[i]; ++i) { if (HEAD[i] == 'K') { add(0, k++, 2, 1); nhead = v; } else { add(1, g++, 4, 2); if (g == 7) vg1 = v; } }
    if (k != 7 || g > 7) ok = false;
    vkvst = nhead + 1;
    while (g < 14) { add(1, g++, 4, 2); if (g == 7) vg1 = v; }
    for (int i = 0; i < 35; ++i) { kind[v] = 2; idx[v] = 56 + i; lo[v] = hi[v] = wl[v] = n; pk[n] = 2; pi[n] = i; ++n; ++v; }
    kind[NV] = kind[NV + 1] = 3; hi[NV] = hi[NV + 1] = 83;
    for (int e : BARS) bar[e] = true;
    bar[U0 + U_KVPUB] = bar[U0 + U_H1PUB] = bar[U0 + U_H2PUB] = bar[U0 + U_CHK2] = bar[U0 + U_CHK3] = true;
    if (U0 + U_KVPUB <= vkvst || U0 + U_H1PUB <= vg1 + 2 * EP1 || bar[U0 + U_CHK1]) ok = false;
    int fr = 0, cc = PRO_DMA, cr = 0, it = 0;
    cum[0] = cc; base[0] = PRO_DMA;
    for (n = 0; n < PRO_DMA; ++n) pdma[n] = n;
    for (v = 0; v < NV; ++v) {
      const int u = v - U0;
      if (bar[v] || u == U_CHK1) fr = lo[v];
      freed[v] = fr;
      nst[v] = 3 * (v == vkvst) + (v == vg1 + EP1) + (v == vg1 + 2 * EP1) + 2 * (u == 56) + (u == U_POLL1) + (u == U_POLL2) + (u == U_POLL3);
      ldlo[v] = it;
      while (it < 36 && ld_iter(it) == v) { pld[it] = base[v] + nst[v] + (it - ldlo[v]); ++it; }
      cr += v < HEAD_N ? HEAD_RATE : 16;
      const int t = imin(imin(cc + imin(cr / 16, 2), fr + 20), 84);
      cr -= 16 * (t - cc); if (cr > 16) cr = 16;
      for (n = cc; n < t; ++n) pdma[n] = base[v] + nst[v] + (it - ldlo[v]) + (n - cc);
      base[v + 1] = base[v] + nst[v] + (it - ldlo[v]) + (t - cc);
      cc = t; cum[v + 1] = cc;
    }
    ldlo[NV] = it;
    if (it != 36 || cc != 84) ok = false;
    for (v = -1; v < NV; ++v) {
      if (v >= 0 && !bar[v]) continue;
      int nb = v + 1; while (nb < NV && !bar[nb]) ++nb;
      const int need = hi[nb + 1], at = v < 0 ? 0 : v;
      if (need >= cum[at]) ok = false;
      int c = base[at] - pdma[need] - 1;
      const int u = v - U0;
      if (u == U_KVPUB) c = imin(c, base[v] - (base[vkvst] + nst[vkvst]));
      if (u == U_H1PUB) c = imin(c, base[v] - (base[vg1 + 2 * EP1] + nst[vg1 + 2 * EP1]));
      if (u == U_H2PUB) c = imin(c, base[v] - (base[U0 + 56] + nst[U0 + 56]));
      if (v < 0) cnt0 = c; else cnt[v] = c;
    }
    for (int i = 0; i < 35; ++i) if (ld_iter(item_of(i)) > U0 + 56 + i) ok = false;
  }
};
constexpr Sched SA{};
static_assert(SA.ok, "schedule: a piece or operand is used before it is requested, or HEAD is malformed");
static_assert(U_CHK1 == 56 && U_POLL1 < U_CHK1 && U_CHK1 < U_POLL2 && U_POLL2 < U_CHK2 && U_CHK2 < U_POLL3 && U_POLL3 < U_CHK3, "");
static_assert(U_KVPUB < U_H1PUB && U_H1PUB < U_CHK1 && U_H2PUB > 56 && U_H2PUB < U_CHK2, "a workgroup publishes a class before it waits for that class");
static_assert(SA.freed[NV - 1] >= 72, "the reduction scratch (slots 4..11 = pieces 64..71) must be free at the end");

struct Ctx {
  LAS char* sm; u32 m0w;
  rsrc_t rx, rqkv, rwg, rwu, rwd, rwo, rws;
  const char* ws;
  u32 pf;
  u32 kc[7], seq;
  u32 vx, vkv, vg, vu, vw2;
};

template <int N> DI void issueA(const Ctx& c) {
  constexpr u32 dst = (N % 20) * SLOT;
  constexpr int k = SA.pk[N], st = SA.pi[N], j = SA.pj[N];
  if constexpr (k == 0) {
    if constexpr (j < 2) dmam<dst, 128>(c.m0w, c.vx + j * 896, c.rx, c.kc[st]);
    else dmam<dst, 64 * 2688>(c.m0w, c.vkv, c.rqkv, c.kc[st]);
  } else if constexpr (k == 1) {
    if constexpr (j == 0) dmam<dst, 64 * 3584>(c.m0w, c.vg + (st / 7) * 1792, c.rwg, c.kc[st % 7]);
    else dmam<dst, 64 * 3584>(c.m0w, c.vu + (st / 7) * 1792, c.rwu, c.kc[st % 7]);
  } else if constexpr (st < 28) dma<dst>(c.m0w, c.vw2, c.rwd, st * 64 * 896);
  else dma<dst>(c.m0w, c.vw2, c.rwo, (st - 28) * 64 * 896);
}

DI void type_a(Ctx& c, int w, int lane, int mt, int role, int R2, int n2, void* out) {
  const int r = lane & 31, hi = lane >> 5, blk = (lane >> 4) & 1, q = (lane & 15) >> 2, p = lane & 3;
  const int sp = ((p & 1) << 1) | (p >> 1);
  const int rg = w & 3, cg = w >> 2, T = mt * 4 + rg;
  const bool isv = role >= 7;
  LAS char* sm = c.sm;
  static_for<2, PRO_DMA>([&](auto N) { issueA<N>(c); });
  const int R = 32 * (rg & 1) + r, xm = (R >> 1) & 7;
  int xa[4];
#pragma unroll
  for (int s = 0; s < 4; ++s) xa[s] = (rg >> 1) * SLOT + R * 128 + (((2 * s + hi) ^ xm) << 4);
  const int rowb = (8 * hi + q) * 128, q2 = q & 2;
  const int akv = rowb + (((2 * cg + blk) ^ q2) << 5) + (isv ? p : sp) * 8;
  int agu[2];
#pragma unroll
  for (int i = 0; i < 2; ++i) agu[i] = blk * SLOT + rowb + (((2 * cg + i) ^ q2 ^ blk) << 5) + sp * 8;
  int aguw[2][3];
#pragma unroll
  for (int i = 0; i < 2; ++i)
#pragma unroll
    for (int k = 0; k < 3; ++k) { aguw[i][k] = agu[i] + k * 65536; pin(aguw[i][k]); }
  const int rt = w & 1, kq = w >> 1;
  int aw2[2], aw2w[2][3];
#pragma unroll
  for (int i = 0; i < 2; ++i) aw2[i] = kq * 2048 + rowb + (((2 * i + blk) ^ q2) << 5) + p * 8;
  const int T2 = (R2 >> 1) * 4 + 2 * rt + (R2 & 1);

  u32x4 xf[28], wf[2][2], af[35], hp[2];
  f32x16 akvacc = {}, g1[2] = {}, g2[2] = {}, ah[2] = {}, ao[2] = {};
  u32x2 ev, pollv; u32 omg; u64 tag;
  const u32 lane16 = lane * 16;
  const u32 poll_off = WS_FLAG + (R2 >> 1) * 256 + (lane & 15) * 8;
  const bool need_h = (lane & 15) < 14;
  const u32 a2_off = WS_A2 + T2 * A2_T + kq * 1024 + lane16;
  const u32 h_off = WS_A2 + T * A2_T + (role * 4 + 2 * cg) * 1024 + lane16;

  auto read_w = [&](auto V, auto I) {
    constexpr int v = V, i = I, k = SA.kind[v], u = SA.idx[v], sl = SA.wl[v] % 20;
    if constexpr (k < 2 && SA.xl[v] >= 0 && (k == 0 || i == 0)) {
      constexpr int ks = k == 0 ? 2 * u + i : u, xs = SA.xl[v] % 20;
      const int wrap = xs == 19 ? (rg >> 1) * (-20 * SLOT) : 0;
      xf[ks] = *(LAS u32x4*)(sm + xa[ks % 4] + wrap + xs * SLOT);
    }
    if constexpr (k == 0) {
      wf[v & 1][i] = trfrag(sm + akv + sl * SLOT + ((2 * u + i) % 4) * 2048);
    } else if constexpr (k == 1) {
      const int wrap = sl == 19 ? blk * (-20 * SLOT) : 0;
      wf[v & 1][i] = trfrag(sm + aguw[i][sl / 8] + wrap + (sl % 8) * SLOT + (u % 4) * 2048);
    } else if constexpr (k == 2) {
      if constexpr (u == 56) {
#pragma unroll
        for (int e = 0; e < 3; ++e) { aw2w[i][e] = aw2[i] + e * 65536; pin(aw2w[i][e]); }
      }
      wf[v & 1][i] = trfrag(sm + aw2w[i][sl / 8] + (sl % 8) * SLOT);
    }
  };
  auto publish = [&](int cls) {
    if (w == 0) put_tag(tag, c.rws, lane == 0 ? role * 8 : OOB, WS_FLAG + cls * 4096 + mt * 256);
  };
  auto load_item = [&](auto IT) {
    constexpr int it = IT;
    if constexpr (it == IT_OMG) omg = ld32_sc1(r * 4, c.rws, WS_OMG + T2 * 128);
    else { constexpr int i = it < IT_OMG ? it : it - 1; af[i] = ld128_sc1(a2_off, c.rws, i * 4096); }
  };
  auto check = [&](auto CNT, int cls, bool need) {
    wait_vm<CNT>();
    pin(pollv);
    if (w == 0 && !match(pollv, need, tag)) spin(c.ws, poll_off + cls * 4096, need, tag);
  };

  sync_vm<SA.cnt0>();
  pin(c.pf);
  static_for<0, 4>([&](auto S) { read_w(IC<S / 2>{}, IC<S % 2>{}); });
  SCHED();
  auto iter = [&](auto V, auto ISV) {
    constexpr int v = V, b = v & 1, kd = SA.kind[v], ui = SA.idx[v], u = v - U0;
    if constexpr (u == U_CHK2) check(IC<SA.base[v] - SA.base[U0 + U_POLL2] - 1>{}, 2, need_h);
    if constexpr (u == U_CHK3) check(IC<SA.base[v] - SA.base[U0 + U_POLL3] - 1>{}, 2, !need_h);
    if constexpr (SA.bar[v]) sync_vm<SA.cnt[v]>();
    if constexpr (u == U_KVPUB) {
      pin(ev);
      tag = tag_of(ev);
      publish(0);
    }
    if constexpr (u == U_H1PUB) publish(1);
    if constexpr (u == U_H2PUB) publish(2);
    if constexpr (u == U_POLL1) pollv = ld64_sc1(w == 0 ? poll_off : OOB, c.rws, 4096);
    if constexpr (u == U_POLL2 || u == U_POLL3) pollv = ld64_sc1(w == 0 ? poll_off : OOB, c.rws, 8192);
    if constexpr (v == SA.vkvst) {
      ev = ld64_sc1(w == 0 ? WS_E : OOB, c.rws, 0);
#pragma unroll
      for (int s = 0; s < 2; ++s) {
        const u32 off = isv ? WS_V + T * KV_T + (s * 2 + hi) * 7168 + ((role - 7) * 64 + cg * 32 + r) * 16
                            : WS_K + T * KV_T + (role * 8 + cg * 4 + 2 * s) * 512 + lane16;
        st128(pk8(akvacc, s), c.rws, off, 0);
      }
    }
    if constexpr (v == SA.vg1 + EP1) st128(hp[0], c.rws, h_off, 0);
    if constexpr (v == SA.vg1 + 2 * EP1) st128(hp[1], c.rws, h_off, 1024);
    if constexpr (u == 56) {
#pragma unroll
      for (int t = 0; t < 2; ++t) {
#pragma unroll
        for (int j = 0; j < 8; j += 2) hp[t][j / 2] = pk(glu(g2[t][j], g2[t][8 + j]), glu(g2[t][j + 1], g2[t][9 + j]));
        st128(hp[t], c.rws, h_off, (56 + t) * 1024);
        SCHED();
      }
    }
    asm volatile("" ::: "memory");
    if constexpr (u == U_CHK1) {
      check(IC<SA.base[v] + SA.nst[v] - SA.base[U0 + U_POLL1] - 1>{}, 1, need_h);
      asm volatile("s_barrier" ::: "memory");
    }
    constexpr bool ld_first = u >= 56 && ld_iter(item_of(u < 56 ? 0 : u - 56)) == v;
    constexpr int nld = SA.ldlo[v + 1] - SA.ldlo[v], nd = SA.cum[v + 1] - SA.cum[v];
    if constexpr (ld_first) static_for<SA.ldlo[v], SA.ldlo[v + 1]>(load_item);
    if constexpr (u >= 56) {
      wait_vm<SA.base[v] + SA.nst[v] + (ld_first ? nld : 0) - SA.pld[item_of(u - 56)] - 1>();
      pin(af[u - 56]);
    }
    SCHED();
    static_for<0, 2>([&](auto I) {
      constexpr int i = I;
      if constexpr (kd == 0) { if constexpr (ISV) mfma_at(akvacc, xf[2 * ui + i], wf[b][i]); else mfma_at(akvacc, wf[b][i], xf[2 * ui + i]); }
      else if constexpr (ui < 28) mfma_at(g1[i], wf[b][i], xf[ui]);
      else if constexpr (ui < 56) mfma_at(g2[i], wf[b][i], xf[ui - 28]);
      else if constexpr (ui < 84) mfma_at(ah[i], wf[b][i], af[ui - 56]);
      else mfma_at(ao[i], wf[b][i], af[ui - 56]);
      SCHED();
      read_w(IC<v + 2>{}, I);
      if constexpr (v >= SA.vg1 && v < SA.vg1 + 2 * EP1) {
        constexpr int t = (v - SA.vg1) / EP1, per = 4 / EP1, k0 = ((v - SA.vg1) % EP1) * per;
#pragma unroll
        for (int k = k0 + (i * per + 1) / 2; k < k0 + ((i + 1) * per + 1) / 2; ++k) {
          u32 hv = pk(glu(g1[t][2 * k], g1[t][8 + 2 * k]), glu(g1[t][2 * k + 1], g1[t][9 + 2 * k]));
          pin(hv);
          hp[t][k] = hv;
        }
      }
      if constexpr (i == 0 && !ld_first) static_for<SA.ldlo[v], SA.ldlo[v + 1]>(load_item);
      if constexpr (i == 0 && nd == 2) issueA<SA.cum[v]>(c);
      if constexpr (i == 1 && nd >= 1) issueA<SA.cum[v + 1] - 1>(c);
      SCHED();
    });
  };
  if (isv) static_for<0, SA.nhead>([&](auto V) { iter(V, IC<1>{}); });
  else static_for<0, SA.nhead>([&](auto V) { iter(V, IC<0>{}); });
  static_for<SA.nhead, NV>([&](auto V) { iter(V, IC<0>{}); });
  pin(omg);
  const float om = __uint_as_float(omg);
  LAS char* red = sm + 4 * SLOT;
#pragma unroll
  for (int t = 0; t < 2; ++t)
#pragma unroll
    for (int a = 0; a < 4; ++a) {
      f32x4 v;
#pragma unroll
      for (int e = 0; e < 4; ++e) v[e] = ah[t][4 * a + e] * om + ao[t][4 * a + e];
      *(LAS f32x4*)(red + kq * 16384 + (2 * r + rt) * 256 + (((8 * t + 2 * a + hi) ^ (r & 15)) << 4)) = v;
    }
  asm volatile("s_waitcnt lgkmcnt(0)\n\ts_barrier" ::: "memory");
  {
    const int row = w * 8 + (lane >> 3), c8 = lane & 7, m = (row >> 1) & 15;
    f32x4 s0 = {}, s1 = {};
#pragma unroll
    for (int k = 0; k < 4; ++k) {
      s0 += *(LAS f32x4*)(red + k * 16384 + row * 256 + (((2 * c8) ^ m) << 4));
      s1 += *(LAS f32x4*)(red + k * 16384 + row * 256 + (((2 * c8 + 1) ^ m) << 4));
    }
    const u32x4 o = {pk(s0[0], s0[1]), pk(s0[2], s0[3]), pk(s1[0], s1[1]), pk(s1[2], s1[3])};
    __builtin_amdgcn_raw_buffer_store_b128(o, mk(out, 2048 * 896), (R2 * 64 + row) * 896 + n2 * 128 + c8 * 16, 0, SC1);
  }
}

constexpr int PRO_B = 12, GB_TOUCH = 18, GB_POLL = 25;
constexpr int cumB(int g) {
  int cc = PRO_B;
  for (int j = 0; j <= g; ++j) cc = imin(imin(cc + 2, 6 * ((j + 1) / 4) + 18), 42);
  return cc;
}
static_assert(cumB(GB_TOUCH - 1) == 42 && GB_POLL > 23, "both sit behind the last ring piece; the poll behind the last counted barrier");
template <int N> DI void issueB(const Ctx& c) {
  constexpr int kc = N / 6, j = N % 6;
  constexpr u32 dst = (N % 18) * SLOT;
  if constexpr (j < 2) dmam<dst, 128>(c.m0w, c.vx + j * 896, c.rx, c.kc[kc]);
  else dmam<dst, 64 * 2688>(c.m0w, c.vkv + (j - 2) * 128, c.rqkv, c.kc[kc]);
}

DI void type_b(Ctx& c, int w, int lane, int mt, int hf, const void* gw) {
  const int r = lane & 31, hi = lane >> 5, blk = (lane >> 4) & 1, q = (lane & 15) >> 2, p = lane & 3;
  const int sp = ((p & 1) << 1) | (p >> 1);
  const int rg = w & 3, cg = w >> 2, T = mt * 4 + rg;
  LAS char* sm = c.sm;
  if (w == 0) dma<18 * SLOT>(c.m0w, ((c.seq >> (4 * (lane >> 3))) & 7) * 128 + (lane & 7) * 16, mk(gw, 896), 0);
  static_for<2, PRO_B>([&](auto N) { issueB<N>(c); });
  const int R = 32 * (rg & 1) + r, xm = (R >> 1) & 7;
  int xa[4], aq[4];
#pragma unroll
  for (int s = 0; s < 4; ++s) xa[s] = (rg >> 1) * SLOT + R * 128 + (((2 * s + hi) ^ xm) << 4);
  const int h0 = 8 * hf + 4 * cg;
#pragma unroll
  for (int i = 0; i < 4; ++i) {
    const int col = 28 * (h0 + i) - 4 * (i & 1) - 192 * hf + 16 * blk + 4 * sp;
    aq[i] = (2 + (col >> 6)) * SLOT + (8 * hi + q) * 128 + (((col & 63) * 2) ^ ((q & 2) << 5));
  }
  const bool first = (mt == 0) && !(rg & 1);
  const int Tp = first ? T : ((rg & 1) ? T - 1 : T - 3);
  const u32 lane16 = lane * 16, lo16 = hi ? OOB : lane16;
  const u32 ob = WS_A2 + T * A2_T + (1792 + 28 * h0) * 64;
  const u32 omg_off = (hf == 0 && cg == 0 && hi == 0) ? r * 4 : OOB;
  const u32 poll_off = WS_FLAG + ((lane & 16) && mt > 0 ? mt - 1 : mt) * 256 + (lane & 15) * 8;
  const bool poll_need = (lane & 15) < 14;
  u32x4 xf[2], gf[2], wf[2][4];
  u32x4 kf[4][2], kp[2], vf[4][2], vp[4];
  f32x16 qa[4] = {};
  u32x2 ev, pollv;
  u32 tw;
  const int sr = (r & 19) | ((r & 4) << 1) | ((r & 8) >> 1);
  const u32 kp_off = (28 * (r >> 3) - 4 * ((r >> 3) & 1)) * 64 + (hi * 32 + 24 + (r & 7)) * 16;
  const u32 v_off = hi * 7168 + sr * 16;
  auto load_kv = [&](auto IT) {
    constexpr int it = IT;
    if constexpr (it < 2) kp[it] = ld128_sc1(kp_off, c.rws, WS_K + Tp * KV_T + 28 * h0 * 64 + it * 1024);
    else if constexpr (it < 10) {
      constexpr int i = (it - 2) / 2, k = it & 1;
      kf[i][k] = ld128_sc1(lane16, c.rws, WS_K + T * KV_T + (28 * (h0 + i) - 4 * (i & 1)) * 64 + k * 1024);
    } else {
      constexpr int i = (it - 10) / 3, k = (it - 10) % 3;
      const int wh = 28 * (h0 + i) - 4 * (i & 1);
      if constexpr (k == 0) vp[i] = ld128_sc1(v_off, c.rws, WS_V + Tp * KV_T + 14336 + wh * 16);
      else vf[i][k - 1] = ld128_sc1(v_off, c.rws, WS_V + T * KV_T + (k - 1) * 14336 + wh * 16);
    }
  };
  auto read_x = [&](auto G) {
    constexpr int g = G;
    if constexpr (g < 28) {
      xf[g & 1] = *(LAS u32x4*)(sm + xa[g % 4] + ((6 * (g / 4)) % 18) * SLOT);
      gf[g & 1] = *(LAS u32x4*)(sm + 18 * SLOT + g * 32 + hi * 16);
    }
  };
  auto read_w = [&](auto G, auto I) {
    constexpr int g = G;
    if constexpr (g < 28) wf[g & 1][I] = trfrag(sm + aq[I] + ((6 * (g / 4)) % 18) * SLOT + (g % 4) * 2048);
  };
  sync_vm<PRO_B - 6>();
  pin(c.pf);
  read_x(IC<0>{});
  static_for<0, 4>([&](auto I) { read_w(IC<0>{}, I); });
  SCHED();
  static_for<0, 28>([&](auto G) {
    constexpr int g = G, b = g & 1;
    if constexpr (g % 4 == 3 && g < 27) { asm volatile("s_waitcnt lgkmcnt(0)" ::: "memory"); sync_vm<cumB(g - 1) - 6 * (g / 4 + 2) + (g > GB_TOUCH)>(); }
    if constexpr (g == GB_POLL) { ev = ld64_sc1(w == 0 ? WS_E : OOB, c.rws, 0); pollv = ld64_sc1(w == 0 ? poll_off : OOB, c.rws, 0); }
    if constexpr (g == GB_TOUCH) {
      const int gi = (mt * 2 + hf) * 8 + w;
      tw = touch(((gi < 196 ? gi : gi - 196) * 64 + lane) * 128, gi < 196 ? c.rwd : c.rwo);
    }
    SCHED();
    static_for<0, 4>([&](auto I) {
      constexpr int i = I;
      mfma_at(qa[i], wf[b][i], xf[b]);
      SCHED();
      if constexpr (i == 0) read_x(IC<g + 1>{});
      read_w(IC<g + 1>{}, I);
      if constexpr (i < 2) {
        wf[b][2][2 * i] = r == 28 ? gf[b][2 * i] : wf[b][2][2 * i];
        wf[b][2][2 * i + 1] = r == 28 ? gf[b][2 * i + 1] : wf[b][2][2 * i + 1];
        pin(wf[b][2]);
      }
      if constexpr (i >= 2 && cumB(g - 1) + i - 2 < cumB(g)) issueB<cumB(g - 1) + i - 2>(c);
      SCHED();
    });
  });
  wait_vm<0>();
  pin(pollv); pin(ev); pin(tw);
  const u64 tag = tag_of(ev);
  if (w == 0 && !match(pollv, poll_need, tag)) spin(c.ws, poll_off, poll_need, tag);
  asm volatile("s_barrier" ::: "memory");
  static_for<0, 6>(load_kv);
  SCHED();
  const float z = __uint_as_float(__builtin_amdgcn_permlane32_swap(__float_as_uint(qa[2][12]), __float_as_uint(qa[2][12]), false, false)[1]);
  const float gate = sigmoidf(z);
  const u32 omg = __float_as_uint(sigmoidf(-z));
  int ab[4];
  u32 msk[5][2];
#pragma unroll
  for (int b = 0; b < 4; ++b) ab[b] = (r >> 3) - ((r & 7) < 4 * hi + b ? 1 : 0);
#pragma unroll
  for (int a = 0; a < 5; ++a)
#pragma unroll
    for (int e = 0; e < 2; ++e) msk[a][e] = (ab[2 * e] == a - 1 ? 0xFFFFu : 0u) | (ab[2 * e + 1] == a - 1 ? 0xFFFF0000u : 0u);
  const float csc = 0.18898223650461362f * 1.4426950408889634f;
  u32 p01[2], p23[2];
  u32x4 qf[4][2];
  static_for<0, 4>([&](auto I) {
    constexpr int i = I;
    qf[i][0] = pk8(qa[i], 0); qf[i][1] = pk8(qa[i], 1);
    if (i & 1) { qf[i][0][0] = hi ? qf[i][0][0] : 0u; qf[i][0][1] = hi ? qf[i][0][1] : 0u; }
    else       { qf[i][1][2] = hi ? 0u : qf[i][1][2]; qf[i][1][3] = hi ? 0u : qf[i][1][3]; }
    pin(qf[i][0]); pin(qf[i][1]);
  });
#pragma unroll
  for (int a = 0; a < 5; ++a) { pin(msk[a][0]); pin(msk[a][1]); }
  __builtin_amdgcn_raw_buffer_store_b32(omg, c.rws, omg_off, WS_OMG + T * 128, SC1);
  asm volatile("" ::: "memory");
  f32x16 so[2], sv[2], oa[2];
  auto scores = [&](auto J) {
    static_for<0, 2>([&](auto E) {
      constexpr int i = 2 * J + E;
      pin(kf[i][0]); pin(kf[i][1]);
      so[E] = mfma(kf[i][0], qf[i][0], f32x16{}); sv[E] = mfma(kp[0], qf[i][0], f32x16{});
      mfma_at(so[E], kf[i][1], qf[i][1]); mfma_at(sv[E], kp[1], qf[i][1]);
    });
  };
  auto softmax = [&](auto J) {
    static_for<0, 2>([&](auto E) {
      constexpr int i = 2 * J + E;
      float v[4];
#pragma unroll
      for (int b = 0; b < 4; ++b) {
        float t = first ? -1e30f : sv[E][4 * i + b];
#pragma unroll
        for (int a = 0; a < 4; ++a) t = ab[b] == a ? so[E][4 * a + b] : t;
        v[b] = t;
      }
      const float mx = xhalf_max(fmaxf(fmaxf(v[0], v[1]), fmaxf(v[2], v[3])));
#pragma unroll
      for (int b = 0; b < 4; ++b) v[b] = __builtin_amdgcn_exp2f((v[b] - mx) * csc);
      const float sc = gate * __builtin_amdgcn_rcpf(xhalf_sum((v[0] + v[1]) + (v[2] + v[3])));
      p01[E] = pk(v[0] * sc, v[1] * sc); p23[E] = pk(v[2] * sc, v[3] * sc);
    });
    pin(p01[0]); pin(p23[0]); pin(p01[1]); pin(p23[1]);
  };
  auto pv = [&](auto J) {
    static_for<0, 2>([&](auto E) {
      constexpr int i = 2 * J + E;
      pin(vp[i]); pin(vf[i][0]); pin(vf[i][1]);
      oa[E] = mfma(vp[i], u32x4{0u, 0u, p01[E] & msk[0][0], p23[E] & msk[0][1]}, f32x16{});
      oa[E] = mfma(vf[i][0], u32x4{p01[E] & msk[1][0], p23[E] & msk[1][1], p01[E] & msk[2][0], p23[E] & msk[2][1]}, oa[E]);
      mfma_at(oa[E], vf[i][1], u32x4{p01[E] & msk[3][0], p23[E] & msk[3][1], p01[E] & msk[4][0], p23[E] & msk[4][1]});
    });
  };
  auto store_o = [&](auto J) {
    u32x4 op[2][2] = {{pk8(oa[0], 0), pk8(oa[0], 1)}, {pk8(oa[1], 0), pk8(oa[1], 1)}};
#pragma unroll
    for (int e = 0; e < 2; ++e) { auto t = __builtin_amdgcn_permlane32_swap(op[0][1][e], op[1][0][e], false, false); op[0][1][e] = t[0]; op[1][0][e] = t[1]; }
    const u32 o2 = ob + J * 3584;
    st128(op[0][0], c.rws, lane16, o2);
    st128(op[0][1], c.rws, lo16, o2 + 1024);
    st128(op[1][0], c.rws, lane16, o2 + 1536);
    st128(op[1][1], c.rws, lane16, o2 + 2560);
  };
  static_for<10, 16>(load_kv);
  wait_vm<7>();
  pin(kp[0]); pin(kp[1]);
  scores(IC<0>{});
  static_for<6, 10>(load_kv);
  softmax(IC<0>{});
  static_for<16, 22>(load_kv);
  wait_vm<6>();
  pv(IC<0>{});
  scores(IC<1>{});
  store_o(IC<0>{});
  softmax(IC<1>{});
  wait_vm<4>();
  pv(IC<1>{});
  store_o(IC<1>{});
  sync_vm<0>();
  if (w == 0) put_tag(tag, c.rws, lane == 0 ? (14 + hf) * 8 : OOB, WS_FLAG + 8192 + mt * 256);
  if (mt == 0 && hf == 0 && w == 0) {
#pragma unroll 1
    for (int i = 0; i < 4; ++i) spin(c.ws, WS_FLAG + 8192 + (i * 4 + (lane >> 4)) * 256 + (lane & 15) * 8, true, tag);
    put_tag(tag, c.rws, lane == 0 ? 0 : OOB, WS_E);
  }
}

__global__ __launch_bounds__(512, 2) void fused_kernel(const void* __restrict__ x, const void* __restrict__ wg, const void* __restrict__ wu,
                                                       const void* __restrict__ wd, const void* __restrict__ wo, const void* __restrict__ wqkv,
                                                       const void* __restrict__ gw, void* __restrict__ out, void* __restrict__ ws) {
  __shared__ __attribute__((aligned(1024))) char smem[163840];
  const int lane = threadIdx.x & 63, w = __builtin_amdgcn_readfirstlane(threadIdx.x >> 6);
  Ctx c;
  {
    u64 pc;
    asm volatile("s_getpc_b64 %0" : "=s"(pc));
    c.pf = touch((w == 6 && lane < 8) ? ((blockIdx.x >> 3) * 8 + lane) * 128 : OOB, mk((const void*)(pc & ~255ull), CODE_BYTES));
  }
  asm volatile("" ::"s"(x), "s"(wg), "s"(wu), "s"(wd), "s"(wo), "s"(wqkv), "s"(gw), "s"(out), "s"(ws));
  const int b = blockIdx.x, xc = b & 7, ix = b >> 3;
  const int mq = xc >> 1, mt = mq * 4 + (ix & 3), rl = ix >> 2, rh = xc & 1;
  const bool isa = rl < 7;
  const int role = rh * 7 + rl;
  c.sm = (LAS char*)smem;
  c.m0w = (u32)(size_t)c.sm + w * 1024;
  c.rx = mk(x, 2048 * 896); c.rqkv = mk(wqkv, 448 * 2688); c.rwg = mk(wg, 448 * 3584); c.rwu = mk(wu, 448 * 3584);
  c.rwd = mk(wd, 1792 * 896); c.rwo = mk(wo, 448 * 896); c.rws = mk(ws, WS_END);
  c.ws = (const char*)ws;
  {
    constexpr u64 mod7 = 0x1065432106543210ull;
    c.seq = (u32)(mod7 >> (4 * ((mod7 >> (4 * (mq + 3 * rh + 2 * (ix & 3)))) & 7)));
#pragma unroll
    for (int j = 0; j < 7; ++j) { u32 k = (c.seq >> (4 * j)) & 7; asm volatile("" : "+s"(k)); c.kc[j] = k; }
  }
  const int r8 = lane >> 3, c8 = lane & 7, R = 8 * w + r8;
  c.vx = (mt * 128 + 2 * R) * 896 + ((c8 ^ ((R >> 1) & 7)) << 4);
  const int sw = (c8 ^ ((r8 & 2) << 1)) << 4;
  const int isai = (rl - 7) >> 31;
  c.vkv = R * 2688 + ((896 + role * 128) & isai) + ((rh * 384) & ~isai) + sw;
  static_for<0, 2>([&](auto N) { issueA<N>(c); });
  if (__builtin_expect(isa, 1)) {
    c.vg = R * 3584 + role * 128 + sw;
    c.vu = R * 3584 + role * 128 + (sw ^ 32);
    c.vw2 = R * 896 + rl * 128 + sw;
    type_a(c, w, lane, mt, role, xc * 4 + (ix & 3), rl, out);
  } else {
    type_b(c, w, lane, mt, rh, gw);
  }
}

extern "C" void kernel_launch(void* const* d_in, const int* in_sizes, int n_in, void* d_out, int out_size, void* d_ws, size_t ws_size,
                              hipStream_t stream) {
  fused_kernel<<<256, 512, 0, stream>>>(d_in[0], d_in[4], d_in[5], d_in[6], d_in[2], d_in[1], d_in[7], d_out, d_ws);
}
```

```cpp
#define PWSTEP 1048576
#include <hip/hip_runtime.h>
#include <hip/hip_bf16.h>

typedef unsigned long long u64;
typedef unsigned u32;
typedef __bf16 bf16x8 __attribute__((ext_vector_type(8)));
typedef __bf16 bf16x2 __attribute__((ext_vector_type(2)));
typedef short s16x4 __attribute__((ext_vector_type(4)));
typedef u32 u32x4 __attribute__((ext_vector_type(4)));
typedef u32 u32x2 __attribute__((ext_vector_type(2)));
typedef float f32x16 __attribute__((ext_vector_type(16)));
typedef float f32x4 __attribute__((ext_vector_type(4)));
typedef __amdgpu_buffer_rsrc_t rsrc_t;
#define LAS __attribute__((address_space(3)))
#define GAS __attribute__((address_space(1)))
#define DI __device__ __forceinline__
#define SCHED() __builtin_amdgcn_sched_barrier(0)
template <int V> using IC = std::integral_constant<int, V>;

constexpr u32 WS_E = 0;
constexpr u32 WS_FLAG = 4096;
constexpr u32 WS_OMG = 32768;
constexpr u32 WS_K = 65536;
constexpr u32 KV_T = 28672;
constexpr u32 WS_V = WS_K + 64 * KV_T;
constexpr u32 WS_A2 = WS_V + 64 * KV_T;
constexpr u32 A2_T = 143360;
constexpr u32 WS_END = WS_A2 + 64 * A2_T;
constexpr u32 OOB = 0xFFFFFF00u;
constexpr u32 CODE_BYTES = 32768;
constexpr int SC1 = 16;
constexpr int SLOT = 8192;
constexpr int SPIN_CAP = 1 << 15;

DI rsrc_t mk(const void* p, u32 bytes) { return __builtin_amdgcn_make_buffer_rsrc((void*)p, (short)0, (int)bytes, 0x00020000); }
template <u32 DST> DI void dma(u32 m0w, u32 voff, rsrc_t rs, u32 soff) {
  asm volatile("s_add_u32 m0, %0, %4\n\ts_nop 0\n\tbuffer_load_dwordx4 %1, %2, %3 offen lds" ::"s"(m0w), "v"(voff), "s"(rs), "s"(soff), "n"(DST) : "memory", "scc");
}
template <u32 DST, u32 MUL> DI void dmam(u32 m0w, u32 voff, rsrc_t rs, u32 k) {
  u32 t;
  asm volatile("s_add_u32 m0, %1, %5\n\ts_mul_i32 %0, %4, %6\n\tbuffer_load_dwordx4 %2, %3, %0 offen lds" : "=&s"(t) : "s"(m0w), "v"(voff), "s"(rs), "s"(k), "n"(DST), "n"(MUL) : "memory", "scc");
}
DI u32x4 ld128_sc1(u32 voff, rsrc_t rs, u32 soff) {
  u32x4 v;
  asm volatile("buffer_load_dwordx4 %0, %1, %2, %3 offen sc1" : "=v"(v) : "v"(voff), "s"(rs), "s"(soff) : "memory");
  return v;
}
DI u32x2 ld64_sc1(u32 voff, rsrc_t rs, u32 soff) {
  u32x2 v;
  asm volatile("buffer_load_dwordx2 %0, %1, %2, %3 offen sc1" : "=v"(v) : "v"(voff), "s"(rs), "s"(soff) : "memory");
  return v;
}
DI u32 ld32_sc1(u32 voff, rsrc_t rs, u32 soff) {
  u32 v;
  asm volatile("buffer_load_dword %0, %1, %2, %3 offen sc1" : "=v"(v) : "v"(voff), "s"(rs), "s"(soff) : "memory");
  return v;
}
DI u32 touch(u32 voff, rsrc_t rs) {
  u32 v;
  asm volatile("buffer_load_dword %0, %1, %2, 0 offen" : "=v"(v) : "v"(voff), "s"(rs) : "memory");
  return v;
}
DI void st128(u32x4 v, rsrc_t rs, u32 voff, u32 soff) {
  asm volatile("buffer_store_dwordx4 %0, %1, %2, %3 offen sc1\n\ts_nop 1" ::"v"(v), "v"(voff), "s"(rs), "s"(soff) : "memory");
}
constexpr int clampvm(int n) { return n > 63 ? 63 : n; }
template <int N> DI void wait_vm() { static_assert(N >= 0, ""); asm volatile("s_waitcnt vmcnt(%0)" ::"n"(clampvm(N)) : "memory"); }
template <int N> DI void sync_vm() { static_assert(N >= 0, ""); asm volatile("s_waitcnt vmcnt(%0)\n\ts_barrier" ::"n"(clampvm(N)) : "memory"); }
template <class T> DI void pin(T& v) { asm volatile("" : "+v"(v)); }
template <int I, int N, class Fn> DI void static_for(Fn&& f) {
  if constexpr (I < N) { f(IC<I>{}); static_for<I + 1, N>(f); }
}

DI u32x4 trfrag(LAS char* p) {
  s16x4 lo = __builtin_amdgcn_ds_read_tr16_b64_v4i16((LAS s16x4*)p);
  s16x4 hi = __builtin_amdgcn_ds_read_tr16_b64_v4i16((LAS s16x4*)(p + 512));
  u32x2 a = __builtin_bit_cast(u32x2, lo), b = __builtin_bit_cast(u32x2, hi);
  return u32x4{a.x, a.y, b.x, b.y};
}
DI f32x16 mfma(u32x4 a, u32x4 b, f32x16 c) {
  return __builtin_amdgcn_mfma_f32_32x32x16_bf16(__builtin_bit_cast(bf16x8, a), __builtin_bit_cast(bf16x8, b), c, 0, 0, 0);
}
DI void mfma_at(f32x16& c, u32x4 a, u32x4 b) { c = mfma(a, b, c); asm volatile("" : "+v"(c)); }
DI u32 pk(float a, float b) { bf16x2 c; c[0] = (__bf16)a; c[1] = (__bf16)b; return __builtin_bit_cast(u32, c); }
DI u32x4 pk8(const f32x16& v, int s) { return u32x4{pk(v[8 * s], v[8 * s + 1]), pk(v[8 * s + 2], v[8 * s + 3]), pk(v[8 * s + 4], v[8 * s + 5]), pk(v[8 * s + 6], v[8 * s + 7])}; }
DI float sigmoidf(float z) { return __builtin_amdgcn_rcpf(1.f + __builtin_amdgcn_exp2f(z * -1.4426950408889634f)); }
DI float glu(float g, float u) { return u * sigmoidf(g); }
DI float xhalf_max(float v) { auto r = __builtin_amdgcn_permlane32_swap(__float_as_uint(v), __float_as_uint(v), false, false); return fmaxf(__uint_as_float(r[0]), __uint_as_float(r[1])); }
DI float xhalf_sum(float v) { auto r = __builtin_amdgcn_permlane32_swap(__float_as_uint(v), __float_as_uint(v), false, false); return __uint_as_float(r[0]) + __uint_as_float(r[1]); }

DI void spin(const char* ws, u32 off, bool need, u64 tag) {
  const GAS u64* p = (const GAS u64*)(ws + off);
#pragma unroll 1
  for (int i = 0; i < SPIN_CAP; ++i) {
    u64 v = __hip_atomic_load(p, __ATOMIC_RELAXED, __HIP_MEMORY_SCOPE_AGENT);
    if (__all(!need || v == tag)) break;
    __builtin_amdgcn_s_sleep(2);
  }
  asm volatile("" ::: "memory");
}
DI bool match(u32x2 v, bool need, u64 tag) { return __all(!need || (v.x == (u32)tag && v.y == (u32)(tag >> 32))); }
DI u64 tag_of(u32x2 ev) {
  u64 e = ((u64)__builtin_amdgcn_readfirstlane(ev.y) << 32) | (u32)__builtin_amdgcn_readfirstlane(ev.x);
  return e * 6364136223846793005ull + 1442695040888963407ull;
}
DI void put_tag(u64 tag, rsrc_t rs, u32 voff, u32 soff) { __builtin_amdgcn_raw_buffer_store_b64(u32x2{(u32)tag, (u32)(tag >> 32)}, rs, voff, soff, SC1); }

constexpr char HEAD[] = "KKGKKGKKGK";
constexpr int BARS[] = {0, 2, 6, 8, 10, 14, 16, 18, 22, 24, 28, 46, 62, 68, 75, 79, 86, 96};
constexpr int PRO_DMA = 9;
constexpr int HEAD_N = 26, HEAD_RATE = 20;
constexpr int EP1 = 2;
constexpr int U_KVPUB = 25, U_H1PUB = 41, U_H2PUB = 57, U_POLL1 = 52, U_CHK1 = 56, U_POLL2 = 64, U_CHK2 = 68, U_POLL3 = 70, U_CHK3 = 76;
constexpr int NV = 105, U0 = 14, IT_OMG = 28;
constexpr int imin(int a, int b) { return a < b ? a : b; }
constexpr int item_of(int i) { return i < IT_OMG ? i : i + 1; }
constexpr int ld_rel(int k) { return k < 6 ? 0 : 1 + (k - 6) / 2; }
constexpr int ld_iter(int it) { return U0 + (it < 14 ? U_CHK1 + ld_rel(it) : (it < 28 ? U_CHK2 + ld_rel(it - 14) : U_CHK3 + (it - 28) / 2)); }
struct Sched {
  int nhead = 0, vkvst = 0, vg1 = 0;
  int xl[NV + 2] = {}, wl[NV + 2] = {};
  int kind[NV + 2] = {}, idx[NV + 2] = {}, lo[NV + 2] = {}, hi[NV + 2] = {};
  int pk[84] = {}, pi[84] = {}, pj[84] = {};
  bool bar[NV] = {};
  int cum[NV + 1] = {}, base[NV + 1] = {}, nst[NV] = {}, ldlo[NV + 1] = {}, cnt[NV] = {}, pdma[84] = {}, pld[36] = {}, freed[NV] = {};
  int cnt0 = 0; bool ok = true;
  constexpr Sched() {
    int v = 0, n = 0, k = 0, g = 0;
    int nx = 0;
    auto add = [&](int kd, int st, int nit, int nw) {
      const int first = n; int x = -1;
      if (st < 7 && st == nx) { x = n; for (int e = 0; e < 2; ++e) { pk[n] = 0; pi[n] = st; pj[n] = e; ++n; } ++nx; }
      for (int e = 0; e < nit; ++e) { kind[v] = kd; idx[v] = nit * st + e; lo[v] = first; hi[v] = n + nw - 1; xl[v] = x; wl[v] = n; ++v; }
      for (int e = 0; e < nw; ++e) { pk[n] = kd; pi[n] = st; pj[n] = kd ? e : 2; ++n; }
    };
    for (int i = 0; HEAD[i]; ++i) { if (HEAD[i] == 'K') { add(0, k++, 2, 1); nhead = v; } else { add(1, g++, 4, 2); if (g == 7) vg1 = v; } }
    if (k != 7 || g > 7) ok = false;
    vkvst = nhead + 1;
    while (g < 14) { add(1, g++, 4, 2); if (g == 7) vg1 = v; }
    for (int i = 0; i < 35; ++i) { kind[v] = 2; idx[v] = 56 + i; lo[v] = hi[v] = wl[v] = n; pk[n] = 2; pi[n] = i; ++n; ++v; }
    kind[NV] = kind[NV + 1] = 3; hi[NV] = hi[NV + 1] = 83;
    for (int e : BARS) bar[e] = true;
    bar[U0 + U_KVPUB] = bar[U0 + U_H1PUB] = bar[U0 + U_H2PUB] = bar[U0 + U_CHK2] = bar[U0 + U_CHK3] = true;
    if (U0 + U_KVPUB <= vkvst || U0 + U_H1PUB <= vg1 + 2 * EP1 || bar[U0 + U_CHK1]) ok = false;
    int fr = 0, cc = PRO_DMA, cr = 0, it = 0;
    cum[0] = cc; base[0] = PRO_DMA + 1;
    for (n = 0; n < PRO_DMA; ++n) pdma[n] = n;
    for (v = 0; v < NV; ++v) {
      const int u = v - U0;
      if (bar[v] || u == U_CHK1) fr = lo[v];
      freed[v] = fr;
      nst[v] = 3 * (v == vkvst) + (v == vg1 + EP1) + (v == vg1 + 2 * EP1) + 2 * (u == 56) + (u == U_POLL1) + (u == U_POLL2) + (u == U_POLL3);
      ldlo[v] = it;
      while (it < 36 && ld_iter(it) == v) { pld[it] = base[v] + nst[v] + (it - ldlo[v]); ++it; }
      cr += v < HEAD_N ? HEAD_RATE : 16;
      const int t = imin(imin(cc + imin(cr / 16, 2), fr + 20), 84);
      cr -= 16 * (t - cc); if (cr > 16) cr = 16;
      for (n = cc; n < t; ++n) pdma[n] = base[v] + nst[v] + (it - ldlo[v]) + (n - cc);
      base[v + 1] = base[v] + nst[v] + (it - ldlo[v]) + (t - cc);
      cc = t; cum[v + 1] = cc;
    }
    ldlo[NV] = it;
    if (it != 36 || cc != 84) ok = false;
    for (v = -1; v < NV; ++v) {
      if (v >= 0 && !bar[v]) continue;
      int nb = v + 1; while (nb < NV && !bar[nb]) ++nb;
      const int need = hi[nb + 1], at = v < 0 ? 0 : v;
      if (need >= cum[at]) ok = false;
      int c = base[at] - pdma[need] - 1;
      const int u = v - U0;
      if (u == U_KVPUB) c = imin(c, base[v] - (base[vkvst] + nst[vkvst]));
      if (u == U_H1PUB) c = imin(c, base[v] - (base[vg1 + 2 * EP1] + nst[vg1 + 2 * EP1]));
      if (u == U_H2PUB) c = imin(c, base[v] - (base[U0 + 56] + nst[U0 + 56]));
      if (v < 0) cnt0 = c; else cnt[v] = c;
    }
    for (int i = 0; i < 35; ++i) if (ld_iter(item_of(i)) > U0 + 56 + i) ok = false;
  }
};
constexpr Sched SA{};
static_assert(SA.ok, "schedule: a piece or operand is used before it is requested, or HEAD is malformed");
static_assert(U_CHK1 == 56 && U_POLL1 < U_CHK1 && U_CHK1 < U_POLL2 && U_POLL2 < U_CHK2 && U_CHK2 < U_POLL3 && U_POLL3 < U_CHK3, "");
static_assert(U_KVPUB < U_H1PUB && U_H1PUB < U_CHK1 && U_H2PUB > 56 && U_H2PUB < U_CHK2, "a workgroup publishes a class before it waits for that class");
static_assert(SA.freed[NV - 1] >= 72, "the reduction scratch (slots 4..11 = pieces 64..71) must be free at the end");

struct Ctx {
  LAS char* sm; u32 m0w;
  rsrc_t rx, rqkv, rwg, rwu, rwd, rwo, rws;
  const char* ws;
  u32 pf;
  u32 kc[7], seq;
  u32 vx, vkv, vg, vu, vw2;
};

template <int N> DI void issueA(const Ctx& c) {
  constexpr u32 dst = (N % 20) * SLOT;
  constexpr int k = SA.pk[N], st = SA.pi[N], j = SA.pj[N];
  if constexpr (k == 0) {
    if constexpr (j < 2) dmam<dst, 128>(c.m0w, c.vx + j * 896, c.rx, c.kc[st]);
    else dmam<dst, 64 * 2688>(c.m0w, c.vkv, c.rqkv, c.kc[st]);
  } else if constexpr (k == 1) {
    if constexpr (j == 0) dmam<dst, 64 * 3584>(c.m0w, c.vg + (st / 7) * 1792, c.rwg, c.kc[st % 7]);
    else dmam<dst, 64 * 3584>(c.m0w, c.vu + (st / 7) * 1792, c.rwu, c.kc[st % 7]);
  } else if constexpr (st < 28) dma<dst>(c.m0w, c.vw2, c.rwd, st * 64 * 896);
  else dma<dst>(c.m0w, c.vw2, c.rwo, (st - 28) * 64 * 896);
}

DI void type_a(Ctx& c, int w, int lane, int mt, int role, int R2, int n2, void* out) {
  const int r = lane & 31, hi = lane >> 5, blk = (lane >> 4) & 1, q = (lane & 15) >> 2, p = lane & 3;
  const int sp = ((p & 1) << 1) | (p >> 1);
  const int rg = w & 3, cg = w >> 2, T = mt * 4 + rg;
  const bool isv = role >= 7;
  LAS char* sm = c.sm;
  static_for<2, PRO_DMA>([&](auto N) { issueA<N>(c); });
  if (w == 0) put_tag(0, c.rws, lane < 3 ? lane * 4096 + role * 8 : OOB, WS_FLAG + mt * 256);
  u32 pw = ld32_sc1((w == 1 || w == 2) && lane < 13 ? lane * PWSTEP + 49152 : OOB, w == 1 ? c.rws : mk(out, 2048 * 896), 0);
  const int R = 32 * (rg & 1) + r, xm = (R >> 1) & 7;
  int xa[4];
#pragma unroll
  for (int s = 0; s < 4; ++s) xa[s] = (rg >> 1) * SLOT + R * 128 + (((2 * s + hi) ^ xm) << 4);
  const int rowb = (8 * hi + q) * 128, q2 = q & 2;
  const int akv = rowb + (((2 * cg + blk) ^ q2) << 5) + (isv ? p : sp) * 8;
  int agu[2];
#pragma unroll
  for (int i = 0; i < 2; ++i) agu[i] = blk * SLOT + rowb + (((2 * cg + i) ^ q2 ^ blk) << 5) + sp * 8;
  int aguw[2][3];
#pragma unroll
  for (int i = 0; i < 2; ++i)
#pragma unroll
    for (int k = 0; k < 3; ++k) { aguw[i][k] = agu[i] + k * 65536; pin(aguw[i][k]); }
  const int rt = w & 1, kq = w >> 1;
  int aw2[2], aw2w[2][3];
#pragma unroll
  for (int i = 0; i < 2; ++i) aw2[i] = kq * 2048 + rowb + (((2 * i + blk) ^ q2) << 5) + p * 8;
  const int T2 = (R2 >> 1) * 4 + 2 * rt + (R2 & 1);

  u32x4 xf[28], wf[2][2], af[35], hp[2];
  f32x16 akvacc = {}, g1[2] = {}, g2[2] = {}, ah[2] = {}, ao[2] = {};
  u32x2 ev, pollv; u32 omg; u64 tag;
  const u32 lane16 = lane * 16;
  const u32 poll_off = WS_FLAG + (R2 >> 1) * 256 + (lane & 15) * 8;
  const bool need_h = (lane & 15) < 14;
  const u32 a2_off = WS_A2 + T2 * A2_T + kq * 1024 + lane16;
  const u32 h_off = WS_A2 + T * A2_T + (role * 4 + 2 * cg) * 1024 + lane16;

  auto read_w = [&](auto V, auto I) {
    constexpr int v = V, i = I, k = SA.kind[v], u = SA.idx[v], sl = SA.wl[v] % 20;
    if constexpr (k < 2 && SA.xl[v] >= 0 && (k == 0 || i == 0)) {
      constexpr int ks = k == 0 ? 2 * u + i : u, xs = SA.xl[v] % 20;
      const int wrap = xs == 19 ? (rg >> 1) * (-20 * SLOT) : 0;
      xf[ks] = *(LAS u32x4*)(sm + xa[ks % 4] + wrap + xs * SLOT);
    }
    if constexpr (k == 0) {
      wf[v & 1][i] = trfrag(sm + akv + sl * SLOT + ((2 * u + i) % 4) * 2048);
    } else if constexpr (k == 1) {
      const int wrap = sl == 19 ? blk * (-20 * SLOT) : 0;
      wf[v & 1][i] = trfrag(sm + aguw[i][sl / 8] + wrap + (sl % 8) * SLOT + (u % 4) * 2048);
    } else if constexpr (k == 2) {
      if constexpr (u == 56) {
#pragma unroll
        for (int e = 0; e < 3; ++e) { aw2w[i][e] = aw2[i] + e * 65536; pin(aw2w[i][e]); }
      }
      wf[v & 1][i] = trfrag(sm + aw2w[i][sl / 8] + (sl % 8) * SLOT);
    }
  };
  auto publish = [&](int cls) {
    if (w == 0) put_tag(tag, c.rws, lane == 0 ? role * 8 : OOB, WS_FLAG + cls * 4096 + mt * 256);
  };
  auto load_item = [&](auto IT) {
    constexpr int it = IT;
    if constexpr (it == IT_OMG) omg = ld32_sc1(r * 4, c.rws, WS_OMG + T2 * 128);
    else { constexpr int i = it < IT_OMG ? it : it - 1; af[i] = ld128_sc1(a2_off, c.rws, i * 4096); }
  };
  auto check = [&](auto CNT, int cls, bool need) {
    wait_vm<CNT>();
    pin(pollv);
    if (w == 0 && !match(pollv, need, tag)) spin(c.ws, poll_off + cls * 4096, need, tag);
  };

  sync_vm<SA.cnt0>();
  pin(c.pf);
  static_for<0, 4>([&](auto S) { read_w(IC<S / 2>{}, IC<S % 2>{}); });
  SCHED();
  auto iter = [&](auto V, auto ISV) {
    constexpr int v = V, b = v & 1, kd = SA.kind[v], ui = SA.idx[v], u = v - U0;
    if constexpr (u == U_CHK2) check(IC<SA.base[v] - SA.base[U0 + U_POLL2] - 1>{}, 2, need_h);
    if constexpr (u == U_CHK3) check(IC<SA.base[v] - SA.base[U0 + U_POLL3] - 1>{}, 2, !need_h);
    if constexpr (SA.bar[v]) sync_vm<SA.cnt[v]>();
    if constexpr (u == U_KVPUB) {
      pin(ev); pin(pw);
      tag = tag_of(ev);
      publish(0);
    }
    if constexpr (u == U_H1PUB) publish(1);
    if constexpr (u == U_H2PUB) publish(2);
    if constexpr (u == U_POLL1) pollv = ld64_sc1(w == 0 ? poll_off : OOB, c.rws, 4096);
    if constexpr (u == U_POLL2 || u == U_POLL3) pollv = ld64_sc1(w == 0 ? poll_off : OOB, c.rws, 8192);
    if constexpr (v == SA.vkvst) {
      ev = ld64_sc1(w == 0 ? WS_E : OOB, c.rws, 0);
#pragma unroll
      for (int s = 0; s < 2; ++s) {
        const u32 off = isv ? WS_V + T * KV_T + (s * 2 + hi) * 7168 + ((role - 7) * 64 + cg * 32 + r) * 16
                            : WS_K + T * KV_T + (role * 8 + cg * 4 + 2 * s) * 512 + lane16;
        st128(pk8(akvacc, s), c.rws, off, 0);
      }
    }
    if constexpr (v == SA.vg1 + EP1) st128(hp[0], c.rws, h_off, 0);
    if constexpr (v == SA.vg1 + 2 * EP1) st128(hp[1], c.rws, h_off, 1024);
    if constexpr (u == 56) {
#pragma unroll
      for (int t = 0; t < 2; ++t) {
#pragma unroll
        for (int j = 0; j < 8; j += 2) hp[t][j / 2] = pk(glu(g2[t][j], g2[t][8 + j]), glu(g2[t][j + 1], g2[t][9 + j]));
        st128(hp[t], c.rws, h_off, (56 + t) * 1024);
        SCHED();
      }
    }
    asm volatile("" ::: "memory");
    if constexpr (u == U_CHK1) {
      check(IC<SA.base[v] + SA.nst[v] - SA.base[U0 + U_POLL1] - 1>{}, 1, need_h);
      asm volatile("s_barrier" ::: "memory");
    }
    constexpr bool ld_first = u >= 56 && ld_iter(item_of(u < 56 ? 0 : u - 56)) == v;
    constexpr int nld = SA.ldlo[v + 1] - SA.ldlo[v], nd = SA.cum[v + 1] - SA.cum[v];
    if constexpr (ld_first) static_for<SA.ldlo[v], SA.ldlo[v + 1]>(load_item);
    if constexpr (u >= 56) {
      wait_vm<SA.base[v] + SA.nst[v] + (ld_first ? nld : 0) - SA.pld[item_of(u - 56)] - 1>();
      pin(af[u - 56]);
    }
    SCHED();
    static_for<0, 2>([&](auto I) {
      constexpr int i = I;
      if constexpr (kd == 0) { if constexpr (ISV) mfma_at(akvacc, xf[2 * ui + i], wf[b][i]); else mfma_at(akvacc, wf[b][i], xf[2 * ui + i]); }
      else if constexpr (ui < 28) mfma_at(g1[i], wf[b][i], xf[ui]);
      else if constexpr (ui < 56) mfma_at(g2[i], wf[b][i], xf[ui - 28]);
      else if constexpr (ui < 84) mfma_at(ah[i], wf[b][i], af[ui - 56]);
      else mfma_at(ao[i], wf[b][i], af[ui - 56]);
      SCHED();
      read_w(IC<v + 2>{}, I);
      if constexpr (v >= SA.vg1 && v < SA.vg1 + 2 * EP1) {
        constexpr int t = (v - SA.vg1) / EP1, per = 4 / EP1, k0 = ((v - SA.vg1) % EP1) * per;
#pragma unroll
        for (int k = k0 + (i * per + 1) / 2; k < k0 + ((i + 1) * per + 1) / 2; ++k) {
          u32 hv = pk(glu(g1[t][2 * k], g1[t][8 + 2 * k]), glu(g1[t][2 * k + 1], g1[t][9 + 2 * k]));
          pin(hv);
          hp[t][k] = hv;
        }
      }
      if constexpr (i == 0 && !ld_first) static_for<SA.ldlo[v], SA.ldlo[v + 1]>(load_item);
      if constexpr (i == 0 && nd == 2) issueA<SA.cum[v]>(c);
      if constexpr (i == 1 && nd >= 1) issueA<SA.cum[v + 1] - 1>(c);
      SCHED();
    });
  };
  if (isv) static_for<0, SA.nhead>([&](auto V) { iter(V, IC<1>{}); });
  else static_for<0, SA.nhead>([&](auto V) { iter(V, IC<0>{}); });
  static_for<SA.nhead, NV>([&](auto V) { iter(V, IC<0>{}); });
  pin(omg);
  const float om = __uint_as_float(omg);
  LAS char* red = sm + 4 * SLOT;
#pragma unroll
  for (int t = 0; t < 2; ++t)
#pragma unroll
    for (int a = 0; a < 4; ++a) {
      f32x4 v;
#pragma unroll
      for (int e = 0; e < 4; ++e) v[e] = ah[t][4 * a + e] * om + ao[t][4 * a + e];
      *(LAS f32x4*)(red + kq * 16384 + (2 * r + rt) * 256 + (((8 * t + 2 * a + hi) ^ (r & 15)) << 4)) = v;
    }
  asm volatile("s_waitcnt lgkmcnt(0)\n\ts_barrier" ::: "memory");
  {
    const int row = w * 8 + (lane >> 3), c8 = lane & 7, m = (row >> 1) & 15;
    f32x4 s0 = {}, s1 = {};
#pragma unroll
    for (int k = 0; k < 4; ++k) {
      s0 += *(LAS f32x4*)(red + k * 16384 + row * 256 + (((2 * c8) ^ m) << 4));
      s1 += *(LAS f32x4*)(red + k * 16384 + row * 256 + (((2 * c8 + 1) ^ m) << 4));
    }
    const u32x4 o = {pk(s0[0], s0[1]), pk(s0[2], s0[3]), pk(s1[0], s1[1]), pk(s1[2], s1[3])};
    __builtin_amdgcn_raw_buffer_store_b128(o, mk(out, 2048 * 896), (R2 * 64 + row) * 896 + n2 * 128 + c8 * 16, 0, SC1);
  }
}

constexpr int PRO_B = 12, GB_TOUCH = 18, GB_POLL = 25;
constexpr int cumB(int g) {
  int cc = PRO_B;
  for (int j = 0; j <= g; ++j) cc = imin(imin(cc + 2, 6 * ((j + 1) / 4) + 18), 42);
  return cc;
}
static_assert(cumB(GB_TOUCH - 1) == 42 && GB_POLL > 23, "both sit behind the last ring piece; the poll behind the last counted barrier");
template <int N> DI void issueB(const Ctx& c) {
  constexpr int kc = N / 6, j = N % 6;
  constexpr u32 dst = (N % 18) * SLOT;
  if constexpr (j < 2) dmam<dst, 128>(c.m0w, c.vx + j * 896, c.rx, c.kc[kc]);
  else dmam<dst, 64 * 2688>(c.m0w, c.vkv + (j - 2) * 128, c.rqkv, c.kc[kc]);
}

DI void type_b(Ctx& c, int w, int lane, int mt, int hf, const void* gw) {
  const int r = lane & 31, hi = lane >> 5, blk = (lane >> 4) & 1, q = (lane & 15) >> 2, p = lane & 3;
  const int sp = ((p & 1) << 1) | (p >> 1);
  const int rg = w & 3, cg = w >> 2, T = mt * 4 + rg;
  LAS char* sm = c.sm;
  if (w == 0) dma<18 * SLOT>(c.m0w, ((c.seq >> (4 * (lane >> 3))) & 7) * 128 + (lane & 7) * 16, mk(gw, 896), 0);
  static_for<2, PRO_B>([&](auto N) { issueB<N>(c); });
  if (w == 0) put_tag(0, c.rws, lane == 0 ? (14 + hf) * 8 : OOB, WS_FLAG + 8192 + mt * 256);
  const int R = 32 * (rg & 1) + r, xm = (R >> 1) & 7;
  int xa[4], aq[4];
#pragma unroll
  for (int s = 0; s < 4; ++s) xa[s] = (rg >> 1) * SLOT + R * 128 + (((2 * s + hi) ^ xm) << 4);
  const int h0 = 8 * hf + 4 * cg;
#pragma unroll
  for (int i = 0; i < 4; ++i) {
    const int col = 28 * (h0 + i) - 4 * (i & 1) - 192 * hf + 16 * blk + 4 * sp;
    aq[i] = (2 + (col >> 6)) * SLOT + (8 * hi + q) * 128 + (((col & 63) * 2) ^ ((q & 2) << 5));
  }
  const bool first = (mt == 0) && !(rg & 1);
  const int Tp = first ? T : ((rg & 1) ? T - 1 : T - 3);
  const u32 lane16 = lane * 16, lo16 = hi ? OOB : lane16;
  const u32 ob = WS_A2 + T * A2_T + (1792 + 28 * h0) * 64;
  const u32 omg_off = (hf == 0 && cg == 0 && hi == 0) ? r * 4 : OOB;
  const u32 poll_off = WS_FLAG + ((lane & 16) && mt > 0 ? mt - 1 : mt) * 256 + (lane & 15) * 8;
  const bool poll_need = (lane & 15) < 14;
  u32x4 xf[2], gf[2], wf[2][4];
  u32x4 kf[4][2], kp[2], vf[4][2], vp[4];
  f32x16 qa[4] = {};
  u32x2 ev, pollv;
  u32 tw;
  const int sr = (r & 19) | ((r & 4) << 1) | ((r & 8) >> 1);
  const u32 kp_off = (28 * (r >> 3) - 4 * ((r >> 3) & 1)) * 64 + (hi * 32 + 24 + (r & 7)) * 16;
  const u32 v_off = hi * 7168 + sr * 16;
  auto load_kv = [&](auto IT) {
    constexpr int it = IT;
    if constexpr (it < 2) kp[it] = ld128_sc1(kp_off, c.rws, WS_K + Tp * KV_T + 28 * h0 * 64 + it * 1024);
    else if constexpr (it < 10) {
      constexpr int i = (it - 2) / 2, k = it & 1;
      kf[i][k] = ld128_sc1(lane16, c.rws, WS_K + T * KV_T + (28 * (h0 + i) - 4 * (i & 1)) * 64 + k * 1024);
    } else {
      constexpr int i = (it - 10) / 3, k = (it - 10) % 3;
      const int wh = 28 * (h0 + i) - 4 * (i & 1);
      if constexpr (k == 0) vp[i] = ld128_sc1(v_off, c.rws, WS_V + Tp * KV_T + 14336 + wh * 16);
      else vf[i][k - 1] = ld128_sc1(v_off, c.rws, WS_V + T * KV_T + (k - 1) * 14336 + wh * 16);
    }
  };
  auto read_x = [&](auto G) {
    constexpr int g = G;
    if constexpr (g < 28) {
      xf[g & 1] = *(LAS u32x4*)(sm + xa[g % 4] + ((6 * (g / 4)) % 18) * SLOT);
      gf[g & 1] = *(LAS u32x4*)(sm + 18 * SLOT + g * 32 + hi * 16);
    }
  };
  auto read_w = [&](auto G, auto I) {
    constexpr int g = G;
    if constexpr (g < 28) wf[g & 1][I] = trfrag(sm + aq[I] + ((6 * (g / 4)) % 18) * SLOT + (g % 4) * 2048);
  };
  sync_vm<PRO_B - 6>();
  pin(c.pf);
  read_x(IC<0>{});
  static_for<0, 4>([&](auto I) { read_w(IC<0>{}, I); });
  SCHED();
  static_for<0, 28>([&](auto G) {
    constexpr int g = G, b = g & 1;
    if constexpr (g % 4 == 3 && g < 27) { asm volatile("s_waitcnt lgkmcnt(0)" ::: "memory"); sync_vm<cumB(g - 1) - 6 * (g / 4 + 2) + (g > GB_TOUCH)>(); }
    if constexpr (g == GB_POLL) { ev = ld64_sc1(w == 0 ? WS_E : OOB, c.rws, 0); pollv = ld64_sc1(w == 0 ? poll_off : OOB, c.rws, 0); }
    if constexpr (g == GB_TOUCH) {
      const int gi = (mt * 2 + hf) * 8 + w;
      tw = touch(((gi < 196 ? gi : gi - 196) * 64 + lane) * 128, gi < 196 ? c.rwd : c.rwo);
    }
    SCHED();
    static_for<0, 4>([&](auto I) {
      constexpr int i = I;
      mfma_at(qa[i], wf[b][i], xf[b]);
      SCHED();
      if constexpr (i == 0) read_x(IC<g + 1>{});
      read_w(IC<g + 1>{}, I);
      if constexpr (i < 2) {
        wf[b][2][2 * i] = r == 28 ? gf[b][2 * i] : wf[b][2][2 * i];
        wf[b][2][2 * i + 1] = r == 28 ? gf[b][2 * i + 1] : wf[b][2][2 * i + 1];
        pin(wf[b][2]);
      }
      if constexpr (i >= 2 && cumB(g - 1) + i - 2 < cumB(g)) issueB<cumB(g - 1) + i - 2>(c);
      SCHED();
    });
  });
  wait_vm<0>();
  pin(pollv); pin(ev); pin(tw);
  const u64 tag = tag_of(ev);
  if (w == 0 && !match(pollv, poll_need, tag)) spin(c.ws, poll_off, poll_need, tag);
  asm volatile("s_barrier" ::: "memory");
  static_for<0, 6>(load_kv);
  SCHED();
  const float z = __uint_as_float(__builtin_amdgcn_permlane32_swap(__float_as_uint(qa[2][12]), __float_as_uint(qa[2][12]), false, false)[1]);
  const float gate = sigmoidf(z);
  const u32 omg = __float_as_uint(sigmoidf(-z));
  int ab[4];
  u32 msk[5][2];
#pragma unroll
  for (int b = 0; b < 4; ++b) ab[b] = (r >> 3) - ((r & 7) < 4 * hi + b ? 1 : 0);
#pragma unroll
  for (int a = 0; a < 5; ++a)
#pragma unroll
    for (int e = 0; e < 2; ++e) msk[a][e] = (ab[2 * e] == a - 1 ? 0xFFFFu : 0u) | (ab[2 * e + 1] == a - 1 ? 0xFFFF0000u : 0u);
  const float csc = 0.18898223650461362f * 1.4426950408889634f;
  u32 p01[2], p23[2];
  u32x4 qf[4][2];
  static_for<0, 4>([&](auto I) {
    constexpr int i = I;
    qf[i][0] = pk8(qa[i], 0); qf[i][1] = pk8(qa[i], 1);
    if (i & 1) { qf[i][0][0] = hi ? qf[i][0][0] : 0u; qf[i][0][1] = hi ? qf[i][0][1] : 0u; }
    else       { qf[i][1][2] = hi ? 0u : qf[i][1][2]; qf[i][1][3] = hi ? 0u : qf[i][1][3]; }
    pin(qf[i][0]); pin(qf[i][1]);
  });
#pragma unroll
  for (int a = 0; a < 5; ++a) { pin(msk[a][0]); pin(msk[a][1]); }
  __builtin_amdgcn_raw_buffer_store_b32(omg, c.rws, omg_off, WS_OMG + T * 128, SC1);
  asm volatile("" ::: "memory");
  f32x16 so[2], sv[2], oa[2];
  auto scores = [&](auto J) {
    static_for<0, 2>([&](auto E) {
      constexpr int i = 2 * J + E;
      pin(kf[i][0]); pin(kf[i][1]);
      so[E] = mfma(kf[i][0], qf[i][0], f32x16{}); sv[E] = mfma(kp[0], qf[i][0], f32x16{});
      mfma_at(so[E], kf[i][1], qf[i][1]); mfma_at(sv[E], kp[1], qf[i][1]);
    });
  };
  auto softmax = [&](auto J) {
    static_for<0, 2>([&](auto E) {
      constexpr int i = 2 * J + E;
      float v[4];
#pragma unroll
      for (int b = 0; b < 4; ++b) {
        float t = first ? -1e30f : sv[E][4 * i + b];
#pragma unroll
        for (int a = 0; a < 4; ++a) t = ab[b] == a ? so[E][4 * a + b] : t;
        v[b] = t;
      }
      const float mx = xhalf_max(fmaxf(fmaxf(v[0], v[1]), fmaxf(v[2], v[3])));
#pragma unroll
      for (int b = 0; b < 4; ++b) v[b] = __builtin_amdgcn_exp2f((v[b] - mx) * csc);
      const float sc = gate * __builtin_amdgcn_rcpf(xhalf_sum((v[0] + v[1]) + (v[2] + v[3])));
      p01[E] = pk(v[0] * sc, v[1] * sc); p23[E] = pk(v[2] * sc, v[3] * sc);
    });
    pin(p01[0]); pin(p23[0]); pin(p01[1]); pin(p23[1]);
  };
  auto pv = [&](auto J) {
    static_for<0, 2>([&](auto E) {
      constexpr int i = 2 * J + E;
      pin(vp[i]); pin(vf[i][0]); pin(vf[i][1]);
      oa[E] = mfma(vp[i], u32x4{0u, 0u, p01[E] & msk[0][0], p23[E] & msk[0][1]}, f32x16{});
      oa[E] = mfma(vf[i][0], u32x4{p01[E] & msk[1][0], p23[E] & msk[1][1], p01[E] & msk[2][0], p23[E] & msk[2][1]}, oa[E]);
      mfma_at(oa[E], vf[i][1], u32x4{p01[E] & msk[3][0], p23[E] & msk[3][1], p01[E] & msk[4][0], p23[E] & msk[4][1]});
    });
  };
  auto store_o = [&](auto J) {
    u32x4 op[2][2] = {{pk8(oa[0], 0), pk8(oa[0], 1)}, {pk8(oa[1], 0), pk8(oa[1], 1)}};
#pragma unroll
    for (int e = 0; e < 2; ++e) { auto t = __builtin_amdgcn_permlane32_swap(op[0][1][e], op[1][0][e], false, false); op[0][1][e] = t[0]; op[1][0][e] = t[1]; }
    const u32 o2 = ob + J * 3584;
    st128(op[0][0], c.rws, lane16, o2);
    st128(op[0][1], c.rws, lo16, o2 + 1024);
    st128(op[1][0], c.rws, lane16, o2 + 1536);
    st128(op[1][1], c.rws, lane16, o2 + 2560);
  };
  static_for<10, 16>(load_kv);
  wait_vm<7>();
  pin(kp[0]); pin(kp[1]);
  scores(IC<0>{});
  static_for<6, 10>(load_kv);
  softmax(IC<0>{});
  static_for<16, 22>(load_kv);
  wait_vm<6>();
  pv(IC<0>{});
  scores(IC<1>{});
  store_o(IC<0>{});
  softmax(IC<1>{});
  wait_vm<4>();
  pv(IC<1>{});
  store_o(IC<1>{});
  sync_vm<0>();
  if (w == 0) put_tag(tag, c.rws, lane == 0 ? (14 + hf) * 8 : OOB, WS_FLAG + 8192 + mt * 256);
  if (mt == 0 && hf == 0 && w == 0) {
#pragma unroll 1
    for (int i = 0; i < 4; ++i) spin(c.ws, WS_FLAG + 8192 + (i * 4 + (lane >> 4)) * 256 + (lane & 15) * 8, true, tag);
    put_tag(tag, c.rws, lane == 0 ? 0 : OOB, WS_E);
  }
}

__global__ __launch_bounds__(512, 2) void fused_kernel(const void* __restrict__ x, const void* __restrict__ wg, const void* __restrict__ wu,
                                                       const void* __restrict__ wd, const void* __restrict__ wo, const void* __restrict__ wqkv,
                                                       const void* __restrict__ gw, void* __restrict__ out, void* __restrict__ ws) {
  __shared__ __attribute__((aligned(1024))) char smem[163840];
  const int lane = threadIdx.x & 63, w = __builtin_amdgcn_readfirstlane(threadIdx.x >> 6);
  Ctx c;
  {
    u64 pc;
    asm volatile("s_getpc_b64 %0" : "=s"(pc));
    c.pf = touch((w == 6 && lane < 8) ? ((blockIdx.x >> 3) * 8 + lane) * 128 : OOB, mk((const void*)(pc & ~255ull), CODE_BYTES));
  }
  asm volatile("" ::"s"(x), "s"(wg), "s"(wu), "s"(wd), "s"(wo), "s"(wqkv), "s"(gw), "s"(out), "s"(ws));
  const int b = blockIdx.x, xc = b & 7, ix = b >> 3;
  const int mq = xc >> 1, mt = mq * 4 + (ix & 3), rl = ix >> 2, rh = xc & 1;
  const bool isa = rl < 7;
  const int role = rh * 7 + rl;
  c.sm = (LAS char*)smem;
  c.m0w = (u32)(size_t)c.sm + w * 1024;
  c.rx = mk(x, 2048 * 896); c.rqkv = mk(wqkv, 448 * 2688); c.rwg = mk(wg, 448 * 3584); c.rwu = mk(wu, 448 * 3584);
  c.rwd = mk(wd, 1792 * 896); c.rwo = mk(wo, 448 * 896); c.rws = mk(ws, WS_END);
  c.ws = (const char*)ws;
  {
    constexpr u64 mod7 = 0x1065432106543210ull;
    c.seq = (u32)(mod7 >> (4 * ((mod7 >> (4 * (mq + 3 * rh + 2 * (ix & 3)))) & 7)));
#pragma unroll
    for (int j = 0; j < 7; ++j) { u32 k = (c.seq >> (4 * j)) & 7; asm volatile("" : "+s"(k)); c.kc[j] = k; }
  }
  const int r8 = lane >> 3, c8 = lane & 7, R = 8 * w + r8;
  c.vx = (mt * 128 + 2 * R) * 896 + ((c8 ^ ((R >> 1) & 7)) << 4);
  const int sw = (c8 ^ ((r8 & 2) << 1)) << 4;
  const int isai = (rl - 7) >> 31;
  c.vkv = R * 2688 + ((896 + role * 128) & isai) + ((rh * 384) & ~isai) + sw;
  static_for<0, 2>([&](auto N) { issueA<N>(c); });
  if (__builtin_expect(isa, 1)) {
    c.vg = R * 3584 + role * 128 + sw;
    c.vu = R * 3584 + role * 128 + (sw ^ 32);
    c.vw2 = R * 896 + rl * 128 + sw;
    type_a(c, w, lane, mt, role, xc * 4 + (ix & 3), rl, out);
  } else {
    type_b(c, w, lane, mt, rh, gw);
  }
}

extern "C" void kernel_launch(void* const* d_in, const int* in_sizes, int n_in, void* d_out, int out_size, void* d_ws, size_t ws_size,
                              hipStream_t stream) {
  fused_kernel<<<256, 512, 0, stream>>>(d_in[0], d_in[4], d_in[5], d_in[6], d_in[2], d_in[1], d_in[7], d_out, d_ws);
}
```

```cpp
#include <hip/hip_runtime.h>
#include <hip/hip_bf16.h>

typedef unsigned long long u64;
typedef unsigned u32;
typedef __bf16 bf16x8 __attribute__((ext_vector_type(8)));
typedef __bf16 bf16x2 __attribute__((ext_vector_type(2)));
typedef short s16x4 __attribute__((ext_vector_type(4)));
typedef u32 u32x4 __attribute__((ext_vector_type(4)));
typedef u32 u32x2 __attribute__((ext_vector_type(2)));
typedef float f32x16 __attribute__((ext_vector_type(16)));
typedef float f32x4 __attribute__((ext_vector_type(4)));
typedef __amdgpu_buffer_rsrc_t rsrc_t;
#define LAS __attribute__((address_space(3)))
#define GAS __attribute__((address_space(1)))
#define DI __device__ __forceinline__
#define SCHED() __builtin_amdgcn_sched_barrier(0)
template <int V> using IC = std::integral_constant<int, V>;

constexpr u32 WS_E = 0;
constexpr u32 WS_FLAG = 4096;
constexpr u32 WS_OMG = 32768;
constexpr u32 WS_K = 65536;
constexpr u32 KV_T = 28672;
constexpr u32 WS_V = WS_K + 64 * KV_T;
constexpr u32 WS_A2 = WS_V + 64 * KV_T;
constexpr u32 A2_T = 143360;
constexpr u32 WS_END = WS_A2 + 64 * A2_T;
constexpr u32 OOB = 0xFFFFFF00u;
constexpr u32 CODE_BYTES = 26624;
constexpr int SC1 = 16;
constexpr int SLOT = 8192;
constexpr int SPIN_CAP = 1 << 15;

DI rsrc_t mk(const void* p, u32 bytes) { return __builtin_amdgcn_make_buffer_rsrc((void*)p, (short)0, (int)bytes, 0x00020000); }
template <u32 DST> DI void dma(u32 m0w, u32 voff, rsrc_t rs, u32 soff) {
  asm volatile("s_add_u32 m0, %0, %4\n\ts_nop 0\n\tbuffer_load_dwordx4 %1, %2, %3 offen lds" ::"s"(m0w), "v"(voff), "s"(rs), "s"(soff), "n"(DST) : "memory", "scc");
}
template <u32 DST, u32 MUL> DI void dmam(u32 m0w, u32 voff, rsrc_t rs, u32 k) {
  u32 t;
  asm volatile("s_add_u32 m0, %1, %5\n\ts_mul_i32 %0, %4, %6\n\tbuffer_load_dwordx4 %2, %3, %0 offen lds" : "=&s"(t) : "s"(m0w), "v"(voff), "s"(rs), "s"(k), "n"(DST), "n"(MUL) : "memory", "scc");
}
DI u32x4 ld128_sc1(u32 voff, rsrc_t rs, u32 soff) {
  u32x4 v;
  asm volatile("buffer_load_dwordx4 %0, %1, %2, %3 offen sc1" : "=v"(v) : "v"(voff), "s"(rs), "s"(soff) : "memory");
  return v;
}
DI u32x2 ld64_sc1(u32 voff, rsrc_t rs, u32 soff) {
  u32x2 v;
  asm volatile("buffer_load_dwordx2 %0, %1, %2, %3 offen sc1" : "=v"(v) : "v"(voff), "s"(rs), "s"(soff) : "memory");
  return v;
}
DI u32 ld32_sc1(u32 voff, rsrc_t rs, u32 soff) {
  u32 v;
  asm volatile("buffer_load_dword %0, %1, %2, %3 offen sc1" : "=v"(v) : "v"(voff), "s"(rs), "s"(soff) : "memory");
  return v;
}
DI u32 touch(u32 voff, rsrc_t rs) {
  u32 v;
  asm volatile("buffer_load_dword %0, %1, %2, 0 offen" : "=v"(v) : "v"(voff), "s"(rs) : "memory");
  return v;
}
DI void st128(u32x4 v, rsrc_t rs, u32 voff, u32 soff) {
  asm volatile("buffer_store_dwordx4 %0, %1, %2, %3 offen sc1\n\ts_nop 1" ::"v"(v), "v"(voff), "s"(rs), "s"(soff) : "memory");
}
constexpr int clampvm(int n) { return n > 63 ? 63 : n; }
template <int N> DI void wait_vm() { static_assert(N >= 0, ""); asm volatile("s_waitcnt vmcnt(%0)" ::"n"(clampvm(N)) : "memory"); }
template <int N> DI void sync_vm() { static_assert(N >= 0, ""); asm volatile("s_waitcnt vmcnt(%0)\n\ts_barrier" ::"n"(clampvm(N)) : "memory"); }
template <class T> DI void pin(T& v) { asm volatile("" : "+v"(v)); }
template <int I, int N, class Fn> DI void static_for(Fn&& f) {
  if constexpr (I < N) { f(IC<I>{}); static_for<I + 1, N>(f); }
}

DI u32x4 trfrag(LAS char* p) {
  s16x4 lo = __builtin_amdgcn_ds_read_tr16_b64_v4i16((LAS s16x4*)p);
  s16x4 hi = __builtin_amdgcn_ds_read_tr16_b64_v4i16((LAS s16x4*)(p + 512));
  u32x2 a = __builtin_bit_cast(u32x2, lo), b = __builtin_bit_cast(u32x2, hi);
  return u32x4{a.x, a.y, b.x, b.y};
}
DI f32x16 mfma(u32x4 a, u32x4 b, f32x16 c) {
  return __builtin_amdgcn_mfma_f32_32x32x16_bf16(__builtin_bit_cast(bf16x8, a), __builtin_bit_cast(bf16x8, b), c, 0, 0, 0);
}
DI void mfma_at(f32x16& c, u32x4 a, u32x4 b) { c = mfma(a, b, c); asm volatile("" : "+v"(c)); }
DI u32 pk(float a, float b) { bf16x2 c; c[0] = (__bf16)a; c[1] = (__bf16)b; return __builtin_bit_cast(u32, c); }
DI u32x4 pk8(const f32x16& v, int s) { return u32x4{pk(v[8 * s], v[8 * s + 1]), pk(v[8 * s + 2], v[8 * s + 3]), pk(v[8 * s + 4], v[8 * s + 5]), pk(v[8 * s + 6], v[8 * s + 7])}; }
DI float sigmoidf(float z) { return __builtin_amdgcn_rcpf(1.f + __builtin_amdgcn_exp2f(z * -1.4426950408889634f)); }
DI float glu(float g, float u) { return u * sigmoidf(g); }
DI float xhalf_max(float v) { auto r = __builtin_amdgcn_permlane32_swap(__float_as_uint(v), __float_as_uint(v), false, false); return fmaxf(__uint_as_float(r[0]), __uint_as_float(r[1])); }
DI float xhalf_sum(float v) { auto r = __builtin_amdgcn_permlane32_swap(__float_as_uint(v), __float_as_uint(v), false, false); return __uint_as_float(r[0]) + __uint_as_float(r[1]); }

DI void spin(const char* ws, u32 off, bool need, u64 tag) {
  const GAS u64* p = (const GAS u64*)(ws + off);
#pragma unroll 1
  for (int i = 0; i < SPIN_CAP; ++i) {
    u64 v = __hip_atomic_load(p, __ATOMIC_RELAXED, __HIP_MEMORY_SCOPE_AGENT);
    if (__all(!need || v == tag)) break;
    __builtin_amdgcn_s_sleep(2);
  }
  asm volatile("" ::: "memory");
}
DI bool match(u32x2 v, bool need, u64 tag) { return __all(!need || (v.x == (u32)tag && v.y == (u32)(tag >> 32))); }
DI u64 tag_of(u32x2 ev) {
  u64 e = ((u64)__builtin_amdgcn_readfirstlane(ev.y) << 32) | (u32)__builtin_amdgcn_readfirstlane(ev.x);
  return e * 6364136223846793005ull + 1442695040888963407ull;
}
DI void put_tag(u64 tag, rsrc_t rs, u32 voff, u32 soff) { __builtin_amdgcn_raw_buffer_store_b64(u32x2{(u32)tag, (u32)(tag >> 32)}, rs, voff, soff, SC1); }

constexpr char HEAD[] = "KKGKKGKKGK";
constexpr int BARS[] = {0, 2, 6, 8, 10, 14, 16, 18, 22, 24, 28, 46, 62, 68, 75, 79, 86, 96};
constexpr int PRO_DMA = 9;
constexpr int HEAD_N = 26, HEAD_RATE = 20;
constexpr int EP1 = 2;
constexpr int U_KVPUB = 25, U_H1PUB = 41, U_H2PUB = 57, U_POLL1 = 52, U_CHK1 = 56, U_POLL2 = 64, U_CHK2 = 68, U_POLL3 = 70, U_CHK3 = 76;
constexpr int NV = 105, U0 = 14, IT_OMG = 28;
constexpr int imin(int a, int b) { return a < b ? a : b; }
constexpr int item_of(int i) { return i < IT_OMG ? i : i + 1; }
constexpr int ld_rel(int k) { return k < 6 ? 0 : 1 + (k - 6) / 2; }
constexpr int ld_iter(int it) { return U0 + (it < 14 ? U_CHK1 + ld_rel(it) : (it < 28 ? U_CHK2 + ld_rel(it - 14) : U_CHK3 + (it - 28) / 2)); }
struct Sched {
  int nhead = 0, vkvst = 0, vg1 = 0;
  int xl[NV + 2] = {}, wl[NV + 2] = {};
  int kind[NV + 2] = {}, idx[NV + 2] = {}, lo[NV + 2] = {}, hi[NV + 2] = {};
  int pk[84] = {}, pi[84] = {}, pj[84] = {};
  bool bar[NV] = {};
  int cum[NV + 1] = {}, base[NV + 1] = {}, nst[NV] = {}, ldlo[NV + 1] = {}, cnt[NV] = {}, pdma[84] = {}, pld[36] = {}, freed[NV] = {};
  int cnt0 = 0; bool ok = true;
  constexpr Sched() {
    int v = 0, n = 0, k = 0, g = 0;
    int nx = 0;
    auto add = [&](int kd, int st, int nit, int nw) {
      const int first = n; int x = -1;
      if (st < 7 && st == nx) { x = n; for (int e = 0; e < 2; ++e) { pk[n] = 0; pi[n] = st; pj[n] = e; ++n; } ++nx; }
      for (int e = 0; e < nit; ++e) { kind[v] = kd; idx[v] = nit * st + e; lo[v] = first; hi[v] = n + nw - 1; xl[v] = x; wl[v] = n; ++v; }
      for (int e = 0; e < nw; ++e) { pk[n] = kd; pi[n] = st; pj[n] = kd ? e : 2; ++n; }
    };
    for (int i = 0; HEAD[i]; ++i) { if (HEAD[i] == 'K') { add(0, k++, 2, 1); nhead = v; } else { add(1, g++, 4, 2); if (g == 7) vg1 = v; } }
    if (k != 7 || g > 7) ok = false;
    vkvst = nhead + 1;
    while (g < 14) { add(1, g++, 4, 2); if (g == 7) vg1 = v; }
    for (int i = 0; i < 35; ++i) { kind[v] = 2; idx[v] = 56 + i; lo[v] = hi[v] = wl[v] = n; pk[n] = 2; pi[n] = i; ++n; ++v; }
    kind[NV] = kind[NV + 1] = 3; hi[NV] = hi[NV + 1] = 83;
    for (int e : BARS) bar[e] = true;
    bar[U0 + U_KVPUB] = bar[U0 + U_H1PUB] = bar[U0 + U_H2PUB] = bar[U0 + U_CHK2] = bar[U0 + U_CHK3] = true;
    if (U0 + U_KVPUB <= vkvst || U0 + U_H1PUB <= vg1 + 2 * EP1 || bar[U0 + U_CHK1]) ok = false;
    int fr = 0, cc = PRO_DMA, cr = 0, it = 0;
    cum[0] = cc; base[0] = PRO_DMA;
    for (n = 0; n < PRO_DMA; ++n) pdma[n] = n;
    for (v = 0; v < NV; ++v) {
      const int u = v - U0;
      if (bar[v] || u == U_CHK1) fr = lo[v];
      freed[v] = fr;
      nst[v] = 3 * (v == vkvst) + (v == vg1 + EP1) + (v == vg1 + 2 * EP1) + 2 * (u == 56) + (u == U_POLL1) + (u == U_POLL2) + (u == U_POLL3);
      ldlo[v] = it;
      while (it < 36 && ld_iter(it) == v) { pld[it] = base[v] + nst[v] + (it - ldlo[v]); ++it; }
      cr += v < HEAD_N ? HEAD_RATE : 16;
      const int t = imin(imin(cc + imin(cr / 16, 2), fr + 20), 84);
      cr -= 16 * (t - cc); if (cr > 16) cr = 16;
      for (n = cc; n < t; ++n) pdma[n] = base[v] + nst[v] + (it - ldlo[v]) + (n - cc);
      base[v + 1] = base[v] + nst[v] + (it - ldlo[v]) + (t - cc);
      cc = t; cum[v + 1] = cc;
    }
    ldlo[NV] = it;
    if (it != 36 || cc != 84) ok = false;
    for (v = -1; v < NV; ++v) {
      if (v >= 0 && !bar[v]) continue;
      int nb = v + 1; while (nb < NV && !bar[nb]) ++nb;
      const int need = hi[nb + 1], at = v < 0 ? 0 : v;
      if (need >= cum[at]) ok = false;
      int c = base[at] - pdma[need] - 1;
      const int u = v - U0;
      if (u == U_KVPUB) c = imin(c, base[v] - (base[vkvst] + nst[vkvst]));
      if (u == U_H1PUB) c = imin(c, base[v] - (base[vg1 + 2 * EP1] + nst[vg1 + 2 * EP1]));
      if (u == U_H2PUB) c = imin(c, base[v] - (base[U0 + 56] + nst[U0 + 56]));
      if (v < 0) cnt0 = c; else cnt[v] = c;
    }
    for (int i = 0; i < 35; ++i) if (ld_iter(item_of(i)) > U0 + 56 + i) ok = false;
  }
};
constexpr Sched SA{};
static_assert(SA.ok, "schedule: a piece or operand is used before it is requested, or HEAD is malformed");
static_assert(U_CHK1 == 56 && U_POLL1 < U_CHK1 && U_CHK1 < U_POLL2 && U_POLL2 < U_CHK2 && U_CHK2 < U_POLL3 && U_POLL3 < U_CHK3, "");
static_assert(U_KVPUB < U_H1PUB && U_H1PUB < U_CHK1 && U_H2PUB > 56 && U_H2PUB < U_CHK2, "a workgroup publishes a class before it waits for that class");
static_assert(SA.freed[NV - 1] >= 72, "the reduction scratch (slots 4..11 = pieces 64..71) must be free at the end");

struct Ctx {
  LAS char* sm; u32 m0w;
  rsrc_t rx, rqkv, rwg, rwu, rwd, rwo, rws;
  const char* ws;
  u32 pf;
  u32 kc[7], seq;
  u32 vx, vkv, vg, vu, vw2;
};

template <int N> DI void issueA(const Ctx& c) {
  constexpr u32 dst = (N % 20) * SLOT;
  constexpr int k = SA.pk[N], st = SA.pi[N], j = SA.pj[N];
  if constexpr (k == 0) {
    if constexpr (j < 2) dmam<dst, 128>(c.m0w, c.vx + j * 896, c.rx, c.kc[st]);
    else dmam<dst, 64 * 2688>(c.m0w, c.vkv, c.rqkv, c.kc[st]);
  } else if constexpr (k == 1) {
    if constexpr (j == 0) dmam<dst, 64 * 3584>(c.m0w, c.vg + (st / 7) * 1792, c.rwg, c.kc[st % 7]);
    else dmam<dst, 64 * 3584>(c.m0w, c.vu + (st / 7) * 1792, c.rwu, c.kc[st % 7]);
  } else if constexpr (st < 28) dma<dst>(c.m0w, c.vw2, c.rwd, st * 64 * 896);
  else dma<dst>(c.m0w, c.vw2, c.rwo, (st - 28) * 64 * 896);
}

DI void type_a(Ctx& c, int w, int lane, int mt, int role, int R2, int n2, void* out) {
  const int r = lane & 31, hi = lane >> 5, blk = (lane >> 4) & 1, q = (lane & 15) >> 2, p = lane & 3;
  const int sp = ((p & 1) << 1) | (p >> 1);
  const int rg = w & 3, cg = w >> 2, T = mt * 4 + rg;
  const bool isv = role >= 7;
  LAS char* sm = c.sm;
  static_for<2, PRO_DMA>([&](auto N) { issueA<N>(c); });
  if (w == 0) put_tag(0, c.rws, lane < 3 ? lane * 4096 + role * 8 : OOB, WS_FLAG + mt * 256);
  const int R = 32 * (rg & 1) + r, xm = (R >> 1) & 7;
  int xa[4];
#pragma unroll
  for (int s = 0; s < 4; ++s) xa[s] = (rg >> 1) * SLOT + R * 128 + (((2 * s + hi) ^ xm) << 4);
  const int rowb = (8 * hi + q) * 128, q2 = q & 2;
  const int akv = rowb + (((2 * cg + blk) ^ q2) << 5) + (isv ? p : sp) * 8;
  int agu[2];
#pragma unroll
  for (int i = 0; i < 2; ++i) agu[i] = blk * SLOT + rowb + (((2 * cg + i) ^ q2 ^ blk) << 5) + sp * 8;
  int aguw[2][3];
#pragma unroll
  for (int i = 0; i < 2; ++i)
#pragma unroll
    for (int k = 0; k < 3; ++k) { aguw[i][k] = agu[i] + k * 65536; pin(aguw[i][k]); }
  const int rt = w & 1, kq = w >> 1;
  int aw2[2], aw2w[2][3];
#pragma unroll
  for (int i = 0; i < 2; ++i) aw2[i] = kq * 2048 + rowb + (((2 * i + blk) ^ q2) << 5) + p * 8;
  const int T2 = (R2 >> 1) * 4 + 2 * rt + (R2 & 1);

  u32x4 xf[28], wf[2][2], af[35], hp[2];
  f32x16 akvacc = {}, g1[2] = {}, g2[2] = {}, ah[2] = {}, ao[2] = {};
  u32x2 ev, pollv; u32 omg; u64 tag;
  const u32 lane16 = lane * 16;
  const u32 poll_off = WS_FLAG + (R2 >> 1) * 256 + (lane & 15) * 8;
  const bool need_h = (lane & 15) < 14;
  const u32 a2_off = WS_A2 + T2 * A2_T + kq * 1024 + lane16;
  const u32 h_off = WS_A2 + T * A2_T + (role * 4 + 2 * cg) * 1024 + lane16;

  auto read_w = [&](auto V, auto I) {
    constexpr int v = V, i = I, k = SA.kind[v], u = SA.idx[v], sl = SA.wl[v] % 20;
    if constexpr (k < 2 && SA.xl[v] >= 0 && (k == 0 || i == 0)) {
      constexpr int ks = k == 0 ? 2 * u + i : u, xs = SA.xl[v] % 20;
      const int wrap = xs == 19 ? (rg >> 1) * (-20 * SLOT) : 0;
      xf[ks] = *(LAS u32x4*)(sm + xa[ks % 4] + wrap + xs * SLOT);
    }
    if constexpr (k == 0) {
      wf[v & 1][i] = trfrag(sm + akv + sl * SLOT + ((2 * u + i) % 4) * 2048);
    } else if constexpr (k == 1) {
      const int wrap = sl == 19 ? blk * (-20 * SLOT) : 0;
      wf[v & 1][i] = trfrag(sm + aguw[i][sl / 8] + wrap + (sl % 8) * SLOT + (u % 4) * 2048);
    } else if constexpr (k == 2) {
      if constexpr (u == 56) {
#pragma unroll
        for (int e = 0; e < 3; ++e) { aw2w[i][e] = aw2[i] + e * 65536; pin(aw2w[i][e]); }
      }
      wf[v & 1][i] = trfrag(sm + aw2w[i][sl / 8] + (sl % 8) * SLOT);
    }
  };
  auto publish = [&](int cls) {
    if (w == 0) put_tag(tag, c.rws, lane == 0 ? role * 8 : OOB, WS_FLAG + cls * 4096 + mt * 256);
  };
  auto load_item = [&](auto IT) {
    constexpr int it = IT;
    if constexpr (it == IT_OMG) omg = ld32_sc1(r * 4, c.rws, WS_OMG + T2 * 128);
    else { constexpr int i = it < IT_OMG ? it : it - 1; af[i] = ld128_sc1(a2_off, c.rws, i * 4096); }
  };
  auto check = [&](auto CNT, int cls, bool need) {
    wait_vm<CNT>();
    pin(pollv);
    if (w == 0 && !match(pollv, need, tag)) spin(c.ws, poll_off + cls * 4096, need, tag);
  };

  sync_vm<SA.cnt0>();
  pin(c.pf);
  static_for<0, 4>([&](auto S) { read_w(IC<S / 2>{}, IC<S % 2>{}); });
  SCHED();
  auto iter = [&](auto V, auto ISV) {
    constexpr int v = V, b = v & 1, kd = SA.kind[v], ui = SA.idx[v], u = v - U0;
    if constexpr (u == U_CHK2) check(IC<SA.base[v] - SA.base[U0 + U_POLL2] - 1>{}, 2, need_h);
    if constexpr (u == U_CHK3) check(IC<SA.base[v] - SA.base[U0 + U_POLL3] - 1>{}, 2, !need_h);
    if constexpr (SA.bar[v]) sync_vm<SA.cnt[v]>();
    if constexpr (u == U_KVPUB) {
      pin(ev);
      tag = tag_of(ev);
      publish(0);
    }
    if constexpr (u == U_H1PUB) publish(1);
    if constexpr (u == U_H2PUB) publish(2);
    if constexpr (u == U_POLL1) pollv = ld64_sc1(w == 0 ? poll_off : OOB, c.rws, 4096);
    if constexpr (u == U_POLL2 || u == U_POLL3) pollv = ld64_sc1(w == 0 ? poll_off : OOB, c.rws, 8192);
    if constexpr (v == SA.vkvst) {
      ev = ld64_sc1(w == 0 ? WS_E : OOB, c.rws, 0);
#pragma unroll
      for (int s = 0; s < 2; ++s) {
        const u32 off = isv ? WS_V + T * KV_T + (s * 2 + hi) * 7168 + ((role - 7) * 64 + cg * 32 + r) * 16
                            : WS_K + T * KV_T + (role * 8 + cg * 4 + 2 * s) * 512 + lane16;
        st128(pk8(akvacc, s), c.rws, off, 0);
      }
    }
    if constexpr (v == SA.vg1 + EP1) st128(hp[0], c.rws, h_off, 0);
    if constexpr (v == SA.vg1 + 2 * EP1) st128(hp[1], c.rws, h_off, 1024);
    if constexpr (u == 56) {
#pragma unroll
      for (int t = 0; t < 2; ++t) {
#pragma unroll
        for (int j = 0; j < 8; j += 2) hp[t][j / 2] = pk(glu(g2[t][j], g2[t][8 + j]), glu(g2[t][j + 1], g2[t][9 + j]));
        st128(hp[t], c.rws, h_off, (56 + t) * 1024);
        SCHED();
      }
    }
    asm volatile("" ::: "memory");
    if constexpr (u == U_CHK1) {
      check(IC<SA.base[v] + SA.nst[v] - SA.base[U0 + U_POLL1] - 1>{}, 1, need_h);
      asm volatile("s_barrier" ::: "memory");
    }
    constexpr bool ld_first = u >= 56 && ld_iter(item_of(u < 56 ? 0 : u - 56)) == v;
    constexpr int nld = SA.ldlo[v + 1] - SA.ldlo[v], nd = SA.cum[v + 1] - SA.cum[v];
    if constexpr (ld_first) static_for<SA.ldlo[v], SA.ldlo[v + 1]>(load_item);
    if constexpr (u >= 56) {
      wait_vm<SA.base[v] + SA.nst[v] + (ld_first ? nld : 0) - SA.pld[item_of(u - 56)] - 1>();
      pin(af[u - 56]);
    }
    SCHED();
    static_for<0, 2>([&](auto I) {
      constexpr int i = I;
      if constexpr (kd == 0) { if constexpr (ISV) mfma_at(akvacc, xf[2 * ui + i], wf[b][i]); else mfma_at(akvacc, wf[b][i], xf[2 * ui + i]); }
      else if constexpr (ui < 28) mfma_at(g1[i], wf[b][i], xf[ui]);
      else if constexpr (ui < 56) mfma_at(g2[i], wf[b][i], xf[ui - 28]);
      else if constexpr (ui < 84) mfma_at(ah[i], wf[b][i], af[ui - 56]);
      else mfma_at(ao[i], wf[b][i], af[ui - 56]);
      SCHED();
      read_w(IC<v + 2>{}, I);
      if constexpr (v >= SA.vg1 && v < SA.vg1 + 2 * EP1) {
        constexpr int t = (v - SA.vg1) / EP1, per = 4 / EP1, k0 = ((v - SA.vg1) % EP1) * per;
#pragma unroll
        for (int k = k0 + (i * per + 1) / 2; k < k0 + ((i + 1) * per + 1) / 2; ++k) {
          u32 hv = pk(glu(g1[t][2 * k], g1[t][8 + 2 * k]), glu(g1[t][2 * k + 1], g1[t][9 + 2 * k]));
          pin(hv);
          hp[t][k] = hv;
        }
      }
      if constexpr (i == 0 && !ld_first) static_for<SA.ldlo[v], SA.ldlo[v + 1]>(load_item);
      if constexpr (i == 0 && nd == 2) issueA<SA.cum[v]>(c);
      if constexpr (i == 1 && nd >= 1) issueA<SA.cum[v + 1] - 1>(c);
      SCHED();
    });
  };
  if (isv) static_for<0, SA.nhead>([&](auto V) { iter(V, IC<1>{}); });
  else static_for<0, SA.nhead>([&](auto V) { iter(V, IC<0>{}); });
  static_for<SA.nhead, NV>([&](auto V) { iter(V, IC<0>{}); });
  pin(omg);
  const float om = __uint_as_float(omg);
  LAS char* red = sm + 4 * SLOT;
#pragma unroll
  for (int t = 0; t < 2; ++t)
#pragma unroll
    for (int a = 0; a < 4; ++a) {
      f32x4 v;
#pragma unroll
      for (int e = 0; e < 4; ++e) v[e] = ah[t][4 * a + e] * om + ao[t][4 * a + e];
      *(LAS f32x4*)(red + kq * 16384 + (2 * r + rt) * 256 + (((8 * t + 2 * a + hi) ^ (r & 15)) << 4)) = v;
    }
  asm volatile("s_waitcnt lgkmcnt(0)\n\ts_barrier" ::: "memory");
  {
    const int row = w * 8 + (lane >> 3), c8 = lane & 7, m = (row >> 1) & 15;
    f32x4 s0 = {}, s1 = {};
#pragma unroll
    for (int k = 0; k < 4; ++k) {
      s0 += *(LAS f32x4*)(red + k * 16384 + row * 256 + (((2 * c8) ^ m) << 4));
      s1 += *(LAS f32x4*)(red + k * 16384 + row * 256 + (((2 * c8 + 1) ^ m) << 4));
    }
    const u32x4 o = {pk(s0[0], s0[1]), pk(s0[2], s0[3]), pk(s1[0], s1[1]), pk(s1[2], s1[3])};
    __builtin_amdgcn_raw_buffer_store_b128(o, mk(out, 2048 * 896), (R2 * 64 + row) * 896 + n2 * 128 + c8 * 16, 0, SC1);
  }
}

constexpr int PRO_B = 12, GB_TOUCH = 18, GB_POLL = 25;
constexpr int cumB(int g) {
  int cc = PRO_B;
  for (int j = 0; j <= g; ++j) cc = imin(imin(cc + 2, 6 * ((j + 1) / 4) + 18), 42);
  return cc;
}
static_assert(cumB(GB_TOUCH - 1) == 42 && GB_POLL > 23, "both sit behind the last ring piece; the poll behind the last counted barrier");
template <int N> DI void issueB(const Ctx& c) {
  constexpr int kc = N / 6, j = N % 6;
  constexpr u32 dst = (N % 18) * SLOT;
  if constexpr (j < 2) dmam<dst, 128>(c.m0w, c.vx + j * 896, c.rx, c.kc[kc]);
  else dmam<dst, 64 * 2688>(c.m0w, c.vkv + (j - 2) * 128, c.rqkv, c.kc[kc]);
}

DI void type_b(Ctx& c, int w, int lane, int mt, int hf, const void* gw) {
  const int r = lane & 31, hi = lane >> 5, blk = (lane >> 4) & 1, q = (lane & 15) >> 2, p = lane & 3;
  const int sp = ((p & 1) << 1) | (p >> 1);
  const int rg = w & 3, cg = w >> 2, T = mt * 4 + rg;
  LAS char* sm = c.sm;
  if (w == 0) dma<18 * SLOT>(c.m0w, ((c.seq >> (4 * (lane >> 3))) & 7) * 128 + (lane & 7) * 16, mk(gw, 896), 0);
  static_for<2, PRO_B>([&](auto N) { issueB<N>(c); });
  if (w == 0) put_tag(0, c.rws, lane == 0 ? (14 + hf) * 8 : OOB, WS_FLAG + 8192 + mt * 256);
  const int R = 32 * (rg & 1) + r, xm = (R >> 1) & 7;
  int xa[4], aq[4];
#pragma unroll
  for (int s = 0; s < 4; ++s) xa[s] = (rg >> 1) * SLOT + R * 128 + (((2 * s + hi) ^ xm) << 4);
  const int h0 = 8 * hf + 4 * cg;
#pragma unroll
  for (int i = 0; i < 4; ++i) {
    const int col = 28 * (h0 + i) - 4 * (i & 1) - 192 * hf + 16 * blk + 4 * sp;
    aq[i] = (2 + (col >> 6)) * SLOT + (8 * hi + q) * 128 + (((col & 63) * 2) ^ ((q & 2) << 5));
  }
  const bool first = (mt == 0) && !(rg & 1);
  const int Tp = first ? T : ((rg & 1) ? T - 1 : T - 3);
  const u32 lane16 = lane * 16, lo16 = hi ? OOB : lane16;
  const u32 ob = WS_A2 + T * A2_T + (1792 + 28 * h0) * 64;
  const u32 omg_off = (hf == 0 && cg == 0 && hi == 0) ? r * 4 : OOB;
  const u32 poll_off = WS_FLAG + ((lane & 16) && mt > 0 ? mt - 1 : mt) * 256 + (lane & 15) * 8;
  const bool poll_need = (lane & 15) < 14;
  u32x4 xf[2], gf[2], wf[2][4];
  u32x4 kf[4][2], kp[2], vf[4][2], vp[4];
  f32x16 qa[4] = {};
  u32x2 ev, pollv;
  u32 tw;
  const int sr = (r & 19) | ((r & 4) << 1) | ((r & 8) >> 1);
  const u32 kp_off = (28 * (r >> 3) - 4 * ((r >> 3) & 1)) * 64 + (hi * 32 + 24 + (r & 7)) * 16;
  const u32 v_off = hi * 7168 + sr * 16;
  auto load_kv = [&](auto IT) {
    constexpr int it = IT;
    if constexpr (it < 2) kp[it] = ld128_sc1(kp_off, c.rws, WS_K + Tp * KV_T + 28 * h0 * 64 + it * 1024);
    else if constexpr (it < 10) {
      constexpr int i = (it - 2) / 2, k = it & 1;
      kf[i][k] = ld128_sc1(lane16, c.rws, WS_K + T * KV_T + (28 * (h0 + i) - 4 * (i & 1)) * 64 + k * 1024);
    } else {
      constexpr int i = (it - 10) / 3, k = (it - 10) % 3;
      const int wh = 28 * (h0 + i) - 4 * (i & 1);
      if constexpr (k == 0) vp[i] = ld128_sc1(v_off, c.rws, WS_V + Tp * KV_T + 14336 + wh * 16);
      else vf[i][k - 1] = ld128_sc1(v_off, c.rws, WS_V + T * KV_T + (k - 1) * 14336 + wh * 16);
    }
  };
  auto read_x = [&](auto G) {
    constexpr int g = G;
    if constexpr (g < 28) {
      xf[g & 1] = *(LAS u32x4*)(sm + xa[g % 4] + ((6 * (g / 4)) % 18) * SLOT);
      gf[g & 1] = *(LAS u32x4*)(sm + 18 * SLOT + g * 32 + hi * 16);
    }
  };
  auto read_w = [&](auto G, auto I) {
    constexpr int g = G;
    if constexpr (g < 28) wf[g & 1][I] = trfrag(sm + aq[I] + ((6 * (g / 4)) % 18) * SLOT + (g % 4) * 2048);
  };
  sync_vm<PRO_B - 6>();
  pin(c.pf);
  read_x(IC<0>{});
  static_for<0, 4>([&](auto I) { read_w(IC<0>{}, I); });
  SCHED();
  static_for<0, 28>([&](auto G) {
    constexpr int g = G, b = g & 1;
    if constexpr (g % 4 == 3 && g < 27) { asm volatile("s_waitcnt lgkmcnt(0)" ::: "memory"); sync_vm<cumB(g - 1) - 6 * (g / 4 + 2) + (g > GB_TOUCH)>(); }
    if constexpr (g == GB_POLL) { ev = ld64_sc1(w == 0 ? WS_E : OOB, c.rws, 0); pollv = ld64_sc1(w == 0 ? poll_off : OOB, c.rws, 0); }
    if constexpr (g == GB_TOUCH) {
      const int gi = (mt * 2 + hf) * 8 + w;
      tw = touch(((gi < 196 ? gi : gi - 196) * 64 + lane) * 128, gi < 196 ? c.rwd : c.rwo);
    }
    SCHED();
    static_for<0, 4>([&](auto I) {
      constexpr int i = I;
      mfma_at(qa[i], wf[b][i], xf[b]);
      SCHED();
      if constexpr (i == 0) read_x(IC<g + 1>{});
      read_w(IC<g + 1>{}, I);
      if constexpr (i < 2) {
        wf[b][2][2 * i] = r == 28 ? gf[b][2 * i] : wf[b][2][2 * i];
        wf[b][2][2 * i + 1] = r == 28 ? gf[b][2 * i + 1] : wf[b][2][2 * i + 1];
        pin(wf[b][2]);
      }
      if constexpr (i >= 2 && cumB(g - 1) + i - 2 < cumB(g)) issueB<cumB(g - 1) + i - 2>(c);
      SCHED();
    });
  });
  wait_vm<0>();
  pin(pollv); pin(ev); pin(tw);
  const u64 tag = tag_of(ev);
  if (w == 0 && !match(pollv, poll_need, tag)) spin(c.ws, poll_off, poll_need, tag);
  asm volatile("s_barrier" ::: "memory");
  static_for<0, 6>(load_kv);
  SCHED();
  const float z = __uint_as_float(__builtin_amdgcn_permlane32_swap(__float_as_uint(qa[2][12]), __float_as_uint(qa[2][12]), false, false)[1]);
  const float gate = sigmoidf(z);
  const u32 omg = __float_as_uint(sigmoidf(-z));
  int ab[4];
  u32 msk[5][2];
#pragma unroll
  for (int b = 0; b < 4; ++b) ab[b] = (r >> 3) - ((r & 7) < 4 * hi + b ? 1 : 0);
#pragma unroll
  for (int a = 0; a < 5; ++a)
#pragma unroll
    for (int e = 0; e < 2; ++e) msk[a][e] = (ab[2 * e] == a - 1 ? 0xFFFFu : 0u) | (ab[2 * e + 1] == a - 1 ? 0xFFFF0000u : 0u);
  const float csc = 0.18898223650461362f * 1.4426950408889634f;
  u32 p01[2], p23[2];
  u32x4 qf[4][2];
  static_for<0, 4>([&](auto I) {
    constexpr int i = I;
    qf[i][0] = pk8(qa[i], 0); qf[i][1] = pk8(qa[i], 1);
    if (i & 1) { qf[i][0][0] = hi ? qf[i][0][0] : 0u; qf[i][0][1] = hi ? qf[i][0][1] : 0u; }
    else       { qf[i][1][2] = hi ? 0u : qf[i][1][2]; qf[i][1][3] = hi ? 0u : qf[i][1][3]; }
    pin(qf[i][0]); pin(qf[i][1]);
  });
#pragma unroll
  for (int a = 0; a < 5; ++a) { pin(msk[a][0]); pin(msk[a][1]); }
  __builtin_amdgcn_raw_buffer_store_b32(omg, c.rws, omg_off, WS_OMG + T * 128, SC1);
  asm volatile("" ::: "memory");
  f32x16 so[2], sv[2], oa[2];
  auto scores = [&](auto J) {
    static_for<0, 2>([&](auto E) {
      constexpr int i = 2 * J + E;
      pin(kf[i][0]); pin(kf[i][1]);
      so[E] = mfma(kf[i][0], qf[i][0], f32x16{}); sv[E] = mfma(kp[0], qf[i][0], f32x16{});
      mfma_at(so[E], kf[i][1], qf[i][1]); mfma_at(sv[E], kp[1], qf[i][1]);
    });
  };
  auto softmax = [&](auto J) {
    static_for<0, 2>([&](auto E) {
      constexpr int i = 2 * J + E;
      float v[4];
#pragma unroll
      for (int b = 0; b < 4; ++b) {
        float t = first ? -1e30f : sv[E][4 * i + b];
#pragma unroll
        for (int a = 0; a < 4; ++a) t = ab[b] == a ? so[E][4 * a + b] : t;
        v[b] = t;
      }
      const float mx = xhalf_max(fmaxf(fmaxf(v[0], v[1]), fmaxf(v[2], v[3])));
#pragma unroll
      for (int b = 0; b < 4; ++b) v[b] = __builtin_amdgcn_exp2f((v[b] - mx) * csc);
      const float sc = gate * __builtin_amdgcn_rcpf(xhalf_sum((v[0] + v[1]) + (v[2] + v[3])));
      p01[E] = pk(v[0] * sc, v[1] * sc); p23[E] = pk(v[2] * sc, v[3] * sc);
    });
    pin(p01[0]); pin(p23[0]); pin(p01[1]); pin(p23[1]);
  };
  auto pv = [&](auto J) {
    static_for<0, 2>([&](auto E) {
      constexpr int i = 2 * J + E;
      pin(vp[i]); pin(vf[i][0]); pin(vf[i][1]);
      oa[E] = mfma(vp[i], u32x4{0u, 0u, p01[E] & msk[0][0], p23[E] & msk[0][1]}, f32x16{});
      oa[E] = mfma(vf[i][0], u32x4{p01[E] & msk[1][0], p23[E] & msk[1][1], p01[E] & msk[2][0], p23[E] & msk[2][1]}, oa[E]);
      mfma_at(oa[E], vf[i][1], u32x4{p01[E] & msk[3][0], p23[E] & msk[3][1], p01[E] & msk[4][0], p23[E] & msk[4][1]});
    });
  };
  auto store_o = [&](auto J) {
    u32x4 op[2][2] = {{pk8(oa[0], 0), pk8(oa[0], 1)}, {pk8(oa[1], 0), pk8(oa[1], 1)}};
#pragma unroll
    for (int e = 0; e < 2; ++e) { auto t = __builtin_amdgcn_permlane32_swap(op[0][1][e], op[1][0][e], false, false); op[0][1][e] = t[0]; op[1][0][e] = t[1]; }
    const u32 o2 = ob + J * 3584;
    st128(op[0][0], c.rws, lane16, o2);
    st128(op[0][1], c.rws, lo16, o2 + 1024);
    st128(op[1][0], c.rws, lane16, o2 + 1536);
    st128(op[1][1], c.rws, lane16, o2 + 2560);
  };
  static_for<10, 16>(load_kv);
  wait_vm<7>();
  pin(kp[0]); pin(kp[1]);
  scores(IC<0>{});
  static_for<6, 10>(load_kv);
  softmax(IC<0>{});
  static_for<16, 22>(load_kv);
  wait_vm<6>();
  pv(IC<0>{});
  scores(IC<1>{});
  store_o(IC<0>{});
  softmax(IC<1>{});
  wait_vm<4>();
  pv(IC<1>{});
  store_o(IC<1>{});
  sync_vm<0>();
  if (w == 0) put_tag(tag, c.rws, lane == 0 ? (14 + hf) * 8 : OOB, WS_FLAG + 8192 + mt * 256);
  if (mt == 0 && hf == 0 && w == 0) {
#pragma unroll 1
    for (int i = 0; i < 4; ++i) spin(c.ws, WS_FLAG + 8192 + (i * 4 + (lane >> 4)) * 256 + (lane & 15) * 8, true, tag);
    put_tag(tag, c.rws, lane == 0 ? 0 : OOB, WS_E);
  }
}

__global__ __launch_bounds__(512, 2) void fused_kernel(const void* __restrict__ x, const void* __restrict__ wg, const void* __restrict__ wu,
                                                       const void* __restrict__ wd, const void* __restrict__ wo, const void* __restrict__ wqkv,
                                                       const void* __restrict__ gw, void* __restrict__ out, void* __restrict__ ws) {
  __shared__ __attribute__((aligned(1024))) char smem[163840];
  const int lane = threadIdx.x & 63, w = __builtin_amdgcn_readfirstlane(threadIdx.x >> 6);
  Ctx c;
  {
    u64 pc;
    asm volatile("s_getpc_b64 %0" : "=s"(pc));
    c.pf = touch((w == 6 && lane < 7) ? ((blockIdx.x >> 3) * 7 + lane) * 128 : OOB, mk((const void*)(pc & ~255ull), CODE_BYTES));
  }
  asm volatile("" ::"s"(x), "s"(wg), "s"(wu), "s"(wd), "s"(wo), "s"(wqkv), "s"(gw), "s"(out), "s"(ws));
  const int b = blockIdx.x, xc = b & 7, ix = b >> 3;
  const int mq = xc >> 1, mt = mq * 4 + (ix & 3), rl = ix >> 2, rh = xc & 1;
  const bool isa = rl < 7;
  const int role = rh * 7 + rl;
  c.sm = (LAS char*)smem;
  c.m0w = (u32)(size_t)c.sm + w * 1024;
  c.rx = mk(x, 2048 * 896); c.rqkv = mk(wqkv, 448 * 2688); c.rwg = mk(wg, 448 * 3584); c.rwu = mk(wu, 448 * 3584);
  c.rwd = mk(wd, 1792 * 896); c.rwo = mk(wo, 448 * 896); c.rws = mk(ws, WS_END);
  c.ws = (const char*)ws;
  {
    constexpr u64 mod7 = 0x1065432106543210ull;
    c.seq = (u32)(mod7 >> (4 * ((mod7 >> (4 * (mq + 3 * rh + 2 * (ix & 3)))) & 7)));
#pragma unroll
    for (int j = 0; j < 7; ++j) { u32 k = (c.seq >> (4 * j)) & 7; asm volatile("" : "+s"(k)); c.kc[j] = k; }
  }
  const int r8 = lane >> 3, c8 = lane & 7, R = 8 * w + r8;
  c.vx = (mt * 128 + 2 * R) * 896 + ((c8 ^ ((R >> 1) & 7)) << 4);
  const int sw = (c8 ^ ((r8 & 2) << 1)) << 4;
  const int isai = (rl - 7) >> 31;
  c.vkv = R * 2688 + ((896 + role * 128) & isai) + ((rh * 384) & ~isai) + sw;
  static_for<0, 2>([&](auto N) { issueA<N>(c); });
  if (__builtin_expect(isa, 1)) {
    c.vg = R * 3584 + role * 128 + sw;
    c.vu = R * 3584 + role * 128 + (sw ^ 32);
    c.vw2 = R * 896 + rl * 128 + sw;
    type_a(c, w, lane, mt, role, xc * 4 + (ix & 3), rl, out);
  } else {
    type_b(c, w, lane, mt, rh, gw);
  }
}

extern "C" void kernel_launch(void* const* d_in, const int* in_sizes, int n_in, void* d_out, int out_size, void* d_ws, size_t ws_size,
                              hipStream_t stream) {
  fused_kernel<<<256, 512, 0, stream>>>(d_in[0], d_in[4], d_in[5], d_in[6], d_in[2], d_in[1], d_in[7], d_out, d_ws);
}
```

```cpp
#include <hip/hip_runtime.h>
#include <hip/hip_bf16.h>

typedef unsigned long long u64;
typedef unsigned u32;
typedef __bf16 bf16x8 __attribute__((ext_vector_type(8)));
typedef __bf16 bf16x2 __attribute__((ext_vector_type(2)));
typedef short s16x4 __attribute__((ext_vector_type(4)));
typedef u32 u32x4 __attribute__((ext_vector_type(4)));
typedef u32 u32x2 __attribute__((ext_vector_type(2)));
typedef float f32x16 __attribute__((ext_vector_type(16)));
typedef float f32x4 __attribute__((ext_vector_type(4)));
typedef __amdgpu_buffer_rsrc_t rsrc_t;
#define LAS __attribute__((address_space(3)))
#define GAS __attribute__((address_space(1)))
#define DI __device__ __forceinline__
#define SCHED() __builtin_amdgcn_sched_barrier(0)
template <int V> using IC = std::integral_constant<int, V>;

constexpr u32 WS_E = 0;
constexpr u32 WS_FLAG = 4096;
constexpr u32 WS_OMG = 32768;
constexpr u32 WS_K = 65536;
constexpr u32 KV_T = 28672;
constexpr u32 WS_V = WS_K + 64 * KV_T;
constexpr u32 WS_A2 = WS_V + 64 * KV_T;
constexpr u32 A2_T = 143360;
constexpr u32 WS_END = WS_A2 + 64 * A2_T;
constexpr u32 OOB = 0xFFFFFF00u;
constexpr u32 CODE_BYTES = 26624;
constexpr int SC1 = 16;
constexpr int SLOT = 8192;
constexpr int SPIN_CAP = 1 << 15;

DI rsrc_t mk(const void* p, u32 bytes) { return __builtin_amdgcn_make_buffer_rsrc((void*)p, (short)0, (int)bytes, 0x00020000); }
template <u32 DST> DI void dma(u32 m0w, u32 voff, rsrc_t rs, u32 soff) {
  asm volatile("s_add_u32 m0, %0, %4\n\ts_nop 0\n\tbuffer_load_dwordx4 %1, %2, %3 offen lds" ::"s"(m0w), "v"(voff), "s"(rs), "s"(soff), "n"(DST) : "memory", "scc");
}
template <u32 DST, u32 MUL> DI void dmam(u32 m0w, u32 voff, rsrc_t rs, u32 k) {
  u32 t;
  asm volatile("s_add_u32 m0, %1, %5\n\ts_mul_i32 %0, %4, %6\n\tbuffer_load_dwordx4 %2, %3, %0 offen lds" : "=&s"(t) : "s"(m0w), "v"(voff), "s"(rs), "s"(k), "n"(DST), "n"(MUL) : "memory", "scc");
}
DI u32x4 ld128_sc1(u32 voff, rsrc_t rs, u32 soff) {
  u32x4 v;
  asm volatile("buffer_load_dwordx4 %0, %1, %2, %3 offen sc1" : "=v"(v) : "v"(voff), "s"(rs), "s"(soff) : "memory");
  return v;
}
DI u32x2 ld64_sc1(u32 voff, rsrc_t rs, u32 soff) {
  u32x2 v;
  asm volatile("buffer_load_dwordx2 %0, %1, %2, %3 offen sc1" : "=v"(v) : "v"(voff), "s"(rs), "s"(soff) : "memory");
  return v;
}
DI u32 ld32_sc1(u32 voff, rsrc_t rs, u32 soff) {
  u32 v;
  asm volatile("buffer_load_dword %0, %1, %2, %3 offen sc1" : "=v"(v) : "v"(voff), "s"(rs), "s"(soff) : "memory");
  return v;
}
DI u32 touch(u32 voff, rsrc_t rs) {
  u32 v;
  asm volatile("buffer_load_dword %0, %1, %2, 0 offen" : "=v"(v) : "v"(voff), "s"(rs) : "memory");
  return v;
}
DI void st128(u32x4 v, rsrc_t rs, u32 voff, u32 soff) {
  asm volatile("buffer_store_dwordx4 %0, %1, %2, %3 offen sc1\n\ts_nop 1" ::"v"(v), "v"(voff), "s"(rs), "s"(soff) : "memory");
}
constexpr int clampvm(int n) { return n > 63 ? 63 : n; }
template <int N> DI void wait_vm() { static_assert(N >= 0, ""); asm volatile("s_waitcnt vmcnt(%0)" ::"n"(clampvm(N)) : "memory"); }
template <int N> DI void sync_vm() { static_assert(N >= 0, ""); asm volatile("s_waitcnt vmcnt(%0)\n\ts_barrier" ::"n"(clampvm(N)) : "memory"); }
template <class T> DI void pin(T& v) { asm volatile("" : "+v"(v)); }
template <int I, int N, class Fn> DI void static_for(Fn&& f) {
  if constexpr (I < N) { f(IC<I>{}); static_for<I + 1, N>(f); }
}

DI u32x4 trfrag(LAS char* p) {
  s16x4 lo = __builtin_amdgcn_ds_read_tr16_b64_v4i16((LAS s16x4*)p);
  s16x4 hi = __builtin_amdgcn_ds_read_tr16_b64_v4i16((LAS s16x4*)(p + 512));
  u32x2 a = __builtin_bit_cast(u32x2, lo), b = __builtin_bit_cast(u32x2, hi);
  return u32x4{a.x, a.y, b.x, b.y};
}
DI f32x16 mfma(u32x4 a, u32x4 b, f32x16 c) {
  return __builtin_amdgcn_mfma_f32_32x32x16_bf16(__builtin_bit_cast(bf16x8, a), __builtin_bit_cast(bf16x8, b), c, 0, 0, 0);
}
DI void mfma_at(f32x16& c, u32x4 a, u32x4 b) { c = mfma(a, b, c); asm volatile("" : "+v"(c)); }
DI u32 pk(float a, float b) { bf16x2 c; c[0] = (__bf16)a; c[1] = (__bf16)b; return __builtin_bit_cast(u32, c); }
DI u32x4 pk8(const f32x16& v, int s) { return u32x4{pk(v[8 * s], v[8 * s + 1]), pk(v[8 * s + 2], v[8 * s + 3]), pk(v[8 * s + 4], v[8 * s + 5]), pk(v[8 * s + 6], v[8 * s + 7])}; }
DI float sigmoidf(float z) { return __builtin_amdgcn_rcpf(1.f + __builtin_amdgcn_exp2f(z * -1.4426950408889634f)); }
DI float glu(float g, float u) { return u * sigmoidf(g); }
DI float xhalf_max(float v) { auto r = __builtin_amdgcn_permlane32_swap(__float_as_uint(v), __float_as_uint(v), false, false); return fmaxf(__uint_as_float(r[0]), __uint_as_float(r[1])); }
DI float xhalf_sum(float v) { auto r = __builtin_amdgcn_permlane32_swap(__float_as_uint(v), __float_as_uint(v), false, false); return __uint_as_float(r[0]) + __uint_as_float(r[1]); }

DI void spin(const char* ws, u32 off, bool need, u64 tag) {
  const GAS u64* p = (const GAS u64*)(ws + off);
#pragma unroll 1
  for (int i = 0; i < SPIN_CAP; ++i) {
    u64 v = __hip_atomic_load(p, __ATOMIC_RELAXED, __HIP_MEMORY_SCOPE_AGENT);
    if (__all(!need || v == tag)) break;
    __builtin_amdgcn_s_sleep(2);
  }
  asm volatile("" ::: "memory");
}
DI bool match(u32x2 v, bool need, u64 tag) { return __all(!need || (v.x == (u32)tag && v.y == (u32)(tag >> 32))); }
DI u64 tag_of(u32x2 ev) {
  u64 e = ((u64)__builtin_amdgcn_readfirstlane(ev.y) << 32) | (u32)__builtin_amdgcn_readfirstlane(ev.x);
  return e * 6364136223846793005ull + 1442695040888963407ull;
}
DI void put_tag(u64 tag, rsrc_t rs, u32 voff, u32 soff) { __builtin_amdgcn_raw_buffer_store_b64(u32x2{(u32)tag, (u32)(tag >> 32)}, rs, voff, soff, SC1); }

constexpr char HEAD[] = "KKGKKGKKGK";
constexpr int BARS[] = {0, 2, 6, 8, 10, 14, 16, 18, 22, 24, 28, 46, 50, 58, 62, 68, 76, 80, 86, 96};
constexpr int PRO_DMA = 9;
constexpr int HEAD_N = 26, HEAD_RATE = 20;
constexpr int EP1 = 2;
constexpr int U_KVPUB = 24, U_H1PUB = 40, U_H2PUB = 57, U_POLL1 = 52, U_CHK1 = 56, U_POLL2 = 64, U_CHK2 = 68, U_POLL3 = 70, U_CHK3 = 76;
constexpr int NV = 105, U0 = 14, IT_OMG = 28;
constexpr int imin(int a, int b) { return a < b ? a : b; }
constexpr int item_of(int i) { return i < IT_OMG ? i : i + 1; }
constexpr int ld_rel(int k) { return k < 6 ? 0 : 1 + (k - 6) / 2; }
constexpr int ld_iter(int it) { return U0 + (it < 14 ? U_CHK1 + ld_rel(it) : (it < 28 ? U_CHK2 + ld_rel(it - 14) : U_CHK3 + (it - 28) / 2)); }
constexpr int TM = 8, U_H2A = 56 - TM / 2 + 2;
constexpr bool tm_it(int u) { return u >= 56 - TM && u < 56; }
constexpr int g_tile(int u, int i) { return tm_it(u) ? (u - (56 - TM)) / (TM / 2) : i; }
constexpr int g_ks(int u, int i) { return tm_it(u) ? 56 - TM + 2 * ((u - (56 - TM)) % (TM / 2)) + i : u; }
constexpr int UE0 = 32, NE = (56 - TM - UE0) / 2;
struct Sched {
  int el[NV + 2] = {}, ej[NV + 2] = {};
  int nhead = 0, vkvst = 0, vg1 = 0;
  int xl[NV + 2] = {}, wl[NV + 2] = {};
  int kind[NV + 2] = {}, idx[NV + 2] = {}, lo[NV + 2] = {}, hi[NV + 2] = {};
  int pk[84] = {}, pi[84] = {}, pj[84] = {};
  bool bar[NV] = {};
  int cum[NV + 1] = {}, base[NV + 1] = {}, nst[NV] = {}, ldlo[NV + 1] = {}, cnt[NV] = {}, pdma[84] = {}, pld[36] = {}, freed[NV] = {};
  int cnt0 = 0; bool ok = true;
  constexpr Sched() {
    int v = 0, n = 0, k = 0, g = 0, ne = 0;
    for (int e = 0; e < NV + 2; ++e) el[e] = -1;
    int nx = 0;
    auto add = [&](int kd, int st, int nit, int nw) {
      const int first = n; int x = -1;
      if (st < 7 && st == nx) { x = n; for (int e = 0; e < 2; ++e) { pk[n] = 0; pi[n] = st; pj[n] = e; ++n; } ++nx; }
      for (int e = 0; e < nit; ++e) { kind[v] = kd; idx[v] = nit * st + e; lo[v] = first; hi[v] = n + nw - 1; xl[v] = x; wl[v] = n; ++v; }
      for (int e = 0; e < nw; ++e) { pk[n] = kd; pi[n] = st; pj[n] = kd ? e : 2; ++n; }
      if (kd == 1 && 4 * st >= UE0 && 4 * st < 56 - TM) {
        for (int e = 0; e < 4; ++e) { el[v - 4 + e] = hi[v - 4 + e] = n + e / 2; ej[v - 4 + e] = 2 * ne + e; }
        for (int e = 0; e < 2; ++e) { pk[n] = 2; pi[n] = ne++; ++n; }
      }
    };
    for (int i = 0; HEAD[i]; ++i) { if (HEAD[i] == 'K') { add(0, k++, 2, 1); nhead = v; } else { add(1, g++, 4, 2); if (g == 7) vg1 = v; } }
    if (k != 7 || g > 7) ok = false;
    vkvst = nhead + 1;
    while (g < 14) { add(1, g++, 4, 2); if (g == 7) vg1 = v; }
    if (ne != NE) ok = false;
    for (int i = 0; i < 35; ++i) { kind[v] = 2; idx[v] = 56 + i; lo[v] = wl[v] = n; if (i < NE) hi[v] = n - 1; else { hi[v] = n; pk[n] = 2; pi[n] = i; ++n; } ++v; }
    const int wa = wl[U0 + 56 - TM];
    for (int u = 56 - TM; u < 56; ++u) {
      wl[U0 + u] = wa + 2 * ((g_ks(u, 0) - (56 - TM)) / 4);
      lo[U0 + u] = g_tile(u, 0) == 0 ? wa : wl[U0 + u];
      hi[U0 + u] = g_tile(u, 0) == 0 ? wl[U0 + u] + 1 : wa + TM / 2 - 1;
    }
    kind[NV] = kind[NV + 1] = 3; hi[NV] = hi[NV + 1] = 83;
    for (int e = 0; e + 1 < NV; ++e) if (lo[e] > lo[e + 1] || hi[e] > hi[e + 1]) ok = false;
    for (int e : BARS) bar[e] = true;
    bar[U0 + U_KVPUB] = bar[U0 + U_H1PUB] = bar[U0 + U_H2PUB] = bar[U0 + U_CHK2] = bar[U0 + U_CHK3] = true;
    if (U0 + U_KVPUB <= vkvst || U0 + U_H1PUB <= vg1 + 2 * EP1 || bar[U0 + U_CHK1]) ok = false;
    int fr = 0, cc = PRO_DMA, cr = 0, it = 0;
    cum[0] = cc; base[0] = PRO_DMA + 1;
    for (n = 0; n < PRO_DMA; ++n) pdma[n] = n;
    for (v = 0; v < NV; ++v) {
      const int u = v - U0;
      if (bar[v] || u == U_CHK1) fr = lo[v];
      freed[v] = fr;
      nst[v] = 3 * (v == vkvst) + (v == vg1 + EP1) + (v == vg1 + 2 * EP1) + (u == U_H2A) + (u == 56) + (u == U_POLL1) + (u == U_POLL2) + (u == U_POLL3);
      ldlo[v] = it;
      while (it < 36 && ld_iter(it) == v) { pld[it] = base[v] + nst[v] + (it - ldlo[v]); ++it; }
      cr += v < HEAD_N ? HEAD_RATE : 16;
      const int t = imin(imin(cc + imin(cr / 16, 2), fr + 20), 84);
      cr -= 16 * (t - cc); if (cr > 16) cr = 16;
      for (n = cc; n < t; ++n) pdma[n] = base[v] + nst[v] + (it - ldlo[v]) + (n - cc);
      base[v + 1] = base[v] + nst[v] + (it - ldlo[v]) + (t - cc);
      cc = t; cum[v + 1] = cc;
    }
    ldlo[NV] = it;
    if (it != 36 || cc != 84) ok = false;
    for (v = -1; v < NV; ++v) {
      if (v >= 0 && !bar[v]) continue;
      int nb = v + 1; while (nb < NV && !bar[nb]) ++nb;
      const int need = hi[nb + 1], at = v < 0 ? 0 : v;
      if (need >= cum[at]) ok = false;
      int c = base[at] - pdma[need] - 1;
      const int u = v - U0;
      if (u == U_KVPUB) c = imin(c, base[v] - (base[vkvst] + nst[vkvst]));
      if (u == U_H1PUB) c = imin(c, base[v] - (base[vg1 + 2 * EP1] + nst[vg1 + 2 * EP1]));
      if (u == U_H2PUB) c = imin(c, base[v] - (base[U0 + 56] + nst[U0 + 56]));
      if (v < 0) cnt0 = c; else cnt[v] = c;
    }
    for (int i = 1; i < 35; ++i) if (ld_iter(item_of(i)) >= U0 + 56 + i) ok = false;
  }
};
constexpr Sched SA{};
static_assert(SA.ok, "schedule: a piece or operand is used before it is requested, or HEAD is malformed");
static_assert(U_CHK1 == 56 && U_POLL1 < U_CHK1 && U_CHK1 < U_POLL2 && U_POLL2 < U_CHK2 && U_CHK2 < U_POLL3 && U_POLL3 < U_CHK3, "");
static_assert(U_KVPUB < U_H1PUB && U_H1PUB < U_CHK1 && U_H2PUB > 56 && U_H2PUB < U_CHK2, "a workgroup publishes a class before it waits for that class");
static_assert(SA.freed[NV - 1] >= 72, "the reduction scratch (slots 4..11 = pieces 64..71) must be free at the end");

struct Ctx {
  LAS char* sm; u32 m0w;
  rsrc_t rx, rqkv, rwg, rwu, rwd, rwo, rws;
  const char* ws;
  u32 pf;
  u32 kc[7], seq, seq2;
  u32 vx, vkv, vg, vu, vw2;
};

template <int I> DI u32 blk2(const Ctx& c) {
  return I < 28 ? (I / 14) * 14 + 2 * ((c.seq2 >> (4 * ((I % 14) / 2))) & 7) + (I & 1) : 28 + ((c.seq2 >> (4 * (I < 28 ? 0 : I - 28))) & 7);
}
template <int N> DI void issueA(const Ctx& c) {
  constexpr u32 dst = (N % 20) * SLOT;
  constexpr int k = SA.pk[N], st = SA.pi[N], j = SA.pj[N];
  if constexpr (k == 0) {
    if constexpr (j < 2) dmam<dst, 128>(c.m0w, c.vx + j * 896, c.rx, c.kc[st]);
    else dmam<dst, 64 * 2688>(c.m0w, c.vkv, c.rqkv, c.kc[st]);
  } else if constexpr (k == 1) {
    if constexpr (j == 0) dmam<dst, 64 * 3584>(c.m0w, c.vg + (st / 7) * 1792, c.rwg, c.kc[st % 7]);
    else dmam<dst, 64 * 3584>(c.m0w, c.vu + (st / 7) * 1792, c.rwu, c.kc[st % 7]);
  } else if constexpr (st < 28) dmam<dst, 64 * 896>(c.m0w, c.vw2, c.rwd, blk2<st>(c));
  else dmam<dst, 64 * 896>(c.m0w, c.vw2, c.rwo, blk2<st>(c) - 28);
}

DI void type_a(Ctx& c, int w, int lane, int mt, int role, int R2, int n2, void* out) {
  const int r = lane & 31, hi = lane >> 5, blk = (lane >> 4) & 1, q = (lane & 15) >> 2, p = lane & 3;
  const int sp = ((p & 1) << 1) | (p >> 1);
  const int rg = w & 3, cg = w >> 2, T = mt * 4 + rg;
  const bool isv = role >= 7;
  LAS char* sm = c.sm;
  static_for<2, PRO_DMA>([&](auto N) { issueA<N>(c); });
  put_tag(0, c.rws, (w == 0 && lane < 3) ? lane * 4096 + role * 8 : OOB, WS_FLAG + mt * 256);
  asm volatile("" ::: "memory");
  const int R = 32 * (rg & 1) + r, xm = (R >> 1) & 7;
  int xa[4];
#pragma unroll
  for (int s = 0; s < 4; ++s) xa[s] = (rg >> 1) * SLOT + R * 128 + (((2 * s + hi) ^ xm) << 4);
  const int rowb = (8 * hi + q) * 128, q2 = q & 2;
  const int akv = rowb + (((2 * cg + blk) ^ q2) << 5) + (isv ? p : sp) * 8;
  int akvw[3];
#pragma unroll
  for (int k = 0; k < 3; ++k) { akvw[k] = akv + k * 65536; pin(akvw[k]); }
  int agu[2];
#pragma unroll
  for (int i = 0; i < 2; ++i) agu[i] = blk * SLOT + rowb + (((2 * cg + i) ^ q2 ^ blk) << 5) + sp * 8;
  int aguw[2][3];
#pragma unroll
  for (int i = 0; i < 2; ++i)
#pragma unroll
    for (int k = 0; k < 3; ++k) { aguw[i][k] = agu[i] + k * 65536; pin(aguw[i][k]); }
  const int rt = w & 1, kq = w >> 1;
  int aw2[2], aw2w[2][3];
#pragma unroll
  for (int i = 0; i < 2; ++i) aw2[i] = kq * 2048 + rowb + (((2 * i + blk) ^ q2) << 5) + p * 8;
  const int T2 = (R2 >> 1) * 4 + 2 * rt + (R2 & 1);

  u32x4 xf[28], wf[2][2], af[35], hp[2], wr[2 * NE];
  f32x16 akvacc = {}, g1[2] = {}, g2[2] = {}, ah[2] = {}, ao[2] = {};
  u32x2 ev, pollv; u32 omg; u64 tag;
  const u32 lane16 = lane * 16;
  const u32 poll_off = WS_FLAG + (R2 >> 1) * 256 + (lane & 15) * 8;
  const bool need_h = (lane & 15) < 14;
  const u32 a2_off = WS_A2 + T2 * A2_T + kq * 1024 + lane16;
  const int isvi = (6 - role) >> 31;
  u32 kv_off = ((WS_V + hi * 7168 + ((role - 7) * 64 + cg * 32 + r) * 16) & isvi) + ((WS_K + (role * 8 + cg * 4) * 512 + lane16) & ~isvi) + T * KV_T;
  pin(kv_off);
  const u32 kv_step = 1024 + (13312 & isvi);
  const u32 h_off = WS_A2 + T * A2_T + (role * 4 + 2 * cg) * 1024 + lane16;

  auto read_w = [&](auto V, auto I) {
    constexpr int v = V, i = I, k = SA.kind[v], u = SA.idx[v], sl = SA.wl[v] % 20;
    if constexpr (k < 2 && SA.xl[v] >= 0 && (k == 0 || i == 0)) {
      constexpr int ks = k == 0 ? 2 * u + i : u, xs = SA.xl[v] % 20;
      const int wrap = xs == 19 ? (rg >> 1) * (-20 * SLOT) : 0;
      xf[ks] = *(LAS u32x4*)(sm + xa[ks % 4] + wrap + xs * SLOT);
    }
    if constexpr (k == 0) {
      wf[v & 1][i] = trfrag(sm + akvw[sl / 8] + (sl % 8) * SLOT + ((2 * u + i) % 4) * 2048);
    } else if constexpr (k == 1) {
      const int wrap = sl == 19 ? blk * (-20 * SLOT) : 0;
      wf[v & 1][i] = trfrag(sm + aguw[g_tile(u, i)][sl / 8] + wrap + (sl % 8) * SLOT + (g_ks(u, i) % 4) * 2048);
      if constexpr (u == UE0 - 2) {
#pragma unroll
        for (int e = 0; e < 3; ++e) { aw2w[i][e] = aw2[i] + e * 65536; pin(aw2w[i][e]); }
      }
      if constexpr (i == 0 && SA.el[v] >= 0) { constexpr int es = SA.el[v] % 20, j = SA.ej[v]; wr[j] = trfrag(sm + aw2w[j & 1][es / 8] + (es % 8) * SLOT); }
    } else if constexpr (k == 2 && u - 56 >= NE) {
      wf[v & 1][i] = trfrag(sm + aw2w[i][sl / 8] + (sl % 8) * SLOT);
    }
  };
  auto publish = [&](int cls) {
    if (w == 0) put_tag(tag, c.rws, lane == 0 ? role * 8 : OOB, WS_FLAG + cls * 4096 + mt * 256);
  };
  auto load_item = [&](auto IT) {
    constexpr int it = IT;
    if constexpr (it == IT_OMG) omg = ld32_sc1(r * 4, c.rws, WS_OMG + T2 * 128);
    else { constexpr int i = it < IT_OMG ? it : it - 1; af[i] = ld128_sc1(a2_off, c.rws, blk2<i>(c) * 4096); }
  };
  auto check = [&](auto CNT, int cls, bool need) {
    wait_vm<CNT>();
    pin(pollv);
    if (w == 0 && !match(pollv, need, tag)) spin(c.ws, poll_off + cls * 4096, need, tag);
  };

  sync_vm<SA.cnt0>();
  pin(c.pf);
  static_for<0, 4>([&](auto S) { read_w(IC<S / 2>{}, IC<S % 2>{}); });
  SCHED();
  auto h_pair = [&](const f32x16& gu, int k) { u32 hv = pk(glu(gu[2 * k], gu[8 + 2 * k]), glu(gu[2 * k + 1], gu[9 + 2 * k])); pin(hv); return hv; };
  auto iter = [&](auto V, auto ISV) {
    constexpr int v = V, b = v & 1, kd = SA.kind[v], ui = SA.idx[v], u = v - U0;
    constexpr bool nbar = v + 1 < NV && SA.bar[v + 1 < NV ? v + 1 : v];
    constexpr int nld = SA.ldlo[v + 1] - SA.ldlo[v], nd = SA.cum[v + 1] - SA.cum[v];
    if constexpr (SA.bar[v]) sync_vm<SA.cnt[v]>();
    if constexpr (u == 56) {
#pragma unroll
      for (int j = 0; j < 8; j += 2) hp[1][j / 2] = pk(glu(g2[1][j], g2[1][8 + j]), glu(g2[1][j + 1], g2[1][9 + j]));
      st128(hp[1], c.rws, h_off, 57 * 1024);
      SCHED();
      check(IC<SA.base[v] + SA.nst[v] - SA.base[U0 + U_POLL1] - 1>{}, 1, need_h);
      asm volatile("s_barrier" ::: "memory");
      static_for<SA.ldlo[v], SA.ldlo[v + 1]>(load_item);
    }
    if constexpr (u >= 56) {
      wait_vm<SA.base[v] + (u == 56 ? SA.nst[v] + nld : 0) - SA.pld[item_of(u - 56)] - 1>();
      pin(af[u - 56]);
    }
    SCHED();
    static_for<0, 2>([&](auto I) {
      constexpr int i = I;
      if constexpr (kd == 0) { if constexpr (ISV) mfma_at(akvacc, xf[2 * ui + i], wf[b][i]); else mfma_at(akvacc, wf[b][i], xf[2 * ui + i]); }
      else if constexpr (ui < 28) mfma_at(g1[i], wf[b][i], xf[ui]);
      else if constexpr (ui < 56) mfma_at(g2[g_tile(ui, i)], wf[b][i], xf[g_ks(ui, i) - 28]);
      else if constexpr (ui - 56 < NE) mfma_at(ah[i], wr[2 * (ui - 56) + i], af[ui - 56]);
      else if constexpr (ui < 84) mfma_at(ah[i], wf[b][i], af[ui - 56]);
      else mfma_at(ao[i], wf[b][i], af[ui - 56]);
      if constexpr (i == 0 && SA.el[v] >= 0) pin(wr[SA.ej[v] + i]);
      SCHED();
      if constexpr (i == 0 && v > 0 && SA.bar[v]) read_w(IC<v + 1>{}, IC<1>{});
      if constexpr (i == 0 || !nbar) read_w(IC<v + 2>{}, I);
      if constexpr (i == 0) {
        if constexpr (u == U_KVPUB) { pin(ev); tag = tag_of(ev); publish(0); }
        if constexpr (u == U_H1PUB) publish(1);
        if constexpr (u == U_H2PUB) publish(2);
        if constexpr (v == SA.vkvst) ev = ld64_sc1(w == 0 ? WS_E : OOB, c.rws, 0);
        if constexpr (u == U_POLL1) pollv = ld64_sc1(w == 0 ? poll_off : OOB, c.rws, 4096);
        if constexpr (u == U_POLL2 || u == U_POLL3) pollv = ld64_sc1(w == 0 ? poll_off : OOB, c.rws, 8192);
        if constexpr (v == SA.vkvst) { st128(pk8(akvacc, 0), c.rws, kv_off, 0); st128(pk8(akvacc, 1), c.rws, kv_off, kv_step); }
        if constexpr (v == SA.vg1 + EP1) st128(hp[0], c.rws, h_off, 0);
        if constexpr (v == SA.vg1 + 2 * EP1) st128(hp[1], c.rws, h_off, 1024);
        if constexpr (u == U_H2A) st128(hp[0], c.rws, h_off, 56 * 1024);
        if constexpr (u != 56) static_for<SA.ldlo[v], SA.ldlo[v + 1]>(load_item);
      }
      if constexpr (v >= SA.vg1 && v < SA.vg1 + 2 * EP1) {
        constexpr int t = (v - SA.vg1) / EP1, per = 4 / EP1, k0 = ((v - SA.vg1) % EP1) * per;
        constexpr int ka = nbar ? (i ? per : 0) : (i * per + 1) / 2, kb = nbar ? per : ((i + 1) * per + 1) / 2;
#pragma unroll
        for (int k = k0 + ka; k < k0 + kb; ++k) hp[t][k] = h_pair(g1[t], k);
      }
      if constexpr (u >= U_H2A - 2 && u < U_H2A) hp[0][2 * (u - (U_H2A - 2)) + i] = h_pair(g2[0], 2 * (u - (U_H2A - 2)) + i);
      if constexpr (i == 0 && nd == 2) issueA<SA.cum[v]>(c);
      if constexpr (i == (nbar ? 0 : 1) && nd >= 1) issueA<SA.cum[v + 1] - 1>(c);
      if constexpr (i == 0 && u + 1 == U_CHK2) check(IC<SA.base[v + 1] - SA.base[U0 + U_POLL2] - 1 + i>{}, 2, need_h);
      if constexpr (i == 0 && u + 1 == U_CHK3) check(IC<SA.base[v + 1] - SA.base[U0 + U_POLL3] - 1 + i>{}, 2, !need_h);
      SCHED();
    });
  };
  if (isv) static_for<0, SA.nhead>([&](auto V) { iter(V, IC<1>{}); });
  else static_for<0, SA.nhead>([&](auto V) { iter(V, IC<0>{}); });
  static_for<SA.nhead, NV>([&](auto V) { iter(V, IC<0>{}); });
  pin(omg);
  const float om = __uint_as_float(omg);
  LAS char* red = sm + 4 * SLOT;
#pragma unroll
  for (int t = 0; t < 2; ++t)
#pragma unroll
    for (int a = 0; a < 4; ++a) {
      f32x4 v;
#pragma unroll
      for (int e = 0; e < 4; ++e) v[e] = ah[t][4 * a + e] * om + ao[t][4 * a + e];
      *(LAS f32x4*)(red + kq * 16384 + (2 * r + rt) * 256 + (((8 * t + 2 * a + hi) ^ (r & 15)) << 4)) = v;
    }
  asm volatile("s_waitcnt lgkmcnt(0)\n\ts_barrier" ::: "memory");
  {
    const int row = w * 8 + (lane >> 3), c8 = lane & 7, m = (row >> 1) & 15;
    f32x4 s0 = {}, s1 = {};
#pragma unroll
    for (int k = 0; k < 4; ++k) {
      s0 += *(LAS f32x4*)(red + k * 16384 + row * 256 + (((2 * c8) ^ m) << 4));
      s1 += *(LAS f32x4*)(red + k * 16384 + row * 256 + (((2 * c8 + 1) ^ m) << 4));
    }
    const u32x4 o = {pk(s0[0], s0[1]), pk(s0[2], s0[3]), pk(s1[0], s1[1]), pk(s1[2], s1[3])};
    __builtin_amdgcn_raw_buffer_store_b128(o, mk(out, 2048 * 896), (R2 * 64 + row) * 896 + n2 * 128 + c8 * 16, 0, SC1);
  }
}

constexpr int PRO_B = 12, GB_TOUCH = 18, GB_POLL = 25;
constexpr int cumB(int g) {
  int cc = PRO_B;
  for (int j = 0; j <= g; ++j) cc = imin(imin(cc + 2, 6 * ((j + 1) / 4) + 18), 42);
  return cc;
}
static_assert(cumB(GB_TOUCH - 1) == 42 && GB_POLL > 23, "both sit behind the last ring piece; the poll behind the last counted barrier");
template <int N> DI void issueB(const Ctx& c) {
  constexpr int kc = N / 6, j = N % 6;
  constexpr u32 dst = (N % 18) * SLOT;
  if constexpr (j < 2) dmam<dst, 128>(c.m0w, c.vx + j * 896, c.rx, c.kc[kc]);
  else dmam<dst, 64 * 2688>(c.m0w, c.vkv + (j - 2) * 128, c.rqkv, c.kc[kc]);
}

DI void type_b(Ctx& c, int w, int lane, int mt, int hf, const void* gw) {
  const int r = lane & 31, hi = lane >> 5, blk = (lane >> 4) & 1, q = (lane & 15) >> 2, p = lane & 3;
  const int sp = ((p & 1) << 1) | (p >> 1);
  const int rg = w & 3, cg = w >> 2, T = mt * 4 + rg;
  LAS char* sm = c.sm;
  if (w == 0) dma<18 * SLOT>(c.m0w, ((c.seq >> (4 * (lane >> 3))) & 7) * 128 + (lane & 7) * 16, mk(gw, 896), 0);
  static_for<2, PRO_B>([&](auto N) { issueB<N>(c); });
  if (w == 0) put_tag(0, c.rws, lane == 0 ? (14 + hf) * 8 : OOB, WS_FLAG + 8192 + mt * 256);
  const int R = 32 * (rg & 1) + r, xm = (R >> 1) & 7;
  int xa[4], aq[4];
#pragma unroll
  for (int s = 0; s < 4; ++s) xa[s] = (rg >> 1) * SLOT + R * 128 + (((2 * s + hi) ^ xm) << 4);
  const int h0 = 8 * hf + 4 * cg;
#pragma unroll
  for (int i = 0; i < 4; ++i) {
    const int col = 28 * (h0 + i) - 4 * (i & 1) - 192 * hf + 16 * blk + 4 * sp;
    aq[i] = (2 + (col >> 6)) * SLOT + (8 * hi + q) * 128 + (((col & 63) * 2) ^ ((q & 2) << 5));
  }
  const bool first = (mt == 0) && !(rg & 1);
  const int Tp = first ? T : ((rg & 1) ? T - 1 : T - 3);
  const u32 lane16 = lane * 16, lo16 = hi ? OOB : lane16;
  const u32 ob = WS_A2 + T * A2_T + (1792 + 28 * h0) * 64;
  const u32 omg_off = (hf == 0 && cg == 0 && hi == 0) ? r * 4 : OOB;
  const u32 poll_off = WS_FLAG + ((lane & 16) && mt > 0 ? mt - 1 : mt) * 256 + (lane & 15) * 8;
  const bool poll_need = (lane & 15) < 14;
  u32x4 xf[2], gf[2], wf[2][4];
  u32x4 kf[4][2], kp[2], vf[4][2], vp[4];
  f32x16 qa[4] = {};
  u32x2 ev, pollv;
  u32 tw;
  const int sr = (r & 19) | ((r & 4) << 1) | ((r & 8) >> 1);
  const u32 kp_off = (28 * (r >> 3) - 4 * ((r >> 3) & 1)) * 64 + (hi * 32 + 24 + (r & 7)) * 16;
  const u32 v_off = hi * 7168 + sr * 16;
  auto load_kv = [&](auto IT) {
    constexpr int it = IT;
    if constexpr (it < 2) kp[it] = ld128_sc1(kp_off, c.rws, WS_K + Tp * KV_T + 28 * h0 * 64 + it * 1024);
    else if constexpr (it < 10) {
      constexpr int i = (it - 2) / 2, k = it & 1;
      kf[i][k] = ld128_sc1(lane16, c.rws, WS_K + T * KV_T + (28 * (h0 + i) - 4 * (i & 1)) * 64 + k * 1024);
    } else {
      constexpr int i = (it - 10) / 3, k = (it - 10) % 3;
      const int wh = 28 * (h0 + i) - 4 * (i & 1);
      if constexpr (k == 0) vp[i] = ld128_sc1(v_off, c.rws, WS_V + Tp * KV_T + 14336 + wh * 16);
      else vf[i][k - 1] = ld128_sc1(v_off, c.rws, WS_V + T * KV_T + (k - 1) * 14336 + wh * 16);
    }
  };
  auto read_x = [&](auto G) {
    constexpr int g = G;
    if constexpr (g < 28) {
      xf[g & 1] = *(LAS u32x4*)(sm + xa[g % 4] + ((6 * (g / 4)) % 18) * SLOT);
      gf[g & 1] = *(LAS u32x4*)(sm + 18 * SLOT + g * 32 + hi * 16);
    }
  };
  auto read_w = [&](auto G, auto I) {
    constexpr int g = G;
    if constexpr (g < 28) wf[g & 1][I] = trfrag(sm + aq[I] + ((6 * (g / 4)) % 18) * SLOT + (g % 4) * 2048);
  };
  sync_vm<PRO_B - 6>();
  pin(c.pf);
  read_x(IC<0>{});
  static_for<0, 4>([&](auto I) { read_w(IC<0>{}, I); });
  SCHED();
  static_for<0, 28>([&](auto G) {
    constexpr int g = G, b = g & 1;
    if constexpr (g % 4 == 3 && g < 27) { asm volatile("s_waitcnt lgkmcnt(0)" ::: "memory"); sync_vm<cumB(g - 1) - 6 * (g / 4 + 2) + (g > GB_TOUCH)>(); }
    if constexpr (g == GB_POLL) { ev = ld64_sc1(w == 0 ? WS_E : OOB, c.rws, 0); pollv = ld64_sc1(w == 0 ? poll_off : OOB, c.rws, 0); }
    if constexpr (g == GB_TOUCH) {
      const int gi = (mt * 2 + hf) * 8 + w;
      tw = touch(((gi < 196 ? gi : gi - 196) * 64 + lane) * 128, gi < 196 ? c.rwd : c.rwo);
    }
    SCHED();
    static_for<0, 4>([&](auto I) {
      constexpr int i = I;
      mfma_at(qa[i], wf[b][i], xf[b]);
      SCHED();
      if constexpr (i == 0) read_x(IC<g + 1>{});
      read_w(IC<g + 1>{}, I);
      if constexpr (i < 2) {
        wf[b][2][2 * i] = r == 28 ? gf[b][2 * i] : wf[b][2][2 * i];
        wf[b][2][2 * i + 1] = r == 28 ? gf[b][2 * i + 1] : wf[b][2][2 * i + 1];
        pin(wf[b][2]);
      }
      if constexpr (i >= 2 && cumB(g - 1) + i - 2 < cumB(g)) issueB<cumB(g - 1) + i - 2>(c);
      SCHED();
    });
  });
  wait_vm<0>();
  pin(pollv); pin(ev); pin(tw);
  const u64 tag = tag_of(ev);
  if (w == 0 && !match(pollv, poll_need, tag)) spin(c.ws, poll_off, poll_need, tag);
  asm volatile("s_barrier" ::: "memory");
  static_for<0, 6>(load_kv);
  SCHED();
  const float z = __uint_as_float(__builtin_amdgcn_permlane32_swap(__float_as_uint(qa[2][12]), __float_as_uint(qa[2][12]), false, false)[1]);
  const float gate = sigmoidf(z);
  const u32 omg = __float_as_uint(sigmoidf(-z));
  int ab[4];
  u32 msk[5][2];
#pragma unroll
  for (int b = 0; b < 4; ++b) ab[b] = (r >> 3) - ((r & 7) < 4 * hi + b ? 1 : 0);
#pragma unroll
  for (int a = 0; a < 5; ++a)
#pragma unroll
    for (int e = 0; e < 2; ++e) msk[a][e] = (ab[2 * e] == a - 1 ? 0xFFFFu : 0u) | (ab[2 * e + 1] == a - 1 ? 0xFFFF0000u : 0u);
  const float csc = 0.18898223650461362f * 1.4426950408889634f;
  u32 p01[2], p23[2];
  u32x4 qf[4][2];
  static_for<0, 4>([&](auto I) {
    constexpr int i = I;
    qf[i][0] = pk8(qa[i], 0); qf[i][1] = pk8(qa[i], 1);
    if (i & 1) { qf[i][0][0] = hi ? qf[i][0][0] : 0u; qf[i][0][1] = hi ? qf[i][0][1] : 0u; }
    else       { qf[i][1][2] = hi ? 0u : qf[i][1][2]; qf[i][1][3] = hi ? 0u : qf[i][1][3]; }
    pin(qf[i][0]); pin(qf[i][1]);
  });
#pragma unroll
  for (int a = 0; a < 5; ++a) { pin(msk[a][0]); pin(msk[a][1]); }
  __builtin_amdgcn_raw_buffer_store_b32(omg, c.rws, omg_off, WS_OMG + T * 128, SC1);
  asm volatile("" ::: "memory");
  f32x16 so[2], sv[2], oa[2];
  auto scores = [&](auto J) {
    static_for<0, 2>([&](auto E) {
      constexpr int i = 2 * J + E;
      pin(kf[i][0]); pin(kf[i][1]);
      so[E] = mfma(kf[i][0], qf[i][0], f32x16{}); sv[E] = mfma(kp[0], qf[i][0], f32x16{});
      mfma_at(so[E], kf[i][1], qf[i][1]); mfma_at(sv[E], kp[1], qf[i][1]);
    });
  };
  auto softmax = [&](auto J) {
    static_for<0, 2>([&](auto E) {
      constexpr int i = 2 * J + E;
      float v[4];
#pragma unroll
      for (int b = 0; b < 4; ++b) {
        float t = first ? -1e30f : sv[E][4 * i + b];
#pragma unroll
        for (int a = 0; a < 4; ++a) t = ab[b] == a ? so[E][4 * a + b] : t;
        v[b] = t;
      }
      const float mx = xhalf_max(fmaxf(fmaxf(v[0], v[1]), fmaxf(v[2], v[3])));
#pragma unroll
      for (int b = 0; b < 4; ++b) v[b] = __builtin_amdgcn_exp2f((v[b] - mx) * csc);
      const float sc = gate * __builtin_amdgcn_rcpf(xhalf_sum((v[0] + v[1]) + (v[2] + v[3])));
      p01[E] = pk(v[0] * sc, v[1] * sc); p23[E] = pk(v[2] * sc, v[3] * sc);
    });
    pin(p01[0]); pin(p23[0]); pin(p01[1]); pin(p23[1]);
  };
  auto pv = [&](auto J) {
    static_for<0, 2>([&](auto E) {
      constexpr int i = 2 * J + E;
      pin(vp[i]); pin(vf[i][0]); pin(vf[i][1]);
      oa[E] = mfma(vp[i], u32x4{0u, 0u, p01[E] & msk[0][0], p23[E] & msk[0][1]}, f32x16{});
      oa[E] = mfma(vf[i][0], u32x4{p01[E] & msk[1][0], p23[E] & msk[1][1], p01[E] & msk[2][0], p23[E] & msk[2][1]}, oa[E]);
      mfma_at(oa[E], vf[i][1], u32x4{p01[E] & msk[3][0], p23[E] & msk[3][1], p01[E] & msk[4][0], p23[E] & msk[4][1]});
    });
  };
  auto store_o = [&](auto J) {
    u32x4 op[2][2] = {{pk8(oa[0], 0), pk8(oa[0], 1)}, {pk8(oa[1], 0), pk8(oa[1], 1)}};
#pragma unroll
    for (int e = 0; e < 2; ++e) { auto t = __builtin_amdgcn_permlane32_swap(op[0][1][e], op[1][0][e], false, false); op[0][1][e] = t[0]; op[1][0][e] = t[1]; }
    const u32 o2 = ob + J * 3584;
    st128(op[0][0], c.rws, lane16, o2);
    st128(op[0][1], c.rws, lo16, o2 + 1024);
    st128(op[1][0], c.rws, lane16, o2 + 1536);
    st128(op[1][1], c.rws, lane16, o2 + 2560);
  };
  static_for<10, 16>(load_kv);
  wait_vm<7>();
  pin(kp[0]); pin(kp[1]);
  scores(IC<0>{});
  static_for<6, 10>(load_kv);
  softmax(IC<0>{});
  static_for<16, 22>(load_kv);
  wait_vm<6>();
  pv(IC<0>{});
  scores(IC<1>{});
  store_o(IC<0>{});
  softmax(IC<1>{});
  wait_vm<4>();
  pv(IC<1>{});
  store_o(IC<1>{});
  sync_vm<0>();
  if (w == 0) put_tag(tag, c.rws, lane == 0 ? (14 + hf) * 8 : OOB, WS_FLAG + 8192 + mt * 256);
  if (mt == 0 && hf == 0 && w == 0) {
#pragma unroll 1
    for (int i = 0; i < 4; ++i) spin(c.ws, WS_FLAG + 8192 + (i * 4 + (lane >> 4)) * 256 + (lane & 15) * 8, true, tag);
    put_tag(tag, c.rws, lane == 0 ? 0 : OOB, WS_E);
  }
}

__global__ __launch_bounds__(512, 2) void fused_kernel(const void* __restrict__ x, const void* __restrict__ wg, const void* __restrict__ wu,
                                                       const void* __restrict__ wd, const void* __restrict__ wo, const void* __restrict__ wqkv,
                                                       const void* __restrict__ gw, void* __restrict__ out, void* __restrict__ ws) {
  __shared__ __attribute__((aligned(1024))) char smem[163840];
  const int lane = threadIdx.x & 63, w = __builtin_amdgcn_readfirstlane(threadIdx.x >> 6);
  Ctx c;
  {
    u64 pc;
    asm volatile("s_getpc_b64 %0" : "=s"(pc));
    c.pf = touch((w == 6 && lane < 7) ? ((blockIdx.x >> 3) * 7 + lane) * 128 : OOB, mk((const void*)(pc & ~255ull), CODE_BYTES));
  }
  asm volatile("" ::"s"(x), "s"(wg), "s"(wu), "s"(wd), "s"(wo), "s"(wqkv), "s"(gw), "s"(out), "s"(ws));
  const int b = blockIdx.x, xc = b & 7, ix = b >> 3;
  const int mq = xc >> 1, mt = mq * 4 + (ix & 3), rl = ix >> 2, rh = xc & 1;
  const bool isa = rl < 7;
  const int role = rh * 7 + rl;
  c.sm = (LAS char*)smem;
  c.m0w = (u32)(size_t)c.sm + w * 1024;
  c.rx = mk(x, 2048 * 896); c.rqkv = mk(wqkv, 448 * 2688); c.rwg = mk(wg, 448 * 3584); c.rwu = mk(wu, 448 * 3584);
  c.rwd = mk(wd, 1792 * 896); c.rwo = mk(wo, 448 * 896); c.rws = mk(ws, WS_END);
  c.ws = (const char*)ws;
  {
    constexpr u64 mod7 = 0x1065432106543210ull;
    c.seq = (u32)(mod7 >> (4 * ((mod7 >> (4 * (mq + 3 * rh + 2 * (ix & 3)))) & 7)));
    c.seq2 = (u32)(mod7 >> (4 * xc));
#pragma unroll
    for (int j = 0; j < 7; ++j) { u32 k = (c.seq >> (4 * j)) & 7; asm volatile("" : "+s"(k)); c.kc[j] = k; }
  }
  const int r8 = lane >> 3, c8 = lane & 7, R = 8 * w + r8;
  c.vx = (mt * 128 + 2 * R) * 896 + ((c8 ^ ((R >> 1) & 7)) << 4);
  const int sw = (c8 ^ ((r8 & 2) << 1)) << 4;
  const int isai = (rl - 7) >> 31;
  c.vkv = R * 2688 + ((896 + role * 128) & isai) + ((rh * 384) & ~isai) + sw;
  static_for<0, 2>([&](auto N) { issueA<N>(c); });
  if (__builtin_expect(isa, 1)) {
    c.vg = R * 3584 + role * 128 + sw;
    c.vu = R * 3584 + role * 128 + (sw ^ 32);
    c.vw2 = R * 896 + rl * 128 + sw;
    type_a(c, w, lane, mt, role, xc * 4 + (ix & 3), rl, out);
  } else {
    type_b(c, w, lane, mt, rh, gw);
  }
}

extern "C" void kernel_launch(void* const* d_in, const int* in_sizes, int n_in, void* d_out, int out_size, void* d_ws, size_t ws_size,
                              hipStream_t stream) {
  fused_kernel<<<256, 512, 0, stream>>>(d_in[0], d_in[4], d_in[5], d_in[6], d_in[2], d_in[1], d_in[7], d_out, d_ws);
}
```

```cpp
#include <hip/hip_runtime.h>
#include <hip/hip_bf16.h>

typedef unsigned long long u64;
typedef unsigned u32;
typedef __bf16 bf16x8 __attribute__((ext_vector_type(8)));
typedef __bf16 bf16x2 __attribute__((ext_vector_type(2)));
typedef short s16x4 __attribute__((ext_vector_type(4)));
typedef u32 u32x4 __attribute__((ext_vector_type(4)));
typedef u32 u32x2 __attribute__((ext_vector_type(2)));
typedef float f32x16 __attribute__((ext_vector_type(16)));
typedef float f32x4 __attribute__((ext_vector_type(4)));
typedef __amdgpu_buffer_rsrc_t rsrc_t;
#define LAS __attribute__((address_space(3)))
#define GAS __attribute__((address_space(1)))
#define DI __device__ __forceinline__
#define SCHED() __builtin_amdgcn_sched_barrier(0)
template <int V> using IC = std::integral_constant<int, V>;

constexpr u32 WS_E = 0;
constexpr u32 WS_FLAG = 4096;
constexpr u32 WS_OMG = 32768;
constexpr u32 WS_K = 65536;
constexpr u32 KV_T = 28672;
constexpr u32 WS_V = WS_K + 64 * KV_T;
constexpr u32 WS_A2 = WS_V + 64 * KV_T;
constexpr u32 A2_T = 143360;
constexpr u32 WS_END = WS_A2 + 64 * A2_T;
constexpr u32 OOB = 0xFFFFFF00u;
constexpr u32 CODE_BYTES = 25600;
constexpr int SC1 = 16;
constexpr int SLOT = 8192;
constexpr int SPIN_CAP = 1 << 15;

DI rsrc_t mk(const void* p, u32 bytes) { return __builtin_amdgcn_make_buffer_rsrc((void*)p, (short)0, (int)bytes, 0x00020000); }
template <u32 DST> DI void dma(u32 m0w, u32 voff, rsrc_t rs, u32 soff) {
  asm volatile("s_add_u32 m0, %0, %4\n\ts_nop 0\n\tbuffer_load_dwordx4 %1, %2, %3 offen lds" ::"s"(m0w), "v"(voff), "s"(rs), "s"(soff), "n"(DST) : "memory", "scc");
}
template <u32 DST, u32 MUL> DI void dmam(u32 m0w, u32 voff, rsrc_t rs, u32 k) {
  u32 t;
  asm volatile("s_add_u32 m0, %1, %5\n\ts_mul_i32 %0, %4, %6\n\tbuffer_load_dwordx4 %2, %3, %0 offen lds" : "=&s"(t) : "s"(m0w), "v"(voff), "s"(rs), "s"(k), "n"(DST), "n"(MUL) : "memory", "scc");
}
DI u32x4 ld128_sc1(u32 voff, rsrc_t rs, u32 soff) {
  u32x4 v;
  asm volatile("buffer_load_dwordx4 %0, %1, %2, %3 offen sc1" : "=v"(v) : "v"(voff), "s"(rs), "s"(soff) : "memory");
  return v;
}
DI u32x2 ld64_sc1(u32 voff, rsrc_t rs, u32 soff) {
  u32x2 v;
  asm volatile("buffer_load_dwordx2 %0, %1, %2, %3 offen sc1" : "=v"(v) : "v"(voff), "s"(rs), "s"(soff) : "memory");
  return v;
}
DI u32 ld32_sc1(u32 voff, rsrc_t rs, u32 soff) {
  u32 v;
  asm volatile("buffer_load_dword %0, %1, %2, %3 offen sc1" : "=v"(v) : "v"(voff), "s"(rs), "s"(soff) : "memory");
  return v;
}
DI u32 touch(u32 voff, rsrc_t rs) {
  u32 v;
  asm volatile("buffer_load_dword %0, %1, %2, 0 offen" : "=v"(v) : "v"(voff), "s"(rs) : "memory");
  return v;
}
DI void st128(u32x4 v, rsrc_t rs, u32 voff, u32 soff) {
  asm volatile("buffer_store_dwordx4 %0, %1, %2, %3 offen sc1\n\ts_nop 1" ::"v"(v), "v"(voff), "s"(rs), "s"(soff) : "memory");
}
constexpr int clampvm(int n) { return n > 63 ? 63 : n; }
template <int N> DI void wait_vm() { static_assert(N >= 0, ""); asm volatile("s_waitcnt vmcnt(%0)" ::"n"(clampvm(N)) : "memory"); }
template <int N> DI void sync_vm() { static_assert(N >= 0, ""); asm volatile("s_waitcnt vmcnt(%0)\n\ts_barrier" ::"n"(clampvm(N)) : "memory"); }
template <class T> DI void pin(T& v) { asm volatile("" : "+v"(v)); }
template <int I, int N, class Fn> DI void static_for(Fn&& f) {
  if constexpr (I < N) { f(IC<I>{}); static_for<I + 1, N>(f); }
}

DI u32x4 trfrag(LAS char* p) {
  s16x4 lo = __builtin_amdgcn_ds_read_tr16_b64_v4i16((LAS s16x4*)p);
  s16x4 hi = __builtin_amdgcn_ds_read_tr16_b64_v4i16((LAS s16x4*)(p + 512));
  u32x2 a = __builtin_bit_cast(u32x2, lo), b = __builtin_bit_cast(u32x2, hi);
  return u32x4{a.x, a.y, b.x, b.y};
}
DI f32x16 mfma(u32x4 a, u32x4 b, f32x16 c) {
  return __builtin_amdgcn_mfma_f32_32x32x16_bf16(__builtin_bit_cast(bf16x8, a), __builtin_bit_cast(bf16x8, b), c, 0, 0, 0);
}
DI void mfma_at(f32x16& c, u32x4 a, u32x4 b) { c = mfma(a, b, c); asm volatile("" : "+v"(c)); }
DI u32 pk(float a, float b) { bf16x2 c; c[0] = (__bf16)a; c[1] = (__bf16)b; return __builtin_bit_cast(u32, c); }
DI u32x4 pk8(const f32x16& v, int s) { return u32x4{pk(v[8 * s], v[8 * s + 1]), pk(v[8 * s + 2], v[8 * s + 3]), pk(v[8 * s + 4], v[8 * s + 5]), pk(v[8 * s + 6], v[8 * s + 7])}; }
DI float sigmoidf(float z) { return __builtin_amdgcn_rcpf(1.f + __builtin_amdgcn_exp2f(z * -1.4426950408889634f)); }
DI float glu(float g, float u) { return u * sigmoidf(g); }
DI float xhalf_max(float v) { auto r = __builtin_amdgcn_permlane32_swap(__float_as_uint(v), __float_as_uint(v), false, false); return fmaxf(__uint_as_float(r[0]), __uint_as_float(r[1])); }
DI float xhalf_sum(float v) { auto r = __builtin_amdgcn_permlane32_swap(__float_as_uint(v), __float_as_uint(v), false, false); return __uint_as_float(r[0]) + __uint_as_float(r[1]); }

DI void spin(const char* ws, u32 off, bool need, u64 tag) {
  const GAS u64* p = (const GAS u64*)(ws + off);
#pragma unroll 1
  for (int i = 0; i < SPIN_CAP; ++i) {
    u64 v = __hip_atomic_load(p, __ATOMIC_RELAXED, __HIP_MEMORY_SCOPE_AGENT);
    if (__all(!need || v == tag)) break;
    __builtin_amdgcn_s_sleep(2);
  }
  asm volatile("" ::: "memory");
}
DI bool match(u32x2 v, bool need, u64 tag) { return __all(!need || (v.x == (u32)tag && v.y == (u32)(tag >> 32))); }
DI u64 tag_of(u32x2 ev) {
  u64 e = ((u64)__builtin_amdgcn_readfirstlane(ev.y) << 32) | (u32)__builtin_amdgcn_readfirstlane(ev.x);
  return e * 6364136223846793005ull + 1442695040888963407ull;
}
DI void put_tag(u64 tag, rsrc_t rs, u32 voff, u32 soff) { __builtin_amdgcn_raw_buffer_store_b64(u32x2{(u32)tag, (u32)(tag >> 32)}, rs, voff, soff, SC1); }

constexpr char HEAD[] = "KKGKKKKK";
constexpr int BARS[] = {0, 2, 6, 8, 10, 12, 14, 16, 20, 24, 36, 46, 50, 58, 62, 68, 76, 80, 86, 96};
constexpr int PRO_DMA = 9;
constexpr int HEAD_N = 18, HEAD_RATE = 22;
constexpr int EP1 = 2;
constexpr int U_KVPUB = 16, U_H1PUB = 40, U_H2PUB = 57, U_POLL1 = 52, U_CHK1 = 56, U_POLL2 = 64, U_CHK2 = 68, U_POLL3 = 70, U_CHK3 = 76;
constexpr int NV = 105, U0 = 14, IT_OMG = 28;
constexpr int imin(int a, int b) { return a < b ? a : b; }
constexpr int item_of(int i) { return i < IT_OMG ? i : i + 1; }
constexpr int ld_rel(int k) { return k < 6 ? 0 : 1 + (k - 6) / 2; }
constexpr int ld_iter(int it) { return U0 + (it < 14 ? U_CHK1 + ld_rel(it) : (it < 28 ? U_CHK2 + ld_rel(it - 14) : U_CHK3 + (it - 28) / 2)); }
constexpr int TM = 8, U_H2A = 56 - TM / 2 + 2;
constexpr bool tm_it(int u) { return u >= 56 - TM && u < 56; }
constexpr int g_tile(int u, int i) { return tm_it(u) ? (u - (56 - TM)) / (TM / 2) : i; }
constexpr int g_ks(int u, int i) { return tm_it(u) ? 56 - TM + 2 * ((u - (56 - TM)) % (TM / 2)) + i : u; }
constexpr int UE0 = 32, NE = (56 - TM - UE0) / 2;
struct Sched {
  int el[NV + 2] = {}, ej[NV + 2] = {};
  int nhead = 0, vkvst = 0, vg1 = 0;
  int xl[NV + 2] = {}, wl[NV + 2] = {};
  int kind[NV + 2] = {}, idx[NV + 2] = {}, lo[NV + 2] = {}, hi[NV + 2] = {};
  int pk[84] = {}, pi[84] = {}, pj[84] = {};
  bool bar[NV] = {};
  int cum[NV + 1] = {}, base[NV + 1] = {}, nst[NV] = {}, ldlo[NV + 1] = {}, cnt[NV] = {}, pdma[84] = {}, pld[36] = {}, freed[NV] = {};
  int cnt0 = 0; bool ok = true;
  constexpr Sched() {
    int v = 0, n = 0, k = 0, g = 0, ne = 0;
    for (int e = 0; e < NV + 2; ++e) el[e] = -1;
    int nx = 0;
    auto add = [&](int kd, int st, int nit, int nw) {
      const int first = n; int x = -1;
      if (st < 7 && st == nx) { x = n; for (int e = 0; e < 2; ++e) { pk[n] = 0; pi[n] = st; pj[n] = e; ++n; } ++nx; }
      for (int e = 0; e < nit; ++e) { kind[v] = kd; idx[v] = nit * st + e; lo[v] = first; hi[v] = n + nw - 1; xl[v] = x; wl[v] = n; ++v; }
      for (int e = 0; e < nw; ++e) { pk[n] = kd; pi[n] = st; pj[n] = kd ? e : 2; ++n; }
      if (kd == 1 && 4 * st >= UE0 && 4 * st < 56 - TM) {
        for (int e = 0; e < 4; ++e) { el[v - 4 + e] = hi[v - 4 + e] = n + e / 2; ej[v - 4 + e] = 2 * ne + e; }
        for (int e = 0; e < 2; ++e) { pk[n] = 2; pi[n] = ne++; ++n; }
      }
    };
    for (int i = 0; HEAD[i]; ++i) { if (HEAD[i] == 'K') { add(0, k++, 2, 1); nhead = v; } else { add(1, g++, 4, 2); if (g == 7) vg1 = v; } }
    if (k != 7 || g > 7) ok = false;
    vkvst = nhead + 1;
    while (g < 14) { add(1, g++, 4, 2); if (g == 7) vg1 = v; }
    if (ne != NE) ok = false;
    for (int i = 0; i < 35; ++i) { kind[v] = 2; idx[v] = 56 + i; lo[v] = wl[v] = n; if (i < NE) hi[v] = n - 1; else { hi[v] = n; pk[n] = 2; pi[n] = i; ++n; } ++v; }
    const int wa = wl[U0 + 56 - TM];
    for (int u = 56 - TM; u < 56; ++u) {
      wl[U0 + u] = wa + 2 * ((g_ks(u, 0) - (56 - TM)) / 4);
      lo[U0 + u] = g_tile(u, 0) == 0 ? wa : wl[U0 + u];
      hi[U0 + u] = g_tile(u, 0) == 0 ? wl[U0 + u] + 1 : wa + TM / 2 - 1;
    }
    kind[NV] = kind[NV + 1] = 3; hi[NV] = hi[NV + 1] = 83;
    for (int e = 0; e + 1 < NV; ++e) if (lo[e] > lo[e + 1] || hi[e] > hi[e + 1]) ok = false;
    for (int e : BARS) bar[e] = true;
    bar[U0 + U_KVPUB] = bar[U0 + U_H1PUB] = bar[U0 + U_H2PUB] = bar[U0 + U_CHK2] = bar[U0 + U_CHK3] = true;
    if (U0 + U_KVPUB <= vkvst || U0 + U_H1PUB <= vg1 + 2 * EP1 || bar[U0 + U_CHK1]) ok = false;
    int fr = 0, cc = PRO_DMA, cr = 0, it = 0;
    cum[0] = cc; base[0] = PRO_DMA + 1;
    for (n = 0; n < PRO_DMA; ++n) pdma[n] = n;
    for (v = 0; v < NV; ++v) {
      const int u = v - U0;
      if (bar[v] || u == U_CHK1) fr = lo[v];
      freed[v] = fr;
      nst[v] = 3 * (v == vkvst) + (v == vg1 + EP1) + (v == vg1 + 2 * EP1) + (u == U_H2A) + (u == 56) + (u == U_POLL1) + (u == U_POLL2) + (u == U_POLL3);
      ldlo[v] = it;
      while (it < 36 && ld_iter(it) == v) { pld[it] = base[v] + nst[v] + (it - ldlo[v]); ++it; }
      cr += v < HEAD_N ? HEAD_RATE : 16;
      const int t = imin(imin(cc + imin(cr / 16, 2), fr + 20), 84);
      cr -= 16 * (t - cc); if (cr > 16) cr = 16;
      for (n = cc; n < t; ++n) pdma[n] = base[v] + nst[v] + (it - ldlo[v]) + (n - cc);
      base[v + 1] = base[v] + nst[v] + (it - ldlo[v]) + (t - cc);
      cc = t; cum[v + 1] = cc;
    }
    ldlo[NV] = it;
    if (it != 36 || cc != 84) ok = false;
    for (v = -1; v < NV; ++v) {
      if (v >= 0 && !bar[v]) continue;
      int nb = v + 1; while (nb < NV && !bar[nb]) ++nb;
      const int need = hi[nb + 1], at = v < 0 ? 0 : v;
      if (need >= cum[at]) ok = false;
      int c = base[at] - pdma[need] - 1;
      const int u = v - U0;
      if (u == U_KVPUB) c = imin(c, base[v] - (base[vkvst] + nst[vkvst]));
      if (u == U_H1PUB) c = imin(c, base[v] - (base[vg1 + 2 * EP1] + nst[vg1 + 2 * EP1]));
      if (u == U_H2PUB) c = imin(c, base[v] - (base[U0 + 56] + nst[U0 + 56]));
      if (v < 0) cnt0 = c; else cnt[v] = c;
    }
    for (int i = 1; i < 35; ++i) if (ld_iter(item_of(i)) >= U0 + 56 + i) ok = false;
  }
};
constexpr Sched SA{};
static_assert(SA.ok, "schedule: a piece or operand is used before it is requested, or HEAD is malformed");
static_assert(U_CHK1 == 56 && U_POLL1 < U_CHK1 && U_CHK1 < U_POLL2 && U_POLL2 < U_CHK2 && U_CHK2 < U_POLL3 && U_POLL3 < U_CHK3, "");
static_assert(U_KVPUB < U_H1PUB && U_H1PUB < U_CHK1 && U_H2PUB > 56 && U_H2PUB < U_CHK2, "a workgroup publishes a class before it waits for that class");
static_assert(SA.freed[NV - 1] >= 72, "the reduction scratch (slots 4..11 = pieces 64..71) must be free at the end");

struct Ctx {
  LAS char* sm; u32 m0w;
  rsrc_t rx, rqkv, rwg, rwu, rwd, rwo, rws;
  const char* ws;
  u32 pf;
  u32 kc[7], seq, seq2;
  u32 vx, vkv, vg, vu, vw2;
};

template <int I> DI u32 blk2(const Ctx& c) {
  return I < 28 ? (I / 14) * 14 + 2 * ((c.seq2 >> (4 * ((I % 14) / 2))) & 7) + (I & 1) : 28 + ((c.seq2 >> (4 * (I < 28 ? 0 : I - 28))) & 7);
}
template <int N> DI void issueA(const Ctx& c) {
  constexpr u32 dst = (N % 20) * SLOT;
  constexpr int k = SA.pk[N], st = SA.pi[N], j = SA.pj[N];
  if constexpr (k == 0) {
    if constexpr (j < 2) dmam<dst, 128>(c.m0w, c.vx + j * 896, c.rx, c.kc[st]);
    else dmam<dst, 64 * 2688>(c.m0w, c.vkv, c.rqkv, c.kc[st]);
  } else if constexpr (k == 1) {
    if constexpr (j == 0) dmam<dst, 64 * 3584>(c.m0w, c.vg + (st / 7) * 1792, c.rwg, c.kc[st % 7]);
    else dmam<dst, 64 * 3584>(c.m0w, c.vu + (st / 7) * 1792, c.rwu, c.kc[st % 7]);
  } else if constexpr (st < 28) dmam<dst, 64 * 896>(c.m0w, c.vw2, c.rwd, blk2<st>(c));
  else dmam<dst, 64 * 896>(c.m0w, c.vw2, c.rwo, blk2<st>(c) - 28);
}

DI void type_a(Ctx& c, int w, int lane, int mt, int role, int R2, int n2, void* out) {
  const int r = lane & 31, hi = lane >> 5, blk = (lane >> 4) & 1, q = (lane & 15) >> 2, p = lane & 3;
  const int sp = ((p & 1) << 1) | (p >> 1);
  const int rg = w & 3, cg = w >> 2, T = mt * 4 + rg;
  const bool isv = role >= 7;
  LAS char* sm = c.sm;
  static_for<2, PRO_DMA>([&](auto N) { issueA<N>(c); });
  put_tag(0, c.rws, (w == 0 && lane < 3) ? lane * 4096 + role * 8 : OOB, WS_FLAG + mt * 256);
  asm volatile("" ::: "memory");
  const int R = 32 * (rg & 1) + r, xm = (R >> 1) & 7;
  int xa[4];
#pragma unroll
  for (int s = 0; s < 4; ++s) xa[s] = (rg >> 1) * SLOT + R * 128 + (((2 * s + hi) ^ xm) << 4);
  const int rowb = (8 * hi + q) * 128, q2 = q & 2;
  const int akv = rowb + (((2 * cg + blk) ^ q2) << 5) + (isv ? p : sp) * 8;
  int akvw[3];
#pragma unroll
  for (int k = 0; k < 3; ++k) { akvw[k] = akv + k * 65536; pin(akvw[k]); }
  int agu[2];
#pragma unroll
  for (int i = 0; i < 2; ++i) agu[i] = blk * SLOT + rowb + (((2 * cg + i) ^ q2 ^ blk) << 5) + sp * 8;
  int aguw[2][3];
#pragma unroll
  for (int i = 0; i < 2; ++i)
#pragma unroll
    for (int k = 0; k < 3; ++k) { aguw[i][k] = agu[i] + k * 65536; pin(aguw[i][k]); }
  const int rt = w & 1, kq = w >> 1;
  int aw2[2], aw2w[2][3];
#pragma unroll
  for (int i = 0; i < 2; ++i) aw2[i] = kq * 2048 + rowb + (((2 * i + blk) ^ q2) << 5) + p * 8;
  const int T2 = (R2 >> 1) * 4 + 2 * rt + (R2 & 1);

  u32x4 xf[28], wf[2][2], af[35], hp[2], wr[2 * NE];
  f32x16 akvacc = {}, g1[2] = {}, g2[2] = {}, ah[2] = {}, ao[2] = {};
  u32x2 ev, pollv; u32 omg; u64 tag;
  const u32 lane16 = lane * 16;
  const u32 poll_off = WS_FLAG + (R2 >> 1) * 256 + (lane & 15) * 8;
  const bool need_h = (lane & 15) < 14;
  const u32 a2_off = WS_A2 + T2 * A2_T + kq * 1024 + lane16;
  const int isvi = (6 - role) >> 31;
  u32 kv_off = ((WS_V + hi * 7168 + ((role - 7) * 64 + cg * 32 + r) * 16) & isvi) + ((WS_K + (role * 8 + cg * 4) * 512 + lane16) & ~isvi) + T * KV_T;
  pin(kv_off);
  const u32 kv_step = 1024 + (13312 & isvi);
  const u32 h_off = WS_A2 + T * A2_T + (role * 4 + 2 * cg) * 1024 + lane16;

  auto read_w = [&](auto V, auto I) {
    constexpr int v = V, i = I, k = SA.kind[v], u = SA.idx[v], sl = SA.wl[v] % 20;
    if constexpr (k < 2 && SA.xl[v] >= 0 && (k == 0 || i == 0)) {
      constexpr int ks = k == 0 ? 2 * u + i : u, xs = SA.xl[v] % 20;
      const int wrap = xs == 19 ? (rg >> 1) * (-20 * SLOT) : 0;
      xf[ks] = *(LAS u32x4*)(sm + xa[ks % 4] + wrap + xs * SLOT);
    }
    if constexpr (k == 0) {
      wf[v & 1][i] = trfrag(sm + akvw[sl / 8] + (sl % 8) * SLOT + ((2 * u + i) % 4) * 2048);
    } else if constexpr (k == 1) {
      const int wrap = sl == 19 ? blk * (-20 * SLOT) : 0;
      wf[v & 1][i] = trfrag(sm + aguw[g_tile(u, i)][sl / 8] + wrap + (sl % 8) * SLOT + (g_ks(u, i) % 4) * 2048);
      if constexpr (u == UE0 - 2) {
#pragma unroll
        for (int e = 0; e < 3; ++e) { aw2w[i][e] = aw2[i] + e * 65536; pin(aw2w[i][e]); }
      }
      if constexpr (i == 0 && SA.el[v] >= 0) { constexpr int es = SA.el[v] % 20, j = SA.ej[v]; wr[j] = trfrag(sm + aw2w[j & 1][es / 8] + (es % 8) * SLOT); }
    } else if constexpr (k == 2 && u - 56 >= NE) {
      wf[v & 1][i] = trfrag(sm + aw2w[i][sl / 8] + (sl % 8) * SLOT);
    }
  };
  auto publish = [&](int cls) {
    if (w == 0) put_tag(tag, c.rws, lane == 0 ? role * 8 : OOB, WS_FLAG + cls * 4096 + mt * 256);
  };
  auto load_item = [&](auto IT) {
    constexpr int it = IT;
    if constexpr (it == IT_OMG) omg = ld32_sc1(r * 4, c.rws, WS_OMG + T2 * 128);
    else { constexpr int i = it < IT_OMG ? it : it - 1; af[i] = ld128_sc1(a2_off, c.rws, blk2<i>(c) * 4096); }
  };
  auto check = [&](auto CNT, int cls, bool need) {
    wait_vm<CNT>();
    pin(pollv);
    if (w == 0 && !match(pollv, need, tag)) spin(c.ws, poll_off + cls * 4096, need, tag);
  };

  sync_vm<SA.cnt0>();
  pin(c.pf);
  static_for<0, 4>([&](auto S) { read_w(IC<S / 2>{}, IC<S % 2>{}); });
  SCHED();
  auto h_pair = [&](const f32x16& gu, int k) { u32 hv = pk(glu(gu[2 * k], gu[8 + 2 * k]), glu(gu[2 * k + 1], gu[9 + 2 * k])); pin(hv); return hv; };
  auto iter = [&](auto V, auto ISV) {
    constexpr int v = V, b = v & 1, kd = SA.kind[v], ui = SA.idx[v], u = v - U0;
    constexpr bool nbar = v + 1 < NV && SA.bar[v + 1 < NV ? v + 1 : v];
    constexpr int nld = SA.ldlo[v + 1] - SA.ldlo[v], nd = SA.cum[v + 1] - SA.cum[v];
    if constexpr (SA.bar[v]) sync_vm<SA.cnt[v]>();
    if constexpr (u == 56) {
#pragma unroll
      for (int j = 0; j < 8; j += 2) hp[1][j / 2] = pk(glu(g2[1][j], g2[1][8 + j]), glu(g2[1][j + 1], g2[1][9 + j]));
      st128(hp[1], c.rws, h_off, 57 * 1024);
      SCHED();
      check(IC<SA.base[v] + SA.nst[v] - SA.base[U0 + U_POLL1] - 1>{}, 1, need_h);
      asm volatile("s_barrier" ::: "memory");
      static_for<SA.ldlo[v], SA.ldlo[v + 1]>(load_item);
    }
    if constexpr (u >= 56) {
      wait_vm<SA.base[v] + (u == 56 ? SA.nst[v] + nld : 0) - SA.pld[item_of(u - 56)] - 1>();
      pin(af[u - 56]);
    }
    SCHED();
    static_for<0, 2>([&](auto I) {
      constexpr int i = I;
      if constexpr (kd == 0) { if constexpr (ISV) mfma_at(akvacc, xf[2 * ui + i], wf[b][i]); else mfma_at(akvacc, wf[b][i], xf[2 * ui + i]); }
      else if constexpr (ui < 28) mfma_at(g1[i], wf[b][i], xf[ui]);
      else if constexpr (ui < 56) mfma_at(g2[g_tile(ui, i)], wf[b][i], xf[g_ks(ui, i) - 28]);
      else if constexpr (ui - 56 < NE) mfma_at(ah[i], wr[2 * (ui - 56) + i], af[ui - 56]);
      else if constexpr (ui < 84) mfma_at(ah[i], wf[b][i], af[ui - 56]);
      else mfma_at(ao[i], wf[b][i], af[ui - 56]);
      if constexpr (i == 0 && SA.el[v] >= 0) pin(wr[SA.ej[v] + i]);
      SCHED();
      if constexpr (i == 0 && v > 0 && SA.bar[v]) read_w(IC<v + 1>{}, IC<1>{});
      if constexpr (i == 0 || !nbar) read_w(IC<v + 2>{}, I);
      if constexpr (i == 0) {
        if constexpr (u == U_KVPUB) { pin(ev); tag = tag_of(ev); publish(0); }
        if constexpr (u == U_H1PUB) publish(1);
        if constexpr (u == U_H2PUB) publish(2);
        if constexpr (v == SA.vkvst) ev = ld64_sc1(w == 0 ? WS_E : OOB, c.rws, 0);
        if constexpr (u == U_POLL1) pollv = ld64_sc1(w == 0 ? poll_off : OOB, c.rws, 4096);
        if constexpr (u == U_POLL2 || u == U_POLL3) pollv = ld64_sc1(w == 0 ? poll_off : OOB, c.rws, 8192);
        if constexpr (v == SA.vkvst) { st128(pk8(akvacc, 0), c.rws, kv_off, 0); st128(pk8(akvacc, 1), c.rws, kv_off, kv_step); }
        if constexpr (v == SA.vg1 + EP1) st128(hp[0], c.rws, h_off, 0);
        if constexpr (v == SA.vg1 + 2 * EP1) st128(hp[1], c.rws, h_off, 1024);
        if constexpr (u == U_H2A) st128(hp[0], c.rws, h_off, 56 * 1024);
        if constexpr (u != 56) static_for<SA.ldlo[v], SA.ldlo[v + 1]>(load_item);
      }
      if constexpr (v >= SA.vg1 && v < SA.vg1 + 2 * EP1) {
        constexpr int t = (v - SA.vg1) / EP1, per = 4 / EP1, k0 = ((v - SA.vg1) % EP1) * per;
        constexpr int ka = nbar ? (i ? per : 0) : (i * per + 1) / 2, kb = nbar ? per : ((i + 1) * per + 1) / 2;
#pragma unroll
        for (int k = k0 + ka; k < k0 + kb; ++k) hp[t][k] = h_pair(g1[t], k);
      }
      if constexpr (u >= U_H2A - 2 && u < U_H2A) hp[0][2 * (u - (U_H2A - 2)) + i] = h_pair(g2[0], 2 * (u - (U_H2A - 2)) + i);
      if constexpr (i == 0 && nd == 2) issueA<SA.cum[v]>(c);
      if constexpr (i == (nbar ? 0 : 1) && nd >= 1) issueA<SA.cum[v + 1] - 1>(c);
      if constexpr (i == 0 && u + 1 == U_CHK2) check(IC<SA.base[v + 1] - SA.base[U0 + U_POLL2] - 1 + i>{}, 2, need_h);
      if constexpr (i == 0 && u + 1 == U_CHK3) check(IC<SA.base[v + 1] - SA.base[U0 + U_POLL3] - 1 + i>{}, 2, !need_h);
      SCHED();
    });
  };
  if (isv) static_for<0, SA.nhead>([&](auto V) { iter(V, IC<1>{}); });
  else static_for<0, SA.nhead>([&](auto V) { iter(V, IC<0>{}); });
  static_for<SA.nhead, NV>([&](auto V) { iter(V, IC<0>{}); });
  pin(omg);
  const float om = __uint_as_float(omg);
  LAS char* red = sm + 4 * SLOT;
#pragma unroll
  for (int t = 0; t < 2; ++t)
#pragma unroll
    for (int a = 0; a < 4; ++a) {
      f32x4 v;
#pragma unroll
      for (int e = 0; e < 4; ++e) v[e] = ah[t][4 * a + e] * om + ao[t][4 * a + e];
      *(LAS f32x4*)(red + kq * 16384 + (2 * r + rt) * 256 + (((8 * t + 2 * a + hi) ^ (r & 15)) << 4)) = v;
    }
  asm volatile("s_waitcnt lgkmcnt(0)\n\ts_barrier" ::: "memory");
  {
    const int row = w * 8 + (lane >> 3), c8 = lane & 7, m = (row >> 1) & 15;
    f32x4 s0 = {}, s1 = {};
#pragma unroll
    for (int k = 0; k < 4; ++k) {
      s0 += *(LAS f32x4*)(red + k * 16384 + row * 256 + (((2 * c8) ^ m) << 4));
      s1 += *(LAS f32x4*)(red + k * 16384 + row * 256 + (((2 * c8 + 1) ^ m) << 4));
    }
    const u32x4 o = {pk(s0[0], s0[1]), pk(s0[2], s0[3]), pk(s1[0], s1[1]), pk(s1[2], s1[3])};
    __builtin_amdgcn_raw_buffer_store_b128(o, mk(out, 2048 * 896), (R2 * 64 + row) * 896 + n2 * 128 + c8 * 16, 0, SC1);
  }
}

constexpr int PRO_B = 12, GB_TOUCH = 18, GB_POLL = 25;
constexpr int cumB(int g) {
  int cc = PRO_B;
  for (int j = 0; j <= g; ++j) cc = imin(imin(cc + 2, 6 * ((j + 1) / 4) + 18), 42);
  return cc;
}
static_assert(cumB(GB_TOUCH - 1) == 42 && GB_POLL > 23, "both sit behind the last ring piece; the poll behind the last counted barrier");
template <int N> DI void issueB(const Ctx& c) {
  constexpr int kc = N / 6, j = N % 6;
  constexpr u32 dst = (N % 18) * SLOT;
  if constexpr (j < 2) dmam<dst, 128>(c.m0w, c.vx + j * 896, c.rx, c.kc[kc]);
  else dmam<dst, 64 * 2688>(c.m0w, c.vkv + (j - 2) * 128, c.rqkv, c.kc[kc]);
}

DI void type_b(Ctx& c, int w, int lane, int mt, int hf, const void* gw) {
  const int r = lane & 31, hi = lane >> 5, blk = (lane >> 4) & 1, q = (lane & 15) >> 2, p = lane & 3;
  const int sp = ((p & 1) << 1) | (p >> 1);
  const int rg = w & 3, cg = w >> 2, T = mt * 4 + rg;
  LAS char* sm = c.sm;
  if (w == 0) dma<18 * SLOT>(c.m0w, ((c.seq >> (4 * (lane >> 3))) & 7) * 128 + (lane & 7) * 16, mk(gw, 896), 0);
  static_for<2, PRO_B>([&](auto N) { issueB<N>(c); });
  if (w == 0) put_tag(0, c.rws, lane == 0 ? (14 + hf) * 8 : OOB, WS_FLAG + 8192 + mt * 256);
  const int R = 32 * (rg & 1) + r, xm = (R >> 1) & 7;
  int xa[4], aq[4];
#pragma unroll
  for (int s = 0; s < 4; ++s) xa[s] = (rg >> 1) * SLOT + R * 128 + (((2 * s + hi) ^ xm) << 4);
  const int h0 = 8 * hf + 4 * cg;
#pragma unroll
  for (int i = 0; i < 4; ++i) {
    const int col = 28 * (h0 + i) - 4 * (i & 1) - 192 * hf + 16 * blk + 4 * sp;
    aq[i] = (2 + (col >> 6)) * SLOT + (8 * hi + q) * 128 + (((col & 63) * 2) ^ ((q & 2) << 5));
  }
  const bool first = (mt == 0) && !(rg & 1);
  const int Tp = first ? T : ((rg & 1) ? T - 1 : T - 3);
  const u32 lane16 = lane * 16, lo16 = hi ? OOB : lane16;
  const u32 ob = WS_A2 + T * A2_T + (1792 + 28 * h0) * 64;
  const u32 omg_off = (hf == 0 && cg == 0 && hi == 0) ? r * 4 : OOB;
  const u32 poll_off = WS_FLAG + ((lane & 16) && mt > 0 ? mt - 1 : mt) * 256 + (lane & 15) * 8;
  const bool poll_need = (lane & 15) < 14;
  u32x4 xf[2], gf[2], wf[2][4];
  u32x4 kf[4][2], kp[2], vf[4][2], vp[4];
  f32x16 qa[4] = {};
  u32x2 ev, pollv;
  u32 tw;
  const int sr = (r & 19) | ((r & 4) << 1) | ((r & 8) >> 1);
  const u32 kp_off = (28 * (r >> 3) - 4 * ((r >> 3) & 1)) * 64 + (hi * 32 + 24 + (r & 7)) * 16;
  const u32 v_off = hi * 7168 + sr * 16;
  auto load_kv = [&](auto IT) {
    constexpr int it = IT;
    if constexpr (it < 2) kp[it] = ld128_sc1(kp_off, c.rws, WS_K + Tp * KV_T + 28 * h0 * 64 + it * 1024);
    else if constexpr (it < 10) {
      constexpr int i = (it - 2) / 2, k = it & 1;
      kf[i][k] = ld128_sc1(lane16, c.rws, WS_K + T * KV_T + (28 * (h0 + i) - 4 * (i & 1)) * 64 + k * 1024);
    } else {
      constexpr int i = (it - 10) / 3, k = (it - 10) % 3;
      const int wh = 28 * (h0 + i) - 4 * (i & 1);
      if constexpr (k == 0) vp[i] = ld128_sc1(v_off, c.rws, WS_V + Tp * KV_T + 14336 + wh * 16);
      else vf[i][k - 1] = ld128_sc1(v_off, c.rws, WS_V + T * KV_T + (k - 1) * 14336 + wh * 16);
    }
  };
  auto read_x = [&](auto G) {
    constexpr int g = G;
    if constexpr (g < 28) {
      xf[g & 1] = *(LAS u32x4*)(sm + xa[g % 4] + ((6 * (g / 4)) % 18) * SLOT);
      gf[g & 1] = *(LAS u32x4*)(sm + 18 * SLOT + g * 32 + hi * 16);
    }
  };
  auto read_w = [&](auto G, auto I) {
    constexpr int g = G;
    if constexpr (g < 28) wf[g & 1][I] = trfrag(sm + aq[I] + ((6 * (g / 4)) % 18) * SLOT + (g % 4) * 2048);
  };
  sync_vm<PRO_B - 6>();
  pin(c.pf);
  read_x(IC<0>{});
  static_for<0, 4>([&](auto I) { read_w(IC<0>{}, I); });
  SCHED();
  static_for<0, 28>([&](auto G) {
    constexpr int g = G, b = g & 1;
    if constexpr (g % 4 == 3 && g < 27) { asm volatile("s_waitcnt lgkmcnt(0)" ::: "memory"); sync_vm<cumB(g - 1) - 6 * (g / 4 + 2) + (g > GB_TOUCH)>(); }
    if constexpr (g == GB_POLL) { ev = ld64_sc1(w == 0 ? WS_E : OOB, c.rws, 0); pollv = ld64_sc1(w == 0 ? poll_off : OOB, c.rws, 0); }
    if constexpr (g == GB_TOUCH) {
      const int gi = (mt * 2 + hf) * 8 + w;
      tw = touch(((gi < 196 ? gi : gi - 196) * 64 + lane) * 128, gi < 196 ? c.rwd : c.rwo);
    }
    SCHED();
    static_for<0, 4>([&](auto I) {
      constexpr int i = I;
      mfma_at(qa[i], wf[b][i], xf[b]);
      SCHED();
      if constexpr (i == 0) read_x(IC<g + 1>{});
      read_w(IC<g + 1>{}, I);
      if constexpr (i < 2) {
        wf[b][2][2 * i] = r == 28 ? gf[b][2 * i] : wf[b][2][2 * i];
        wf[b][2][2 * i + 1] = r == 28 ? gf[b][2 * i + 1] : wf[b][2][2 * i + 1];
        pin(wf[b][2]);
      }
      if constexpr (i >= 2 && cumB(g - 1) + i - 2 < cumB(g)) issueB<cumB(g - 1) + i - 2>(c);
      SCHED();
    });
  });
  wait_vm<0>();
  pin(pollv); pin(ev); pin(tw);
  const u64 tag = tag_of(ev);
  if (w == 0 && !match(pollv, poll_need, tag)) spin(c.ws, poll_off, poll_need, tag);
  asm volatile("s_barrier" ::: "memory");
  static_for<0, 6>(load_kv);
  SCHED();
  const float z = __uint_as_float(__builtin_amdgcn_permlane32_swap(__float_as_uint(qa[2][12]), __float_as_uint(qa[2][12]), false, false)[1]);
  const float gate = sigmoidf(z);
  const u32 omg = __float_as_uint(sigmoidf(-z));
  int ab[4];
  u32 msk[5][2];
#pragma unroll
  for (int b = 0; b < 4; ++b) ab[b] = (r >> 3) - ((r & 7) < 4 * hi + b ? 1 : 0);
#pragma unroll
  for (int a = 0; a < 5; ++a)
#pragma unroll
    for (int e = 0; e < 2; ++e) msk[a][e] = (ab[2 * e] == a - 1 ? 0xFFFFu : 0u) | (ab[2 * e + 1] == a - 1 ? 0xFFFF0000u : 0u);
  const float csc = 0.18898223650461362f * 1.4426950408889634f;
  u32 p01[2], p23[2];
  u32x4 qf[4][2];
  static_for<0, 4>([&](auto I) {
    constexpr int i = I;
    qf[i][0] = pk8(qa[i], 0); qf[i][1] = pk8(qa[i], 1);
    if (i & 1) { qf[i][0][0] = hi ? qf[i][0][0] : 0u; qf[i][0][1] = hi ? qf[i][0][1] : 0u; }
    else       { qf[i][1][2] = hi ? 0u : qf[i][1][2]; qf[i][1][3] = hi ? 0u : qf[i][1][3]; }
    pin(qf[i][0]); pin(qf[i][1]);
  });
#pragma unroll
  for (int a = 0; a < 5; ++a) { pin(msk[a][0]); pin(msk[a][1]); }
  __builtin_amdgcn_raw_buffer_store_b32(omg, c.rws, omg_off, WS_OMG + T * 128, SC1);
  asm volatile("" ::: "memory");
  f32x16 so[2], sv[2], oa[2];
  auto scores = [&](auto J) {
    static_for<0, 2>([&](auto E) {
      constexpr int i = 2 * J + E;
      pin(kf[i][0]); pin(kf[i][1]);
      so[E] = mfma(kf[i][0], qf[i][0], f32x16{}); sv[E] = mfma(kp[0], qf[i][0], f32x16{});
      mfma_at(so[E], kf[i][1], qf[i][1]); mfma_at(sv[E], kp[1], qf[i][1]);
    });
  };
  auto softmax = [&](auto J) {
    static_for<0, 2>([&](auto E) {
      constexpr int i = 2 * J + E;
      float v[4];
#pragma unroll
      for (int b = 0; b < 4; ++b) {
        float t = first ? -1e30f : sv[E][4 * i + b];
#pragma unroll
        for (int a = 0; a < 4; ++a) t = ab[b] == a ? so[E][4 * a + b] : t;
        v[b] = t;
      }
      const float mx = xhalf_max(fmaxf(fmaxf(v[0], v[1]), fmaxf(v[2], v[3])));
#pragma unroll
      for (int b = 0; b < 4; ++b) v[b] = __builtin_amdgcn_exp2f((v[b] - mx) * csc);
      const float sc = gate * __builtin_amdgcn_rcpf(xhalf_sum((v[0] + v[1]) + (v[2] + v[3])));
      p01[E] = pk(v[0] * sc, v[1] * sc); p23[E] = pk(v[2] * sc, v[3] * sc);
    });
    pin(p01[0]); pin(p23[0]); pin(p01[1]); pin(p23[1]);
  };
  auto pv = [&](auto J) {
    static_for<0, 2>([&](auto E) {
      constexpr int i = 2 * J + E;
      pin(vp[i]); pin(vf[i][0]); pin(vf[i][1]);
      oa[E] = mfma(vp[i], u32x4{0u, 0u, p01[E] & msk[0][0], p23[E] & msk[0][1]}, f32x16{});
      oa[E] = mfma(vf[i][0], u32x4{p01[E] & msk[1][0], p23[E] & msk[1][1], p01[E] & msk[2][0], p23[E] & msk[2][1]}, oa[E]);
      mfma_at(oa[E], vf[i][1], u32x4{p01[E] & msk[3][0], p23[E] & msk[3][1], p01[E] & msk[4][0], p23[E] & msk[4][1]});
    });
  };
  auto store_o = [&](auto J) {
    u32x4 op[2][2] = {{pk8(oa[0], 0), pk8(oa[0], 1)}, {pk8(oa[1], 0), pk8(oa[1], 1)}};
#pragma unroll
    for (int e = 0; e < 2; ++e) { auto t = __builtin_amdgcn_permlane32_swap(op[0][1][e], op[1][0][e], false, false); op[0][1][e] = t[0]; op[1][0][e] = t[1]; }
    const u32 o2 = ob + J * 3584;
    st128(op[0][0], c.rws, lane16, o2);
    st128(op[0][1], c.rws, lo16, o2 + 1024);
    st128(op[1][0], c.rws, lane16, o2 + 1536);
    st128(op[1][1], c.rws, lane16, o2 + 2560);
  };
  static_for<10, 16>(load_kv);
  wait_vm<7>();
  pin(kp[0]); pin(kp[1]);
  scores(IC<0>{});
  static_for<6, 10>(load_kv);
  softmax(IC<0>{});
  static_for<16, 22>(load_kv);
  wait_vm<6>();
  pv(IC<0>{});
  scores(IC<1>{});
  store_o(IC<0>{});
  softmax(IC<1>{});
  wait_vm<4>();
  pv(IC<1>{});
  store_o(IC<1>{});
  sync_vm<0>();
  if (w == 0) put_tag(tag, c.rws, lane == 0 ? (14 + hf) * 8 : OOB, WS_FLAG + 8192 + mt * 256);
  if (mt == 0 && hf == 0 && w == 0) {
#pragma unroll 1
    for (int i = 0; i < 4; ++i) spin(c.ws, WS_FLAG + 8192 + (i * 4 + (lane >> 4)) * 256 + (lane & 15) * 8, true, tag);
    put_tag(tag, c.rws, lane == 0 ? 0 : OOB, WS_E);
  }
}

__global__ __launch_bounds__(512, 2) void fused_kernel(const void* __restrict__ x, const void* __restrict__ wg, const void* __restrict__ wu,
                                                       const void* __restrict__ wd, const void* __restrict__ wo, const void* __restrict__ wqkv,
                                                       const void* __restrict__ gw, void* __restrict__ out, void* __restrict__ ws) {
  __shared__ __attribute__((aligned(1024))) char smem[163840];
  const int lane = threadIdx.x & 63, w = __builtin_amdgcn_readfirstlane(threadIdx.x >> 6);
  Ctx c;
  {
    u64 pc;
    asm volatile("s_getpc_b64 %0" : "=s"(pc));
    c.pf = touch((w == 6 && lane < 7) ? ((blockIdx.x >> 3) * 7 + lane) * 128 : OOB, mk((const void*)(pc & ~255ull), CODE_BYTES));
  }
  asm volatile("" ::"s"(x), "s"(wg), "s"(wu), "s"(wd), "s"(wo), "s"(wqkv), "s"(gw), "s"(out), "s"(ws));
  const int b = blockIdx.x, xc = b & 7, ix = b >> 3;
  const int mq = xc >> 1, mt = mq * 4 + (ix & 3), rl = ix >> 2, rh = xc & 1;
  const bool isa = rl < 7;
  const int role = rh * 7 + rl;
  c.sm = (LAS char*)smem;
  c.m0w = (u32)(size_t)c.sm + w * 1024;
  c.rx = mk(x, 2048 * 896); c.rqkv = mk(wqkv, 448 * 2688); c.rwg = mk(wg, 448 * 3584); c.rwu = mk(wu, 448 * 3584);
  c.rwd = mk(wd, 1792 * 896); c.rwo = mk(wo, 448 * 896); c.rws = mk(ws, WS_END);
  c.ws = (const char*)ws;
  {
    constexpr u64 mod7 = 0x1065432106543210ull;
    c.seq = (u32)(mod7 >> (4 * ((mod7 >> (4 * (2 * mq + 4 * rh + 2 * (rl & 1) + (ix & 1)))) & 7)));
    c.seq2 = (u32)(mod7 >> (4 * xc));
#pragma unroll
    for (int j = 0; j < 7; ++j) { u32 k = (c.seq >> (4 * j)) & 7; asm volatile("" : "+s"(k)); c.kc[j] = k; }
  }
  const int r8 = lane >> 3, c8 = lane & 7, R = 8 * w + r8;
  c.vx = (mt * 128 + 2 * R) * 896 + ((c8 ^ ((R >> 1) & 7)) << 4);
  const int sw = (c8 ^ ((r8 & 2) << 1)) << 4;
  const int isai = (rl - 7) >> 31;
  c.vkv = R * 2688 + ((896 + role * 128) & isai) + ((rh * 384) & ~isai) + sw;
  static_for<0, 2>([&](auto N) { issueA<N>(c); });
  if (__builtin_expect(isa, 1)) {
    c.vg = R * 3584 + role * 128 + sw;
    c.vu = R * 3584 + role * 128 + (sw ^ 32);
    c.vw2 = R * 896 + rl * 128 + sw;
    type_a(c, w, lane, mt, role, xc * 4 + (ix & 3), rl, out);
  } else {
    type_b(c, w, lane, mt, rh, gw);
  }
}

extern "C" void kernel_launch(void* const* d_in, const int* in_sizes, int n_in, void* d_out, int out_size, void* d_ws, size_t ws_size,
                              hipStream_t stream) {
  fused_kernel<<<256, 512, 0, stream>>>(d_in[0], d_in[4], d_in[5], d_in[6], d_in[2], d_in[1], d_in[7], d_out, d_ws);
}
```

```cpp
#define TN 28
#include <hip/hip_runtime.h>
#include <hip/hip_bf16.h>

typedef unsigned long long u64;
typedef unsigned u32;
typedef __bf16 bf16x8 __attribute__((ext_vector_type(8)));
typedef __bf16 bf16x2 __attribute__((ext_vector_type(2)));
typedef short s16x4 __attribute__((ext_vector_type(4)));
typedef u32 u32x4 __attribute__((ext_vector_type(4)));
typedef u32 u32x2 __attribute__((ext_vector_type(2)));
typedef float f32x16 __attribute__((ext_vector_type(16)));
typedef float f32x4 __attribute__((ext_vector_type(4)));
typedef __amdgpu_buffer_rsrc_t rsrc_t;
#define LAS __attribute__((address_space(3)))
#define GAS __attribute__((address_space(1)))
#define DI __device__ __forceinline__
#define SCHED() __builtin_amdgcn_sched_barrier(0)
template <int V> using IC = std::integral_constant<int, V>;

constexpr u32 WS_E = 0;
constexpr u32 WS_FLAG = 4096;
constexpr u32 WS_OMG = 32768;
constexpr u32 WS_K = 65536;
constexpr u32 KV_T = 28672;
constexpr u32 WS_V = WS_K + 64 * KV_T;
constexpr u32 WS_A2 = WS_V + 64 * KV_T;
constexpr u32 A2_T = 143360;
constexpr u32 WS_END = WS_A2 + 64 * A2_T;
constexpr u32 OOB = 0xFFFFFF00u;
constexpr u32 CODE_BYTES = 25600;
constexpr int SC1 = 16;
constexpr int SLOT = 8192;
constexpr int SPIN_CAP = 1 << 15;

DI rsrc_t mk(const void* p, u32 bytes) { return __builtin_amdgcn_make_buffer_rsrc((void*)p, (short)0, (int)bytes, 0x00020000); }
template <u32 DST> DI void dma(u32 m0w, u32 voff, rsrc_t rs, u32 soff) {
  asm volatile("s_add_u32 m0, %0, %4\n\ts_nop 0\n\tbuffer_load_dwordx4 %1, %2, %3 offen lds" ::"s"(m0w), "v"(voff), "s"(rs), "s"(soff), "n"(DST) : "memory", "scc");
}
template <u32 DST, u32 MUL> DI void dmam(u32 m0w, u32 voff, rsrc_t rs, u32 k) {
  u32 t;
  asm volatile("s_add_u32 m0, %1, %5\n\ts_mul_i32 %0, %4, %6\n\tbuffer_load_dwordx4 %2, %3, %0 offen lds" : "=&s"(t) : "s"(m0w), "v"(voff), "s"(rs), "s"(k), "n"(DST), "n"(MUL) : "memory", "scc");
}
DI u32x4 ld128_sc1(u32 voff, rsrc_t rs, u32 soff) {
  u32x4 v;
  asm volatile("buffer_load_dwordx4 %0, %1, %2, %3 offen sc1" : "=v"(v) : "v"(voff), "s"(rs), "s"(soff) : "memory");
  return v;
}
DI u32x2 ld64_sc1(u32 voff, rsrc_t rs, u32 soff) {
  u32x2 v;
  asm volatile("buffer_load_dwordx2 %0, %1, %2, %3 offen sc1" : "=v"(v) : "v"(voff), "s"(rs), "s"(soff) : "memory");
  return v;
}
DI u32 ld32_sc1(u32 voff, rsrc_t rs, u32 soff) {
  u32 v;
  asm volatile("buffer_load_dword %0, %1, %2, %3 offen sc1" : "=v"(v) : "v"(voff), "s"(rs), "s"(soff) : "memory");
  return v;
}
DI u32 touch(u32 voff, rsrc_t rs) {
  u32 v;
  asm volatile("buffer_load_dword %0, %1, %2, 0 offen" : "=v"(v) : "v"(voff), "s"(rs) : "memory");
  return v;
}
DI void st128(u32x4 v, rsrc_t rs, u32 voff, u32 soff) {
  asm volatile("buffer_store_dwordx4 %0, %1, %2, %3 offen sc1\n\ts_nop 1" ::"v"(v), "v"(voff), "s"(rs), "s"(soff) : "memory");
}
constexpr int clampvm(int n) { return n > 63 ? 63 : n; }
template <int N> DI void wait_vm() { static_assert(N >= 0, ""); asm volatile("s_waitcnt vmcnt(%0)" ::"n"(clampvm(N)) : "memory"); }
template <int N> DI void sync_vm() { static_assert(N >= 0, ""); asm volatile("s_waitcnt vmcnt(%0)\n\ts_barrier" ::"n"(clampvm(N)) : "memory"); }
template <class T> DI void pin(T& v) { asm volatile("" : "+v"(v)); }
template <int I, int N, class Fn> DI void static_for(Fn&& f) {
  if constexpr (I < N) { f(IC<I>{}); static_for<I + 1, N>(f); }
}

DI u32x4 trfrag(LAS char* p) {
  s16x4 lo = __builtin_amdgcn_ds_read_tr16_b64_v4i16((LAS s16x4*)p);
  s16x4 hi = __builtin_amdgcn_ds_read_tr16_b64_v4i16((LAS s16x4*)(p + 512));
  u32x2 a = __builtin_bit_cast(u32x2, lo), b = __builtin_bit_cast(u32x2, hi);
  return u32x4{a.x, a.y, b.x, b.y};
}
DI f32x16 mfma(u32x4 a, u32x4 b, f32x16 c) {
  return __builtin_amdgcn_mfma_f32_32x32x16_bf16(__builtin_bit_cast(bf16x8, a), __builtin_bit_cast(bf16x8, b), c, 0, 0, 0);
}
DI void mfma_at(f32x16& c, u32x4 a, u32x4 b) { c = mfma(a, b, c); asm volatile("" : "+v"(c)); }
DI u32 pk(float a, float b) { bf16x2 c; c[0] = (__bf16)a; c[1] = (__bf16)b; return __builtin_bit_cast(u32, c); }
DI u32x4 pk8(const f32x16& v, int s) { return u32x4{pk(v[8 * s], v[8 * s + 1]), pk(v[8 * s + 2], v[8 * s + 3]), pk(v[8 * s + 4], v[8 * s + 5]), pk(v[8 * s + 6], v[8 * s + 7])}; }
DI float sigmoidf(float z) { return __builtin_amdgcn_rcpf(1.f + __builtin_amdgcn_exp2f(z * -1.4426950408889634f)); }
DI float glu(float g, float u) { return u * sigmoidf(g); }
DI float xhalf_max(float v) { auto r = __builtin_amdgcn_permlane32_swap(__float_as_uint(v), __float_as_uint(v), false, false); return fmaxf(__uint_as_float(r[0]), __uint_as_float(r[1])); }
DI float xhalf_sum(float v) { auto r = __builtin_amdgcn_permlane32_swap(__float_as_uint(v), __float_as_uint(v), false, false); return __uint_as_float(r[0]) + __uint_as_float(r[1]); }

DI void spin(const char* ws, u32 off, bool need, u64 tag) {
  const GAS u64* p = (const GAS u64*)(ws + off);
#pragma unroll 1
  for (int i = 0; i < SPIN_CAP; ++i) {
    u64 v = __hip_atomic_load(p, __ATOMIC_RELAXED, __HIP_MEMORY_SCOPE_AGENT);
    if (__all(!need || v == tag)) break;
    __builtin_amdgcn_s_sleep(2);
  }
  asm volatile("" ::: "memory");
}
DI bool match(u32x2 v, bool need, u64 tag) { return __all(!need || (v.x == (u32)tag && v.y == (u32)(tag >> 32))); }
DI u64 tag_of(u32x2 ev) {
  u64 e = ((u64)__builtin_amdgcn_readfirstlane(ev.y) << 32) | (u32)__builtin_amdgcn_readfirstlane(ev.x);
  return e * 6364136223846793005ull + 1442695040888963407ull;
}
DI void put_tag(u64 tag, rsrc_t rs, u32 voff, u32 soff) { __builtin_amdgcn_raw_buffer_store_b64(u32x2{(u32)tag, (u32)(tag >> 32)}, rs, voff, soff, SC1); }

constexpr char HEAD[] = "KKKKKKK";
constexpr int BARS[] = {0, 2, 4, 6, 8, 10, 12, 16, 20, 34, 46, 50, 58, 62, 68, 76, 80, 86, 96};
constexpr int PRO_DMA = 9;
constexpr int HEAD_N = 14, HEAD_RATE = 24;
constexpr int EP1 = 2;
constexpr int U_KVPUB = 12, U_H1PUB = 40, U_H2PUB = 57, U_POLL1 = 52, U_CHK1 = 56, U_POLL2 = 64, U_CHK2 = 68, U_POLL3 = 70, U_CHK3 = 76;
constexpr int NV = 105, U0 = 14, IT_OMG = 28;
constexpr int imin(int a, int b) { return a < b ? a : b; }
constexpr int item_of(int i) { return i < IT_OMG ? i : i + 1; }
constexpr int ld_rel(int k) { return k < 6 ? 0 : 1 + (k - 6) / 2; }
constexpr int ld_iter(int it) { return U0 + (it < 14 ? U_CHK1 + ld_rel(it) : (it < 28 ? U_CHK2 + ld_rel(it - 14) : U_CHK3 + (it - 28) / 2)); }
constexpr int TM = 8, U_H2A = 56 - TM / 2 + 2;
constexpr bool tm_it(int u) { return u >= 56 - TM && u < 56; }
constexpr int g_tile(int u, int i) { return tm_it(u) ? (u - (56 - TM)) / (TM / 2) : i; }
constexpr int g_ks(int u, int i) { return tm_it(u) ? 56 - TM + 2 * ((u - (56 - TM)) % (TM / 2)) + i : u; }
constexpr int UE0 = 32, NE = (56 - TM - UE0) / 2;
struct Sched {
  int el[NV + 2] = {}, ej[NV + 2] = {};
  int nhead = 0, vkvst = 0, vg1 = 0;
  int xl[NV + 2] = {}, wl[NV + 2] = {};
  int kind[NV + 2] = {}, idx[NV + 2] = {}, lo[NV + 2] = {}, hi[NV + 2] = {};
  int pk[84] = {}, pi[84] = {}, pj[84] = {};
  bool bar[NV] = {};
  int cum[NV + 1] = {}, base[NV + 1] = {}, nst[NV] = {}, ldlo[NV + 1] = {}, cnt[NV] = {}, pdma[84] = {}, pld[36] = {}, freed[NV] = {};
  int cnt0 = 0; bool ok = true;
  constexpr Sched() {
    int v = 0, n = 0, k = 0, g = 0, ne = 0;
    for (int e = 0; e < NV + 2; ++e) el[e] = -1;
    int nx = 0;
    auto add = [&](int kd, int st, int nit, int nw) {
      const int first = n; int x = -1;
      if (st < 7 && st == nx) { x = n; for (int e = 0; e < 2; ++e) { pk[n] = 0; pi[n] = st; pj[n] = e; ++n; } ++nx; }
      for (int e = 0; e < nit; ++e) { kind[v] = kd; idx[v] = nit * st + e; lo[v] = first; hi[v] = n + nw - 1; xl[v] = x; wl[v] = n; ++v; }
      for (int e = 0; e < nw; ++e) { pk[n] = kd; pi[n] = st; pj[n] = kd ? e : 2; ++n; }
      if (kd == 1 && 4 * st >= UE0 && 4 * st < 56 - TM) {
        for (int e = 0; e < 4; ++e) { el[v - 4 + e] = hi[v - 4 + e] = n + e / 2; ej[v - 4 + e] = 2 * ne + e; }
        for (int e = 0; e < 2; ++e) { pk[n] = 2; pi[n] = ne++; ++n; }
      }
    };
    for (int i = 0; HEAD[i]; ++i) { if (HEAD[i] == 'K') { add(0, k++, 2, 1); nhead = v; } else { add(1, g++, 4, 2); if (g == 7) vg1 = v; } }
    if (k != 7 || g > 7) ok = false;
    vkvst = nhead + 1;
    while (g < 14) { add(1, g++, 4, 2); if (g == 7) vg1 = v; }
    if (ne != NE) ok = false;
    for (int i = 0; i < 35; ++i) { kind[v] = 2; idx[v] = 56 + i; lo[v] = wl[v] = n; if (i < NE) hi[v] = n - 1; else { hi[v] = n; pk[n] = 2; pi[n] = i; ++n; } ++v; }
    const int wa = wl[U0 + 56 - TM];
    for (int u = 56 - TM; u < 56; ++u) {
      wl[U0 + u] = wa + 2 * ((g_ks(u, 0) - (56 - TM)) / 4);
      lo[U0 + u] = g_tile(u, 0) == 0 ? wa : wl[U0 + u];
      hi[U0 + u] = g_tile(u, 0) == 0 ? wl[U0 + u] + 1 : wa + TM / 2 - 1;
    }
    kind[NV] = kind[NV + 1] = 3; hi[NV] = hi[NV + 1] = 83;
    for (int e = 0; e + 1 < NV; ++e) if (lo[e] > lo[e + 1] || hi[e] > hi[e + 1]) ok = false;
    for (int e : BARS) bar[e] = true;
    bar[U0 + U_KVPUB] = bar[U0 + U_H1PUB] = bar[U0 + U_H2PUB] = bar[U0 + U_CHK2] = bar[U0 + U_CHK3] = true;
    if (U0 + U_KVPUB <= vkvst || U0 + U_H1PUB <= vg1 + 2 * EP1 || bar[U0 + U_CHK1]) ok = false;
    int fr = 0, cc = PRO_DMA, cr = 0, it = 0;
    cum[0] = cc; base[0] = PRO_DMA + 2;
    for (n = 0; n < PRO_DMA; ++n) pdma[n] = n;
    for (v = 0; v < NV; ++v) {
      const int u = v - U0;
      if (bar[v] || u == U_CHK1) fr = lo[v];
      freed[v] = fr;
      nst[v] = 3 * (v == vkvst) + (v == vg1 + EP1) + (v == vg1 + 2 * EP1) + (u == U_H2A) + (u == 56) + (u == U_POLL1) + (u == U_POLL2) + (u == U_POLL3);
      ldlo[v] = it;
      while (it < 36 && ld_iter(it) == v) { pld[it] = base[v] + nst[v] + (it - ldlo[v]); ++it; }
      cr += v < HEAD_N ? HEAD_RATE : 16;
      const int t = imin(imin(cc + imin(cr / 16, 2), fr + 20), 84);
      cr -= 16 * (t - cc); if (cr > 16) cr = 16;
      for (n = cc; n < t; ++n) pdma[n] = base[v] + nst[v] + (it - ldlo[v]) + (n - cc);
      base[v + 1] = base[v] + nst[v] + (it - ldlo[v]) + (t - cc);
      cc = t; cum[v + 1] = cc;
    }
    ldlo[NV] = it;
    if (it != 36 || cc != 84) ok = false;
    for (v = -1; v < NV; ++v) {
      if (v >= 0 && !bar[v]) continue;
      int nb = v + 1; while (nb < NV && !bar[nb]) ++nb;
      const int need = hi[nb + 1], at = v < 0 ? 0 : v;
      if (need >= cum[at]) ok = false;
      int c = base[at] - pdma[need] - 1;
      const int u = v - U0;
      if (u == U_KVPUB) c = imin(c, base[v] - (base[vkvst] + nst[vkvst]));
      if (u == U_H1PUB) c = imin(c, base[v] - (base[vg1 + 2 * EP1] + nst[vg1 + 2 * EP1]));
      if (u == U_H2PUB) c = imin(c, base[v] - (base[U0 + 56] + nst[U0 + 56]));
      if (v < 0) cnt0 = c; else cnt[v] = c;
    }
    for (int i = 1; i < 35; ++i) if (ld_iter(item_of(i)) >= U0 + 56 + i) ok = false;
  }
};
constexpr Sched SA{};
static_assert(SA.ok, "schedule: a piece or operand is used before it is requested, or HEAD is malformed");
static_assert(U_CHK1 == 56 && U_POLL1 < U_CHK1 && U_CHK1 < U_POLL2 && U_POLL2 < U_CHK2 && U_CHK2 < U_POLL3 && U_POLL3 < U_CHK3, "");
static_assert(U_KVPUB < U_H1PUB && U_H1PUB < U_CHK1 && U_H2PUB > 56 && U_H2PUB < U_CHK2, "a workgroup publishes a class before it waits for that class");
static_assert(SA.freed[NV - 1] >= 72, "the reduction scratch (slots 4..11 = pieces 64..71) must be free at the end");

struct Ctx {
  LAS char* sm; u32 m0w;
  rsrc_t rx, rqkv, rwg, rwu, rwd, rwo, rws;
  const char* ws;
  u32 pf;
  u32 kc[7], seq, seq2;
  u32 vx, vkv, vg, vu, vw2;
};

template <int I> DI u32 blk2(const Ctx& c) {
  return I < 28 ? (I / 14) * 14 + 2 * ((c.seq2 >> (4 * ((I % 14) / 2))) & 7) + (I & 1) : 28 + ((c.seq2 >> (4 * (I < 28 ? 0 : I - 28))) & 7);
}
template <int N> DI void issueA(const Ctx& c) {
  constexpr u32 dst = (N % 20) * SLOT;
  constexpr int k = SA.pk[N], st = SA.pi[N], j = SA.pj[N];
  if constexpr (k == 0) {
    if constexpr (j < 2) dmam<dst, 128>(c.m0w, c.vx + j * 896, c.rx, c.kc[st]);
    else dmam<dst, 64 * 2688>(c.m0w, c.vkv, c.rqkv, c.kc[st]);
  } else if constexpr (k == 1) {
    if constexpr (j == 0) dmam<dst, 64 * 3584>(c.m0w, c.vg + (st / 7) * 1792, c.rwg, c.kc[st % 7]);
    else dmam<dst, 64 * 3584>(c.m0w, c.vu + (st / 7) * 1792, c.rwu, c.kc[st % 7]);
  } else if constexpr (st < 28) dmam<dst, 64 * 896>(c.m0w, c.vw2, c.rwd, blk2<st>(c));
  else dmam<dst, 64 * 896>(c.m0w, c.vw2, c.rwo, blk2<st>(c) - 28);
}

DI void type_a(Ctx& c, int w, int lane, int mt, int role, int R2, int n2, void* out) {
  const int r = lane & 31, hi = lane >> 5, blk = (lane >> 4) & 1, q = (lane & 15) >> 2, p = lane & 3;
  const int sp = ((p & 1) << 1) | (p >> 1);
  const int rg = w & 3, cg = w >> 2, T = mt * 4 + rg;
  const bool isv = role >= 7;
  LAS char* sm = c.sm;
  static_for<2, PRO_DMA>([&](auto N) { issueA<N>(c); });
  u32 tg;
  { const int run = (lane * 37) >> 8;
    tg = touch(((w == 1 || w == 2) && lane < TN) ? ((mt >> 2) * 112 + (n2 * 4 + (mt & 3)) * 4 + (run & 3)) * 3584 + ((run >> 2) * 14 + (role - n2) + lane - 7 * run) * 128 : OOB, w == 1 ? c.rwg : c.rwu); }
  put_tag(0, c.rws, (w == 0 && lane < 3) ? lane * 4096 + role * 8 : OOB, WS_FLAG + mt * 256);
  asm volatile("" ::: "memory");
  const int R = 32 * (rg & 1) + r, xm = (R >> 1) & 7;
  int xa[4];
#pragma unroll
  for (int s = 0; s < 4; ++s) xa[s] = (rg >> 1) * SLOT + R * 128 + (((2 * s + hi) ^ xm) << 4);
  const int rowb = (8 * hi + q) * 128, q2 = q & 2;
  const int akv = rowb + (((2 * cg + blk) ^ q2) << 5) + (isv ? p : sp) * 8;
  int akvw[3];
#pragma unroll
  for (int k = 0; k < 3; ++k) { akvw[k] = akv + k * 65536; pin(akvw[k]); }
  int agu[2];
#pragma unroll
  for (int i = 0; i < 2; ++i) agu[i] = blk * SLOT + rowb + (((2 * cg + i) ^ q2 ^ blk) << 5) + sp * 8;
  int aguw[2][3];
#pragma unroll
  for (int i = 0; i < 2; ++i)
#pragma unroll
    for (int k = 0; k < 3; ++k) { aguw[i][k] = agu[i] + k * 65536; pin(aguw[i][k]); }
  const int rt = w & 1, kq = w >> 1;
  int aw2[2], aw2w[2][3];
#pragma unroll
  for (int i = 0; i < 2; ++i) aw2[i] = kq * 2048 + rowb + (((2 * i + blk) ^ q2) << 5) + p * 8;
  const int T2 = (R2 >> 1) * 4 + 2 * rt + (R2 & 1);

  u32x4 xf[28], wf[2][2], af[35], hp[2], wr[2 * NE];
  f32x16 akvacc = {}, g1[2] = {}, g2[2] = {}, ah[2] = {}, ao[2] = {};
  u32x2 ev, pollv; u32 omg; u64 tag;
  const u32 lane16 = lane * 16;
  const u32 poll_off = WS_FLAG + (R2 >> 1) * 256 + (lane & 15) * 8;
  const bool need_h = (lane & 15) < 14;
  const u32 a2_off = WS_A2 + T2 * A2_T + kq * 1024 + lane16;
  const int isvi = (6 - role) >> 31;
  u32 kv_off = ((WS_V + hi * 7168 + ((role - 7) * 64 + cg * 32 + r) * 16) & isvi) + ((WS_K + (role * 8 + cg * 4) * 512 + lane16) & ~isvi) + T * KV_T;
  pin(kv_off);
  const u32 kv_step = 1024 + (13312 & isvi);
  const u32 h_off = WS_A2 + T * A2_T + (role * 4 + 2 * cg) * 1024 + lane16;

  auto read_w = [&](auto V, auto I) {
    constexpr int v = V, i = I, k = SA.kind[v], u = SA.idx[v], sl = SA.wl[v] % 20;
    if constexpr (k < 2 && SA.xl[v] >= 0 && (k == 0 || i == 0)) {
      constexpr int ks = k == 0 ? 2 * u + i : u, xs = SA.xl[v] % 20;
      const int wrap = xs == 19 ? (rg >> 1) * (-20 * SLOT) : 0;
      xf[ks] = *(LAS u32x4*)(sm + xa[ks % 4] + wrap + xs * SLOT);
    }
    if constexpr (k == 0) {
      wf[v & 1][i] = trfrag(sm + akvw[sl / 8] + (sl % 8) * SLOT + ((2 * u + i) % 4) * 2048);
    } else if constexpr (k == 1) {
      const int wrap = sl == 19 ? blk * (-20 * SLOT) : 0;
      wf[v & 1][i] = trfrag(sm + aguw[g_tile(u, i)][sl / 8] + wrap + (sl % 8) * SLOT + (g_ks(u, i) % 4) * 2048);
      if constexpr (u == UE0 - 2) {
#pragma unroll
        for (int e = 0; e < 3; ++e) { aw2w[i][e] = aw2[i] + e * 65536; pin(aw2w[i][e]); }
      }
      if constexpr (i == 0 && SA.el[v] >= 0) { constexpr int es = SA.el[v] % 20, j = SA.ej[v]; wr[j] = trfrag(sm + aw2w[j & 1][es / 8] + (es % 8) * SLOT); }
    } else if constexpr (k == 2 && u - 56 >= NE) {
      wf[v & 1][i] = trfrag(sm + aw2w[i][sl / 8] + (sl % 8) * SLOT);
    }
  };
  auto publish = [&](int cls) {
    if (w == 0) put_tag(tag, c.rws, lane == 0 ? role * 8 : OOB, WS_FLAG + cls * 4096 + mt * 256);
  };
  auto load_item = [&](auto IT) {
    constexpr int it = IT;
    if constexpr (it == IT_OMG) omg = ld32_sc1(r * 4, c.rws, WS_OMG + T2 * 128);
    else { constexpr int i = it < IT_OMG ? it : it - 1; af[i] = ld128_sc1(a2_off, c.rws, blk2<i>(c) * 4096); }
  };
  auto check = [&](auto CNT, int cls, bool need) {
    wait_vm<CNT>();
    pin(pollv);
    if (w == 0 && !match(pollv, need, tag)) spin(c.ws, poll_off + cls * 4096, need, tag);
  };

  sync_vm<SA.cnt0>();
  pin(c.pf);
  static_for<0, 4>([&](auto S) { read_w(IC<S / 2>{}, IC<S % 2>{}); });
  SCHED();
  auto h_pair = [&](const f32x16& gu, int k) { u32 hv = pk(glu(gu[2 * k], gu[8 + 2 * k]), glu(gu[2 * k + 1], gu[9 + 2 * k])); pin(hv); return hv; };
  auto iter = [&](auto V, auto ISV) {
    constexpr int v = V, b = v & 1, kd = SA.kind[v], ui = SA.idx[v], u = v - U0;
    constexpr bool nbar = v + 1 < NV && SA.bar[v + 1 < NV ? v + 1 : v];
    constexpr int nld = SA.ldlo[v + 1] - SA.ldlo[v], nd = SA.cum[v + 1] - SA.cum[v];
    if constexpr (SA.bar[v]) sync_vm<SA.cnt[v]>();
    if constexpr (u == 56) {
#pragma unroll
      for (int j = 0; j < 8; j += 2) hp[1][j / 2] = pk(glu(g2[1][j], g2[1][8 + j]), glu(g2[1][j + 1], g2[1][9 + j]));
      st128(hp[1], c.rws, h_off, 57 * 1024);
      SCHED();
      check(IC<SA.base[v] + SA.nst[v] - SA.base[U0 + U_POLL1] - 1>{}, 1, need_h);
      asm volatile("s_barrier" ::: "memory");
      static_for<SA.ldlo[v], SA.ldlo[v + 1]>(load_item);
    }
    if constexpr (u >= 56) {
      wait_vm<SA.base[v] + (u == 56 ? SA.nst[v] + nld : 0) - SA.pld[item_of(u - 56)] - 1>();
      pin(af[u - 56]);
    }
    SCHED();
    static_for<0, 2>([&](auto I) {
      constexpr int i = I;
      if constexpr (kd == 0) { if constexpr (ISV) mfma_at(akvacc, xf[2 * ui + i], wf[b][i]); else mfma_at(akvacc, wf[b][i], xf[2 * ui + i]); }
      else if constexpr (ui < 28) mfma_at(g1[i], wf[b][i], xf[ui]);
      else if constexpr (ui < 56) mfma_at(g2[g_tile(ui, i)], wf[b][i], xf[g_ks(ui, i) - 28]);
      else if constexpr (ui - 56 < NE) mfma_at(ah[i], wr[2 * (ui - 56) + i], af[ui - 56]);
      else if constexpr (ui < 84) mfma_at(ah[i], wf[b][i], af[ui - 56]);
      else mfma_at(ao[i], wf[b][i], af[ui - 56]);
      if constexpr (i == 0 && SA.el[v] >= 0) pin(wr[SA.ej[v] + i]);
      SCHED();
      if constexpr (i == 0 && v > 0 && SA.bar[v]) read_w(IC<v + 1>{}, IC<1>{});
      if constexpr (i == 0 || !nbar) read_w(IC<v + 2>{}, I);
      if constexpr (i == 0) {
        if constexpr (u == U_KVPUB) { pin(ev); pin(tg); tag = tag_of(ev); publish(0); }
        if constexpr (u == U_H1PUB) publish(1);
        if constexpr (u == U_H2PUB) publish(2);
        if constexpr (v == SA.vkvst) ev = ld64_sc1(w == 0 ? WS_E : OOB, c.rws, 0);
        if constexpr (u == U_POLL1) pollv = ld64_sc1(w == 0 ? poll_off : OOB, c.rws, 4096);
        if constexpr (u == U_POLL2 || u == U_POLL3) pollv = ld64_sc1(w == 0 ? poll_off : OOB, c.rws, 8192);
        if constexpr (v == SA.vkvst) { st128(pk8(akvacc, 0), c.rws, kv_off, 0); st128(pk8(akvacc, 1), c.rws, kv_off, kv_step); }
        if constexpr (v == SA.vg1 + EP1) st128(hp[0], c.rws, h_off, 0);
        if constexpr (v == SA.vg1 + 2 * EP1) st128(hp[1], c.rws, h_off, 1024);
        if constexpr (u == U_H2A) st128(hp[0], c.rws, h_off, 56 * 1024);
        if constexpr (u != 56) static_for<SA.ldlo[v], SA.ldlo[v + 1]>(load_item);
      }
      if constexpr (v >= SA.vg1 && v < SA.vg1 + 2 * EP1) {
        constexpr int t = (v - SA.vg1) / EP1, per = 4 / EP1, k0 = ((v - SA.vg1) % EP1) * per;
        constexpr int ka = nbar ? (i ? per : 0) : (i * per + 1) / 2, kb = nbar ? per : ((i + 1) * per + 1) / 2;
#pragma unroll
        for (int k = k0 + ka; k < k0 + kb; ++k) hp[t][k] = h_pair(g1[t], k);
      }
      if constexpr (u >= U_H2A - 2 && u < U_H2A) hp[0][2 * (u - (U_H2A - 2)) + i] = h_pair(g2[0], 2 * (u - (U_H2A - 2)) + i);
      if constexpr (i == 0 && nd == 2) issueA<SA.cum[v]>(c);
      if constexpr (i == (nbar ? 0 : 1) && nd >= 1) issueA<SA.cum[v + 1] - 1>(c);
      if constexpr (i == 0 && u + 1 == U_CHK2) check(IC<SA.base[v + 1] - SA.base[U0 + U_POLL2] - 1 + i>{}, 2, need_h);
      if constexpr (i == 0 && u + 1 == U_CHK3) check(IC<SA.base[v + 1] - SA.base[U0 + U_POLL3] - 1 + i>{}, 2, !need_h);
      SCHED();
    });
  };
  if (isv) static_for<0, SA.nhead>([&](auto V) { iter(V, IC<1>{}); });
  else static_for<0, SA.nhead>([&](auto V) { iter(V, IC<0>{}); });
  static_for<SA.nhead, NV>([&](auto V) { iter(V, IC<0>{}); });
  pin(omg);
  const float om = __uint_as_float(omg);
  LAS char* red = sm + 4 * SLOT;
#pragma unroll
  for (int t = 0; t < 2; ++t)
#pragma unroll
    for (int a = 0; a < 4; ++a) {
      f32x4 v;
#pragma unroll
      for (int e = 0; e < 4; ++e) v[e] = ah[t][4 * a + e] * om + ao[t][4 * a + e];
      *(LAS f32x4*)(red + kq * 16384 + (2 * r + rt) * 256 + (((8 * t + 2 * a + hi) ^ (r & 15)) << 4)) = v;
    }
  asm volatile("s_waitcnt lgkmcnt(0)\n\ts_barrier" ::: "memory");
  {
    const int row = w * 8 + (lane >> 3), c8 = lane & 7, m = (row >> 1) & 15;
    f32x4 s0 = {}, s1 = {};
#pragma unroll
    for (int k = 0; k < 4; ++k) {
      s0 += *(LAS f32x4*)(red + k * 16384 + row * 256 + (((2 * c8) ^ m) << 4));
      s1 += *(LAS f32x4*)(red + k * 16384 + row * 256 + (((2 * c8 + 1) ^ m) << 4));
    }
    const u32x4 o = {pk(s0[0], s0[1]), pk(s0[2], s0[3]), pk(s1[0], s1[1]), pk(s1[2], s1[3])};
    __builtin_amdgcn_raw_buffer_store_b128(o, mk(out, 2048 * 896), (R2 * 64 + row) * 896 + n2 * 128 + c8 * 16, 0, SC1);
  }
}

constexpr int PRO_B = 12, GB_TOUCH = 18, GB_POLL = 25;
constexpr int cumB(int g) {
  int cc = PRO_B;
  for (int j = 0; j <= g; ++j) cc = imin(imin(cc + 2, 6 * ((j + 1) / 4) + 18), 42);
  return cc;
}
static_assert(cumB(GB_TOUCH - 1) == 42 && GB_POLL > 23, "both sit behind the last ring piece; the poll behind the last counted barrier");
template <int N> DI void issueB(const Ctx& c) {
  constexpr int kc = N / 6, j = N % 6;
  constexpr u32 dst = (N % 18) * SLOT;
  if constexpr (j < 2) dmam<dst, 128>(c.m0w, c.vx + j * 896, c.rx, c.kc[kc]);
  else dmam<dst, 64 * 2688>(c.m0w, c.vkv + (j - 2) * 128, c.rqkv, c.kc[kc]);
}

DI void type_b(Ctx& c, int w, int lane, int mt, int hf, const void* gw) {
  const int r = lane & 31, hi = lane >> 5, blk = (lane >> 4) & 1, q = (lane & 15) >> 2, p = lane & 3;
  const int sp = ((p & 1) << 1) | (p >> 1);
  const int rg = w & 3, cg = w >> 2, T = mt * 4 + rg;
  LAS char* sm = c.sm;
  if (w == 0) dma<18 * SLOT>(c.m0w, ((c.seq >> (4 * (lane >> 3))) & 7) * 128 + (lane & 7) * 16, mk(gw, 896), 0);
  static_for<2, PRO_B>([&](auto N) { issueB<N>(c); });
  if (w == 0) put_tag(0, c.rws, lane == 0 ? (14 + hf) * 8 : OOB, WS_FLAG + 8192 + mt * 256);
  const int R = 32 * (rg & 1) + r, xm = (R >> 1) & 7;
  int xa[4], aq[4];
#pragma unroll
  for (int s = 0; s < 4; ++s) xa[s] = (rg >> 1) * SLOT + R * 128 + (((2 * s + hi) ^ xm) << 4);
  const int h0 = 8 * hf + 4 * cg;
#pragma unroll
  for (int i = 0; i < 4; ++i) {
    const int col = 28 * (h0 + i) - 4 * (i & 1) - 192 * hf + 16 * blk + 4 * sp;
    aq[i] = (2 + (col >> 6)) * SLOT + (8 * hi + q) * 128 + (((col & 63) * 2) ^ ((q & 2) << 5));
  }
  const bool first = (mt == 0) && !(rg & 1);
  const int Tp = first ? T : ((rg & 1) ? T - 1 : T - 3);
  const u32 lane16 = lane * 16, lo16 = hi ? OOB : lane16;
  const u32 ob = WS_A2 + T * A2_T + (1792 + 28 * h0) * 64;
  const u32 omg_off = (hf == 0 && cg == 0 && hi == 0) ? r * 4 : OOB;
  const u32 poll_off = WS_FLAG + ((lane & 16) && mt > 0 ? mt - 1 : mt) * 256 + (lane & 15) * 8;
  const bool poll_need = (lane & 15) < 14;
  u32x4 xf[2], gf[2], wf[2][4];
  u32x4 kf[4][2], kp[2], vf[4][2], vp[4];
  f32x16 qa[4] = {};
  u32x2 ev, pollv;
  u32 tw;
  const int sr = (r & 19) | ((r & 4) << 1) | ((r & 8) >> 1);
  const u32 kp_off = (28 * (r >> 3) - 4 * ((r >> 3) & 1)) * 64 + (hi * 32 + 24 + (r & 7)) * 16;
  const u32 v_off = hi * 7168 + sr * 16;
  auto load_kv = [&](auto IT) {
    constexpr int it = IT;
    if constexpr (it < 2) kp[it] = ld128_sc1(kp_off, c.rws, WS_K + Tp * KV_T + 28 * h0 * 64 + it * 1024);
    else if constexpr (it < 10) {
      constexpr int i = (it - 2) / 2, k = it & 1;
      kf[i][k] = ld128_sc1(lane16, c.rws, WS_K + T * KV_T + (28 * (h0 + i) - 4 * (i & 1)) * 64 + k * 1024);
    } else {
      constexpr int i = (it - 10) / 3, k = (it - 10) % 3;
      const int wh = 28 * (h0 + i) - 4 * (i & 1);
      if constexpr (k == 0) vp[i] = ld128_sc1(v_off, c.rws, WS_V + Tp * KV_T + 14336 + wh * 16);
      else vf[i][k - 1] = ld128_sc1(v_off, c.rws, WS_V + T * KV_T + (k - 1) * 14336 + wh * 16);
    }
  };
  auto read_x = [&](auto G) {
    constexpr int g = G;
    if constexpr (g < 28) {
      xf[g & 1] = *(LAS u32x4*)(sm + xa[g % 4] + ((6 * (g / 4)) % 18) * SLOT);
      gf[g & 1] = *(LAS u32x4*)(sm + 18 * SLOT + g * 32 + hi * 16);
    }
  };
  auto read_w = [&](auto G, auto I) {
    constexpr int g = G;
    if constexpr (g < 28) wf[g & 1][I] = trfrag(sm + aq[I] + ((6 * (g / 4)) % 18) * SLOT + (g % 4) * 2048);
  };
  sync_vm<PRO_B - 6>();
  pin(c.pf);
  read_x(IC<0>{});
  static_for<0, 4>([&](auto I) { read_w(IC<0>{}, I); });
  SCHED();
  static_for<0, 28>([&](auto G) {
    constexpr int g = G, b = g & 1;
    if constexpr (g % 4 == 3 && g < 27) { asm volatile("s_waitcnt lgkmcnt(0)" ::: "memory"); sync_vm<cumB(g - 1) - 6 * (g / 4 + 2) + (g > GB_TOUCH)>(); }
    if constexpr (g == GB_POLL) { ev = ld64_sc1(w == 0 ? WS_E : OOB, c.rws, 0); pollv = ld64_sc1(w == 0 ? poll_off : OOB, c.rws, 0); }
    if constexpr (g == GB_TOUCH) {
      const int gi = (mt * 2 + hf) * 8 + w;
      tw = touch(((gi < 196 ? gi : gi - 196) * 64 + lane) * 128, gi < 196 ? c.rwd : c.rwo);
    }
    SCHED();
    static_for<0, 4>([&](auto I) {
      constexpr int i = I;
      mfma_at(qa[i], wf[b][i], xf[b]);
      SCHED();
      if constexpr (i == 0) read_x(IC<g + 1>{});
      read_w(IC<g + 1>{}, I);
      if constexpr (i < 2) {
        wf[b][2][2 * i] = r == 28 ? gf[b][2 * i] : wf[b][2][2 * i];
        wf[b][2][2 * i + 1] = r == 28 ? gf[b][2 * i + 1] : wf[b][2][2 * i + 1];
        pin(wf[b][2]);
      }
      if constexpr (i >= 2 && cumB(g - 1) + i - 2 < cumB(g)) issueB<cumB(g - 1) + i - 2>(c);
      SCHED();
    });
  });
  wait_vm<0>();
  pin(pollv); pin(ev); pin(tw);
  const u64 tag = tag_of(ev);
  if (w == 0 && !match(pollv, poll_need, tag)) spin(c.ws, poll_off, poll_need, tag);
  asm volatile("s_barrier" ::: "memory");
  static_for<0, 6>(load_kv);
  SCHED();
  const float z = __uint_as_float(__builtin_amdgcn_permlane32_swap(__float_as_uint(qa[2][12]), __float_as_uint(qa[2][12]), false, false)[1]);
  const float gate = sigmoidf(z);
  const u32 omg = __float_as_uint(sigmoidf(-z));
  int ab[4];
  u32 msk[5][2];
#pragma unroll
  for (int b = 0; b < 4; ++b) ab[b] = (r >> 3) - ((r & 7) < 4 * hi + b ? 1 : 0);
#pragma unroll
  for (int a = 0; a < 5; ++a)
#pragma unroll
    for (int e = 0; e < 2; ++e) msk[a][e] = (ab[2 * e] == a - 1 ? 0xFFFFu : 0u) | (ab[2 * e + 1] == a - 1 ? 0xFFFF0000u : 0u);
  const float csc = 0.18898223650461362f * 1.4426950408889634f;
  u32 p01[2], p23[2];
  u32x4 qf[4][2];
  static_for<0, 4>([&](auto I) {
    constexpr int i = I;
    qf[i][0] = pk8(qa[i], 0); qf[i][1] = pk8(qa[i], 1);
    if (i & 1) { qf[i][0][0] = hi ? qf[i][0][0] : 0u; qf[i][0][1] = hi ? qf[i][0][1] : 0u; }
    else       { qf[i][1][2] = hi ? 0u : qf[i][1][2]; qf[i][1][3] = hi ? 0u : qf[i][1][3]; }
    pin(qf[i][0]); pin(qf[i][1]);
  });
#pragma unroll
  for (int a = 0; a < 5; ++a) { pin(msk[a][0]); pin(msk[a][1]); }
  __builtin_amdgcn_raw_buffer_store_b32(omg, c.rws, omg_off, WS_OMG + T * 128, SC1);
  asm volatile("" ::: "memory");
  f32x16 so[2], sv[2], oa[2];
  auto scores = [&](auto J) {
    static_for<0, 2>([&](auto E) {
      constexpr int i = 2 * J + E;
      pin(kf[i][0]); pin(kf[i][1]);
      so[E] = mfma(kf[i][0], qf[i][0], f32x16{}); sv[E] = mfma(kp[0], qf[i][0], f32x16{});
      mfma_at(so[E], kf[i][1], qf[i][1]); mfma_at(sv[E], kp[1], qf[i][1]);
    });
  };
  auto softmax = [&](auto J) {
    static_for<0, 2>([&](auto E) {
      constexpr int i = 2 * J + E;
      float v[4];
#pragma unroll
      for (int b = 0; b < 4; ++b) {
        float t = first ? -1e30f : sv[E][4 * i + b];
#pragma unroll
        for (int a = 0; a < 4; ++a) t = ab[b] == a ? so[E][4 * a + b] : t;
        v[b] = t;
      }
      const float mx = xhalf_max(fmaxf(fmaxf(v[0], v[1]), fmaxf(v[2], v[3])));
#pragma unroll
      for (int b = 0; b < 4; ++b) v[b] = __builtin_amdgcn_exp2f((v[b] - mx) * csc);
      const float sc = gate * __builtin_amdgcn_rcpf(xhalf_sum((v[0] + v[1]) + (v[2] + v[3])));
      p01[E] = pk(v[0] * sc, v[1] * sc); p23[E] = pk(v[2] * sc, v[3] * sc);
    });
    pin(p01[0]); pin(p23[0]); pin(p01[1]); pin(p23[1]);
  };
  auto pv = [&](auto J) {
    static_for<0, 2>([&](auto E) {
      constexpr int i = 2 * J + E;
      pin(vp[i]); pin(vf[i][0]); pin(vf[i][1]);
      oa[E] = mfma(vp[i], u32x4{0u, 0u, p01[E] & msk[0][0], p23[E] & msk[0][1]}, f32x16{});
      oa[E] = mfma(vf[i][0], u32x4{p01[E] & msk[1][0], p23[E] & msk[1][1], p01[E] & msk[2][0], p23[E] & msk[2][1]}, oa[E]);
      mfma_at(oa[E], vf[i][1], u32x4{p01[E] & msk[3][0], p23[E] & msk[3][1], p01[E] & msk[4][0], p23[E] & msk[4][1]});
    });
  };
  auto store_o = [&](auto J) {
    u32x4 op[2][2] = {{pk8(oa[0], 0), pk8(oa[0], 1)}, {pk8(oa[1], 0), pk8(oa[1], 1)}};
#pragma unroll
    for (int e = 0; e < 2; ++e) { auto t = __builtin_amdgcn_permlane32_swap(op[0][1][e], op[1][0][e], false, false); op[0][1][e] = t[0]; op[1][0][e] = t[1]; }
    const u32 o2 = ob + J * 3584;
    st128(op[0][0], c.rws, lane16, o2);
    st128(op[0][1], c.rws, lo16, o2 + 1024);
    st128(op[1][0], c.rws, lane16, o2 + 1536);
    st128(op[1][1], c.rws, lane16, o2 + 2560);
  };
  static_for<10, 16>(load_kv);
  wait_vm<7>();
  pin(kp[0]); pin(kp[1]);
  scores(IC<0>{});
  static_for<6, 10>(load_kv);
  softmax(IC<0>{});
  static_for<16, 22>(load_kv);
  wait_vm<6>();
  pv(IC<0>{});
  scores(IC<1>{});
  store_o(IC<0>{});
  softmax(IC<1>{});
  wait_vm<4>();
  pv(IC<1>{});
  store_o(IC<1>{});
  sync_vm<0>();
  if (w == 0) put_tag(tag, c.rws, lane == 0 ? (14 + hf) * 8 : OOB, WS_FLAG + 8192 + mt * 256);
  if (mt == 0 && hf == 0 && w == 0) {
#pragma unroll 1
    for (int i = 0; i < 4; ++i) spin(c.ws, WS_FLAG + 8192 + (i * 4 + (lane >> 4)) * 256 + (lane & 15) * 8, true, tag);
    put_tag(tag, c.rws, lane == 0 ? 0 : OOB, WS_E);
  }
}

__global__ __launch_bounds__(512, 2) void fused_kernel(const void* __restrict__ x, const void* __restrict__ wg, const void* __restrict__ wu,
                                                       const void* __restrict__ wd, const void* __restrict__ wo, const void* __restrict__ wqkv,
                                                       const void* __restrict__ gw, void* __restrict__ out, void* __restrict__ ws) {
  __shared__ __attribute__((aligned(1024))) char smem[163840];
  const int lane = threadIdx.x & 63, w = __builtin_amdgcn_readfirstlane(threadIdx.x >> 6);
  Ctx c;
  {
    u64 pc;
    asm volatile("s_getpc_b64 %0" : "=s"(pc));
    c.pf = touch((w == 6 && lane < 7) ? ((blockIdx.x >> 3) * 7 + lane) * 128 : OOB, mk((const void*)(pc & ~255ull), CODE_BYTES));
  }
  asm volatile("" ::"s"(x), "s"(wg), "s"(wu), "s"(wd), "s"(wo), "s"(wqkv), "s"(gw), "s"(out), "s"(ws));
  const int b = blockIdx.x, xc = b & 7, ix = b >> 3;
  const int mq = xc >> 1, mt = mq * 4 + (ix & 3), rl = ix >> 2, rh = xc & 1;
  const bool isa = rl < 7;
  const int role = rh * 7 + rl;
  c.sm = (LAS char*)smem;
  c.m0w = (u32)(size_t)c.sm + w * 1024;
  c.rx = mk(x, 2048 * 896); c.rqkv = mk(wqkv, 448 * 2688); c.rwg = mk(wg, 448 * 3584); c.rwu = mk(wu, 448 * 3584);
  c.rwd = mk(wd, 1792 * 896); c.rwo = mk(wo, 448 * 896); c.rws = mk(ws, WS_END);
  c.ws = (const char*)ws;
  {
    constexpr u64 mod7 = 0x1065432106543210ull;
    c.seq = (u32)(mod7 >> (4 * ((mod7 >> (4 * (2 * mq + 4 * rh + 2 * (rl & 1) + (ix & 1)))) & 7)));
    c.seq2 = (u32)(mod7 >> (4 * xc));
#pragma unroll
    for (int j = 0; j < 7; ++j) { u32 k = (c.seq >> (4 * j)) & 7; asm volatile("" : "+s"(k)); c.kc[j] = k; }
  }
  const int r8 = lane >> 3, c8 = lane & 7, R = 8 * w + r8;
  c.vx = (mt * 128 + 2 * R) * 896 + ((c8 ^ ((R >> 1) & 7)) << 4);
  const int sw = (c8 ^ ((r8 & 2) << 1)) << 4;
  const int isai = (rl - 7) >> 31;
  c.vkv = R * 2688 + ((896 + role * 128) & isai) + ((rh * 384) & ~isai) + sw;
  static_for<0, 2>([&](auto N) { issueA<N>(c); });
  if (__builtin_expect(isa, 1)) {
    c.vg = R * 3584 + role * 128 + sw;
    c.vu = R * 3584 + role * 128 + (sw ^ 32);
    c.vw2 = R * 896 + rl * 128 + sw;
    type_a(c, w, lane, mt, role, xc * 4 + (ix & 3), rl, out);
  } else {
    type_b(c, w, lane, mt, rh, gw);
  }
}

extern "C" void kernel_launch(void* const* d_in, const int* in_sizes, int n_in, void* d_out, int out_size, void* d_ws, size_t ws_size,
                              hipStream_t stream) {
  fused_kernel<<<256, 512, 0, stream>>>(d_in[0], d_in[4], d_in[5], d_in[6], d_in[2], d_in[1], d_in[7], d_out, d_ws);
}
```

```cpp
#include <hip/hip_runtime.h>
#include <hip/hip_bf16.h>

typedef unsigned long long u64;
typedef unsigned u32;
typedef __bf16 bf16x8 __attribute__((ext_vector_type(8)));
typedef __bf16 bf16x2 __attribute__((ext_vector_type(2)));
typedef short s16x4 __attribute__((ext_vector_type(4)));
typedef u32 u32x4 __attribute__((ext_vector_type(4)));
typedef u32 u32x2 __attribute__((ext_vector_type(2)));
typedef float f32x16 __attribute__((ext_vector_type(16)));
typedef float f32x4 __attribute__((ext_vector_type(4)));
typedef __amdgpu_buffer_rsrc_t rsrc_t;
#define LAS __attribute__((address_space(3)))
#define GAS __attribute__((address_space(1)))
#define DI __device__ __forceinline__
#define SCHED() __builtin_amdgcn_sched_barrier(0)
template <int V> using IC = std::integral_constant<int, V>;

constexpr u32 WS_E = 0;
constexpr u32 WS_FLAG = 4096;
constexpr u32 WS_OMG = 32768;
constexpr u32 WS_K = 65536;
constexpr u32 KV_T = 28672;
constexpr u32 WS_V = WS_K + 64 * KV_T;
constexpr u32 WS_A2 = WS_V + 64 * KV_T;
constexpr u32 A2_T = 143360;
constexpr u32 WS_END = WS_A2 + 64 * A2_T;
constexpr u32 OOB = 0xFFFFFF00u;
constexpr u32 CODE_BYTES = 25600;
constexpr int SC1 = 16;
constexpr int SLOT = 8192;
constexpr int SPIN_CAP = 1 << 15;

DI rsrc_t mk(const void* p, u32 bytes) { return __builtin_amdgcn_make_buffer_rsrc((void*)p, (short)0, (int)bytes, 0x00020000); }
template <u32 DST> DI void dma(u32 m0w, u32 voff, rsrc_t rs, u32 soff) {
  asm volatile("s_add_u32 m0, %0, %4\n\ts_nop 0\n\tbuffer_load_dwordx4 %1, %2, %3 offen lds" ::"s"(m0w), "v"(voff), "s"(rs), "s"(soff), "n"(DST) : "memory", "scc");
}
template <u32 DST, u32 MUL> DI void dmam(u32 m0w, u32 voff, rsrc_t rs, u32 k) {
  u32 t;
  asm volatile("s_add_u32 m0, %1, %5\n\ts_mul_i32 %0, %4, %6\n\tbuffer_load_dwordx4 %2, %3, %0 offen lds" : "=&s"(t) : "s"(m0w), "v"(voff), "s"(rs), "s"(k), "n"(DST), "n"(MUL) : "memory", "scc");
}
DI u32x4 ld128_sc1(u32 voff, rsrc_t rs, u32 soff) {
  u32x4 v;
  asm volatile("buffer_load_dwordx4 %0, %1, %2, %3 offen sc1" : "=v"(v) : "v"(voff), "s"(rs), "s"(soff) : "memory");
  return v;
}
DI u32x2 ld64_sc1(u32 voff, rsrc_t rs, u32 soff) {
  u32x2 v;
  asm volatile("buffer_load_dwordx2 %0, %1, %2, %3 offen sc1" : "=v"(v) : "v"(voff), "s"(rs), "s"(soff) : "memory");
  return v;
}
DI u32 ld32_sc1(u32 voff, rsrc_t rs, u32 soff) {
  u32 v;
  asm volatile("buffer_load_dword %0, %1, %2, %3 offen sc1" : "=v"(v) : "v"(voff), "s"(rs), "s"(soff) : "memory");
  return v;
}
DI u32 touch(u32 voff, rsrc_t rs) {
  u32 v;
  asm volatile("buffer_load_dword %0, %1, %2, 0 offen" : "=v"(v) : "v"(voff), "s"(rs) : "memory");
  return v;
}
DI void st128(u32x4 v, rsrc_t rs, u32 voff, u32 soff) {
  asm volatile("buffer_store_dwordx4 %0, %1, %2, %3 offen sc1\n\ts_nop 1" ::"v"(v), "v"(voff), "s"(rs), "s"(soff) : "memory");
}
constexpr int clampvm(int n) { return n > 63 ? 63 : n; }
template <int N> DI void wait_vm() { static_assert(N >= 0, ""); asm volatile("s_waitcnt vmcnt(%0)" ::"n"(clampvm(N)) : "memory"); }
template <int N> DI void sync_vm() { static_assert(N >= 0, ""); asm volatile("s_waitcnt vmcnt(%0)\n\ts_barrier" ::"n"(clampvm(N)) : "memory"); }
template <class T> DI void pin(T& v) { asm volatile("" : "+v"(v)); }
template <int I, int N, class Fn> DI void static_for(Fn&& f) {
  if constexpr (I < N) { f(IC<I>{}); static_for<I + 1, N>(f); }
}

DI u32x4 trfrag(LAS char* p) {
  s16x4 lo = __builtin_amdgcn_ds_read_tr16_b64_v4i16((LAS s16x4*)p);
  s16x4 hi = __builtin_amdgcn_ds_read_tr16_b64_v4i16((LAS s16x4*)(p + 512));
  u32x2 a = __builtin_bit_cast(u32x2, lo), b = __builtin_bit_cast(u32x2, hi);
  return u32x4{a.x, a.y, b.x, b.y};
}
DI f32x16 mfma(u32x4 a, u32x4 b, f32x16 c) {
  return __builtin_amdgcn_mfma_f32_32x32x16_bf16(__builtin_bit_cast(bf16x8, a), __builtin_bit_cast(bf16x8, b), c, 0, 0, 0);
}
DI void mfma_at(f32x16& c, u32x4 a, u32x4 b) { c = mfma(a, b, c); asm volatile("" : "+v"(c)); }
DI u32 pk(float a, float b) { bf16x2 c; c[0] = (__bf16)a; c[1] = (__bf16)b; return __builtin_bit_cast(u32, c); }
DI u32x4 pk8(const f32x16& v, int s) { return u32x4{pk(v[8 * s], v[8 * s + 1]), pk(v[8 * s + 2], v[8 * s + 3]), pk(v[8 * s + 4], v[8 * s + 5]), pk(v[8 * s + 6], v[8 * s + 7])}; }
DI float rbf(float a) { return (float)(__bf16)a; }
DI float sigmoidf(float z) { return __builtin_amdgcn_rcpf(1.f + __builtin_amdgcn_exp2f(z * -1.4426950408889634f)); }
DI float glu(float g, float u) { return u * sigmoidf(g); }
DI float xhalf_max(float v) { auto r = __builtin_amdgcn_permlane32_swap(__float_as_uint(v), __float_as_uint(v), false, false); return fmaxf(__uint_as_float(r[0]), __uint_as_float(r[1])); }
DI float xhalf_sum(float v) { auto r = __builtin_amdgcn_permlane32_swap(__float_as_uint(v), __float_as_uint(v), false, false); return __uint_as_float(r[0]) + __uint_as_float(r[1]); }

DI void spin(const char* ws, u32 off, bool need, u64 tag) {
  const GAS u64* p = (const GAS u64*)(ws + off);
#pragma unroll 1
  for (int i = 0; i < SPIN_CAP; ++i) {
    u64 v = __hip_atomic_load(p, __ATOMIC_RELAXED, __HIP_MEMORY_SCOPE_AGENT);
    if (__all(!need || v == tag)) break;
    __builtin_amdgcn_s_sleep(2);
  }
  asm volatile("" ::: "memory");
}
DI bool match(u32x2 v, bool need, u64 tag) { return __all(!need || (v.x == (u32)tag && v.y == (u32)(tag >> 32))); }
DI u64 tag_of(u32x2 ev) {
  u64 e = ((u64)__builtin_amdgcn_readfirstlane(ev.y) << 32) | (u32)__builtin_amdgcn_readfirstlane(ev.x);
  return e * 6364136223846793005ull + 1442695040888963407ull;
}
DI void put_tag(u64 tag, rsrc_t rs, u32 voff, u32 soff) { __builtin_amdgcn_raw_buffer_store_b64(u32x2{(u32)tag, (u32)(tag >> 32)}, rs, voff, soff, SC1); }

constexpr char HEAD[] = "KKKKKKK";
constexpr int BARS[] = {0, 2, 4, 6, 8, 10, 12, 16, 20, 34, 46, 50, 58, 62, 68, 76, 80, 86, 96};
constexpr int PRO_DMA = 9;
constexpr int HEAD_N = 14, HEAD_RATE = 24;
constexpr int EP1 = 2;
constexpr int U_KVPUB = 12, U_H1PUB = 40, U_H2PUB = 57, U_POLL1 = 52, U_CHK1 = 56, U_POLL2 = 64, U_CHK2 = 68, U_POLL3 = 70, U_CHK3 = 76;
constexpr int NV = 105, U0 = 14, IT_OMG = 28;
constexpr int imin(int a, int b) { return a < b ? a : b; }
constexpr int item_of(int i) { return i < IT_OMG ? i : i + 1; }
constexpr int ld_rel(int k) { return k < 6 ? 0 : 1 + (k - 6) / 2; }
constexpr int ld_iter(int it) { return U0 + (it < 14 ? U_CHK1 + ld_rel(it) : (it < 28 ? U_CHK2 + ld_rel(it - 14) : U_CHK3 + (it - 28) / 2)); }
constexpr int TM = 8, U_H2A = 56 - TM / 2 + 2;
constexpr bool tm_it(int u) { return u >= 56 - TM && u < 56; }
constexpr int g_tile(int u, int i) { return tm_it(u) ? (u - (56 - TM)) / (TM / 2) : i; }
constexpr int g_ks(int u, int i) { return tm_it(u) ? 56 - TM + 2 * ((u - (56 - TM)) % (TM / 2)) + i : u; }
constexpr int h_tile(int u, int i) { return u >= 85 ? (u - 85) / 3 : i; }
constexpr int h_blk(int u, int i) { return u >= 85 ? 29 + 2 * ((u - 85) % 3) + i : u - 56; }
constexpr int UE0 = 32, NE = (56 - TM - UE0) / 2;
struct Sched {
  int el[NV + 2] = {}, ej[NV + 2] = {};
  int nhead = 0, vkvst = 0, vg1 = 0;
  int xl[NV + 2] = {}, wl[NV + 2] = {};
  int kind[NV + 2] = {}, idx[NV + 2] = {}, lo[NV + 2] = {}, hi[NV + 2] = {};
  int pk[84] = {}, pi[84] = {}, pj[84] = {};
  bool bar[NV] = {};
  int cum[NV + 1] = {}, base[NV + 1] = {}, nst[NV] = {}, ldlo[NV + 1] = {}, cnt[NV] = {}, pdma[84] = {}, pld[36] = {}, freed[NV] = {};
  int cnt0 = 0; bool ok = true;
  constexpr Sched() {
    int v = 0, n = 0, k = 0, g = 0, ne = 0;
    for (int e = 0; e < NV + 2; ++e) el[e] = -1;
    int nx = 0;
    auto add = [&](int kd, int st, int nit, int nw) {
      const int first = n; int x = -1;
      if (st < 7 && st == nx) { x = n; for (int e = 0; e < 2; ++e) { pk[n] = 0; pi[n] = st; pj[n] = e; ++n; } ++nx; }
      for (int e = 0; e < nit; ++e) { kind[v] = kd; idx[v] = nit * st + e; lo[v] = first; hi[v] = n + nw - 1; xl[v] = x; wl[v] = n; ++v; }
      for (int e = 0; e < nw; ++e) { pk[n] = kd; pi[n] = st; pj[n] = kd ? e : 2; ++n; }
      if (kd == 1 && 4 * st >= UE0 && 4 * st < 56 - TM) {
        for (int e = 0; e < 4; ++e) { el[v - 4 + e] = hi[v - 4 + e] = n + e / 2; ej[v - 4 + e] = 2 * ne + e; }
        for (int e = 0; e < 2; ++e) { pk[n] = 2; pi[n] = ne++; ++n; }
      }
    };
    for (int i = 0; HEAD[i]; ++i) { if (HEAD[i] == 'K') { add(0, k++, 2, 1); nhead = v; } else { add(1, g++, 4, 2); if (g == 7) vg1 = v; } }
    if (k != 7 || g > 7) ok = false;
    vkvst = nhead + 1;
    while (g < 14) { add(1, g++, 4, 2); if (g == 7) vg1 = v; }
    if (ne != NE) ok = false;
    for (int i = 0; i < 35; ++i) { kind[v] = 2; idx[v] = 56 + i; lo[v] = wl[v] = n; if (i < NE) hi[v] = n - 1; else { hi[v] = n; pk[n] = 2; pi[n] = i; ++n; } ++v; }
    const int wa = wl[U0 + 56 - TM];
    for (int u = 56 - TM; u < 56; ++u) {
      wl[U0 + u] = wa + 2 * ((g_ks(u, 0) - (56 - TM)) / 4);
      lo[U0 + u] = g_tile(u, 0) == 0 ? wa : wl[U0 + u];
      hi[U0 + u] = g_tile(u, 0) == 0 ? wl[U0 + u] + 1 : wa + TM / 2 - 1;
    }
    for (int u = 85; u < 91; ++u) { lo[U0 + u] = wl[U0 + 85]; hi[U0 + u] = 83; }
    kind[NV] = kind[NV + 1] = 3; hi[NV] = hi[NV + 1] = 83;
    for (int e = 0; e + 1 < NV; ++e) if (lo[e] > lo[e + 1] || hi[e] > hi[e + 1]) ok = false;
    for (int e : BARS) bar[e] = true;
    bar[U0 + U_KVPUB] = bar[U0 + U_H1PUB] = bar[U0 + U_H2PUB] = bar[U0 + U_CHK2] = bar[U0 + U_CHK3] = true;
    if (U0 + U_KVPUB <= vkvst || U0 + U_H1PUB <= vg1 + 2 * EP1 || bar[U0 + U_CHK1]) ok = false;
    int fr = 0, cc = PRO_DMA, cr = 0, it = 0;
    cum[0] = cc; base[0] = PRO_DMA + 2;
    for (n = 0; n < PRO_DMA; ++n) pdma[n] = n;
    for (v = 0; v < NV; ++v) {
      const int u = v - U0;
      if (bar[v] || u == U_CHK1) fr = lo[v];
      freed[v] = fr;
      nst[v] = 3 * (v == vkvst) + (v == vg1 + EP1) + (v == vg1 + 2 * EP1) + (u == U_H2A) + (u == 56) + (u == U_POLL1) + (u == U_POLL2) + (u == U_POLL3);
      ldlo[v] = it;
      while (it < 36 && ld_iter(it) == v) { pld[it] = base[v] + nst[v] + (it - ldlo[v]); ++it; }
      cr += v < HEAD_N ? HEAD_RATE : 16;
      const int t = imin(imin(cc + imin(cr / 16, 2), fr + 20), 84);
      cr -= 16 * (t - cc); if (cr > 16) cr = 16;
      for (n = cc; n < t; ++n) pdma[n] = base[v] + nst[v] + (it - ldlo[v]) + (n - cc);
      base[v + 1] = base[v] + nst[v] + (it - ldlo[v]) + (t - cc);
      cc = t; cum[v + 1] = cc;
    }
    ldlo[NV] = it;
    if (it != 36 || cc != 84) ok = false;
    for (v = -1; v < NV; ++v) {
      if (v >= 0 && !bar[v]) continue;
      int nb = v + 1; while (nb < NV && !bar[nb]) ++nb;
      const int need = hi[nb + 1], at = v < 0 ? 0 : v;
      if (need >= cum[at]) ok = false;
      int c = base[at] - pdma[need] - 1;
      const int u = v - U0;
      if (u == U_KVPUB) c = imin(c, base[v] - (base[vkvst] + nst[vkvst]));
      if (u == U_H1PUB) c = imin(c, base[v] - (base[vg1 + 2 * EP1] + nst[vg1 + 2 * EP1]));
      if (u == U_H2PUB) c = imin(c, base[v] - (base[U0 + 56] + nst[U0 + 56]));
      if (v < 0) cnt0 = c; else cnt[v] = c;
    }
    for (int i = 1; i < 35; ++i) if (ld_iter(item_of(i)) >= U0 + 56 + i) ok = false;
  }
};
constexpr Sched SA{};
static_assert(HEAD[0] == 'K', "the common prologue requests x, x, K/V");
static_assert(SA.ok, "schedule: a piece or operand is used before it is requested, or HEAD is malformed");
static_assert(U_CHK1 == 56 && U_POLL1 < U_CHK1 && U_CHK1 < U_POLL2 && U_POLL2 < U_CHK2 && U_CHK2 < U_POLL3 && U_POLL3 < U_CHK3, "");
static_assert(U_KVPUB < U_H1PUB && U_H1PUB < U_CHK1 && U_H2PUB > 56 && U_H2PUB < U_CHK2, "a workgroup publishes a class before it waits for that class");
static_assert(ld_iter(35) < U0 + 85 && SA.freed[U0 + 88] >= 72, "the reduction scratch (slots 4..11 = pieces 64..71) must be free at the end");

struct Ctx {
  LAS char* sm; u32 m0w;
  rsrc_t rx, rqkv, rwg, rwu, rwd, rwo, rws;
  const char* ws;
  u32 pf;
  u32 kc[7], seq, seq2;
  u32 vx, vkv, vg, vu, vw2;
};

template <int I> DI u32 blk2(const Ctx& c) {
  return I < 28 ? (I / 14) * 14 + 2 * ((c.seq2 >> (4 * ((I % 14) / 2))) & 7) + (I & 1) : 28 + ((c.seq2 >> (4 * (I < 28 ? 0 : I - 28))) & 7);
}
template <int N> DI void issueA(const Ctx& c) {
  constexpr u32 dst = (N % 20) * SLOT;
  constexpr int k = SA.pk[N], st = SA.pi[N], j = SA.pj[N];
  if constexpr (k == 0) {
    if constexpr (j < 2) dmam<dst, 128>(c.m0w, c.vx + j * 896, c.rx, c.kc[st]);
    else dmam<dst, 64 * 2688>(c.m0w, c.vkv, c.rqkv, c.kc[st]);
  } else if constexpr (k == 1) {
    if constexpr (j == 0) dmam<dst, 64 * 3584>(c.m0w, c.vg + (st / 7) * 1792, c.rwg, c.kc[st % 7]);
    else dmam<dst, 64 * 3584>(c.m0w, c.vu + (st / 7) * 1792, c.rwu, c.kc[st % 7]);
  } else if constexpr (st < 28) dmam<dst, 64 * 896>(c.m0w, c.vw2, c.rwd, blk2<st>(c));
  else dmam<dst, 64 * 896>(c.m0w, c.vw2, c.rwo, blk2<st>(c) - 28);
}

DI void type_a(Ctx& c, int w, int lane, int mt, int role, int R2, int n2, void* out) {
  const int r = lane & 31, hi = lane >> 5, blk = (lane >> 4) & 1, q = (lane & 15) >> 2, p = lane & 3;
  const int sp = ((p & 1) << 1) | (p >> 1);
  const int rg = w & 3, cg = w >> 2, T = mt * 4 + rg;
  const bool isv = role >= 7;
  LAS char* sm = c.sm;
  static_for<3, PRO_DMA>([&](auto N) { issueA<N>(c); });
  const int run = (lane * 37) >> 8;
  u32 tg = touch(((w == 1 || w == 2) && lane < 28) ? ((mt >> 2) * 112 + (n2 * 4 + (mt & 3)) * 4 + run) * 3584 + (role - n2 + lane - 7 * run) * 128 : OOB,
                 w == 1 ? c.rwg : c.rwu);
  put_tag(0, c.rws, (w == 0 && lane < 3) ? lane * 4096 + role * 8 : OOB, WS_FLAG + mt * 256);
  asm volatile("" ::: "memory");
  const int R = 32 * (rg & 1) + r, xm = (R >> 1) & 7;
  int xa[4];
#pragma unroll
  for (int s = 0; s < 4; ++s) xa[s] = (rg >> 1) * SLOT + R * 128 + (((2 * s + hi) ^ xm) << 4);
  const int rowb = (8 * hi + q) * 128, q2 = q & 2;
  const int akv = rowb + (((2 * cg + blk) ^ q2) << 5) + (isv ? p : sp) * 8;
  int akvw[3];
#pragma unroll
  for (int k = 0; k < 3; ++k) { akvw[k] = akv + k * 65536; pin(akvw[k]); }
  int agu[2];
#pragma unroll
  for (int i = 0; i < 2; ++i) agu[i] = blk * SLOT + rowb + (((2 * cg + i) ^ q2 ^ blk) << 5) + sp * 8;
  int aguw[2][3];
#pragma unroll
  for (int i = 0; i < 2; ++i)
#pragma unroll
    for (int k = 0; k < 3; ++k) { aguw[i][k] = agu[i] + k * 65536; pin(aguw[i][k]); }
  const int rt = w & 1, kq = w >> 1;
  int aw2[2], aw2w[2][3];
#pragma unroll
  for (int i = 0; i < 2; ++i) aw2[i] = kq * 2048 + rowb + (((2 * i + blk) ^ q2) << 5) + p * 8;
  const int T2 = (R2 >> 1) * 4 + 2 * rt + (R2 & 1);

  u32x4 xf[28], wf[2][2], af[35], hp[2], wr[2 * NE];
  f32x16 akvacc = {}, g1[2] = {}, g2[2] = {}, ah[2] = {}, ao[2] = {};
  u32x2 ev, pollv; u32 omg; u64 tag;
  const u32 lane16 = lane * 16;
  const u32 poll_off = WS_FLAG + (R2 >> 1) * 256 + (lane & 15) * 8;
  const bool need_h = (lane & 15) < 14;
  const u32 a2_off = WS_A2 + T2 * A2_T + kq * 1024 + lane16;
  const int isvi = (6 - role) >> 31;
  u32 kv_off = ((WS_V + hi * 7168 + ((role - 7) * 64 + cg * 32 + r) * 16) & isvi) + ((WS_K + (role * 8 + cg * 4) * 512 + lane16) & ~isvi) + T * KV_T;
  pin(kv_off);
  const u32 kv_step = 1024 + (13312 & isvi);
  const u32 h_off = WS_A2 + T * A2_T + (role * 4 + 2 * cg) * 1024 + lane16;

  auto read_w = [&](auto V, auto I) {
    constexpr int v = V, i = I, k = SA.kind[v], u = SA.idx[v], sl = SA.wl[v] % 20;
    if constexpr (k < 2 && SA.xl[v] >= 0 && (k == 0 || i == 0)) {
      constexpr int ks = k == 0 ? 2 * u + i : u, xs = SA.xl[v] % 20;
      const int wrap = xs == 19 ? (rg >> 1) * (-20 * SLOT) : 0;
      xf[ks] = *(LAS u32x4*)(sm + xa[ks % 4] + wrap + xs * SLOT);
    }
    if constexpr (k == 0) {
      wf[v & 1][i] = trfrag(sm + akvw[sl / 8] + (sl % 8) * SLOT + ((2 * u + i) % 4) * 2048);
    } else if constexpr (k == 1) {
      const int wrap = sl == 19 ? blk * (-20 * SLOT) : 0;
      wf[v & 1][i] = trfrag(sm + aguw[g_tile(u, i)][sl / 8] + wrap + (sl % 8) * SLOT + (g_ks(u, i) % 4) * 2048);
      if constexpr (u == UE0 - 2) {
#pragma unroll
        for (int e = 0; e < 3; ++e) { aw2w[i][e] = aw2[i] + e * 65536; pin(aw2w[i][e]); }
      }
      if constexpr (i == 0 && SA.el[v] >= 0) { constexpr int es = SA.el[v] % 20, j = SA.ej[v]; wr[j] = trfrag(sm + aw2w[j & 1][es / 8] + (es % 8) * SLOT); }
    } else if constexpr (k == 2 && u - 56 >= NE) {
      constexpr int s2 = SA.wl[U0 + 56 + h_blk(u, i)] % 20;
      wf[v & 1][i] = trfrag(sm + aw2w[h_tile(u, i)][s2 / 8] + (s2 % 8) * SLOT);
    }
  };
  auto publish = [&](int cls) {
    if (w == 0) put_tag(tag, c.rws, lane == 0 ? role * 8 : OOB, WS_FLAG + cls * 4096 + mt * 256);
  };
  auto load_item = [&](auto IT) {
    constexpr int it = IT;
    if constexpr (it == IT_OMG) omg = ld32_sc1(r * 4, c.rws, WS_OMG + T2 * 128);
    else { constexpr int i = it < IT_OMG ? it : it - 1; af[i] = ld128_sc1(a2_off, c.rws, blk2<i>(c) * 4096); }
  };
  auto check = [&](auto CNT, int cls, bool need) {
    wait_vm<CNT>();
    pin(pollv);
    if (w == 0 && !match(pollv, need, tag)) spin(c.ws, poll_off + cls * 4096, need, tag);
  };

  sync_vm<SA.cnt0>();
  pin(c.pf);
  static_for<0, 4>([&](auto S) { read_w(IC<S / 2>{}, IC<S % 2>{}); });
  SCHED();
  auto h_pair = [&](const f32x16& gu, int k) { u32 hv = pk(glu(gu[2 * k], gu[8 + 2 * k]), glu(gu[2 * k + 1], gu[9 + 2 * k])); pin(hv); return hv; };
  LAS char* red = sm + 4 * SLOT;
  int rda[2]; u32 out_off;
  auto put_partial = [&](auto TT, auto A) {
    constexpr int t = TT, a = A;
    f32x4 v;
#pragma unroll
    for (int e = 0; e < 4; ++e) v[e] = ah[t][4 * a + e] * __uint_as_float(omg) + ao[t][4 * a + e];
    *(LAS f32x4*)(red + kq * 16384 + (2 * r + rt) * 256 + (((8 * t + 2 * a + hi) ^ (r & 15)) << 4)) = v;
  };
  auto iter = [&](auto V, auto ISV) {
    constexpr int v = V, b = v & 1, kd = SA.kind[v], ui = SA.idx[v], u = v - U0;
    constexpr bool nbar = v + 1 < NV && SA.bar[v + 1 < NV ? v + 1 : v];
    constexpr int nld = SA.ldlo[v + 1] - SA.ldlo[v], nd = SA.cum[v + 1] - SA.cum[v];
    if constexpr (SA.bar[v]) sync_vm<SA.cnt[v]>();
    if constexpr (u == 56) {
#pragma unroll
      for (int j = 0; j < 8; j += 2) hp[1][j / 2] = pk(glu(g2[1][j], g2[1][8 + j]), glu(g2[1][j + 1], g2[1][9 + j]));
      st128(hp[1], c.rws, h_off, 57 * 1024);
      SCHED();
      check(IC<SA.base[v] + SA.nst[v] - SA.base[U0 + U_POLL1] - 1>{}, 1, need_h);
      asm volatile("s_barrier" ::: "memory");
      static_for<SA.ldlo[v], SA.ldlo[v + 1]>(load_item);
    }
    if constexpr (u >= 56 && u < 88) {
      wait_vm<SA.base[v] + (u == 56 ? SA.nst[v] + nld : 0) - SA.pld[item_of(h_blk(u, 1))] - 1>();
      pin(af[h_blk(u, 0)]);
      if constexpr (u >= 85) pin(af[h_blk(u, 1)]);
      if constexpr (u == 84) pin(omg);
    }
    SCHED();
    static_for<0, 2>([&](auto I) {
      constexpr int i = I;
      if constexpr (kd == 0) { if constexpr (ISV) mfma_at(akvacc, xf[2 * ui + i], wf[b][i]); else mfma_at(akvacc, wf[b][i], xf[2 * ui + i]); }
      else if constexpr (ui < 28) mfma_at(g1[i], wf[b][i], xf[ui]);
      else if constexpr (ui < 56) mfma_at(g2[g_tile(ui, i)], wf[b][i], xf[g_ks(ui, i) - 28]);
      else if constexpr (ui - 56 < NE) mfma_at(ah[i], wr[2 * (ui - 56) + i], af[ui - 56]);
      else if constexpr (ui < 84) mfma_at(ah[i], wf[b][i], af[ui - 56]);
      else mfma_at(ao[h_tile(ui, i)], wf[b][i], af[h_blk(ui, i)]);
      if constexpr (i == 0 && SA.el[v] >= 0) pin(wr[SA.ej[v] + i]);
      SCHED();
      if constexpr (i == 0 && v > 0 && SA.bar[v]) read_w(IC<v + 1>{}, IC<1>{});
      if constexpr (i == 0 || !nbar) read_w(IC<v + 2>{}, I);
      if constexpr (i == 0) {
        if constexpr (u == U_KVPUB) { pin(ev); pin(tg); tag = tag_of(ev); publish(0); }
        if constexpr (u == U_H1PUB) publish(1);
        if constexpr (u == U_H2PUB) publish(2);
        if constexpr (v == SA.vkvst) ev = ld64_sc1(w == 0 ? WS_E : OOB, c.rws, 0);
        if constexpr (u == U_POLL1) pollv = ld64_sc1(w == 0 ? poll_off : OOB, c.rws, 4096);
        if constexpr (u == U_POLL2 || u == U_POLL3) pollv = ld64_sc1(w == 0 ? poll_off : OOB, c.rws, 8192);
        if constexpr (v == SA.vkvst) { st128(pk8(akvacc, 0), c.rws, kv_off, 0); st128(pk8(akvacc, 1), c.rws, kv_off, kv_step); }
        if constexpr (v == SA.vg1 + EP1) st128(hp[0], c.rws, h_off, 0);
        if constexpr (v == SA.vg1 + 2 * EP1) st128(hp[1], c.rws, h_off, 1024);
        if constexpr (u == U_H2A) st128(hp[0], c.rws, h_off, 56 * 1024);
        if constexpr (u != 56) static_for<SA.ldlo[v], SA.ldlo[v + 1]>(load_item);
      }
      if constexpr (v >= SA.vg1 && v < SA.vg1 + 2 * EP1) {
        constexpr int t = (v - SA.vg1) / EP1, per = 4 / EP1, k0 = ((v - SA.vg1) % EP1) * per;
        constexpr int ka = nbar ? (i ? per : 0) : (i * per + 1) / 2, kb = nbar ? per : ((i + 1) * per + 1) / 2;
#pragma unroll
        for (int k = k0 + ka; k < k0 + kb; ++k) hp[t][k] = h_pair(g1[t], k);
      }
      if constexpr (u >= U_H2A - 2 && u < U_H2A) hp[0][2 * (u - (U_H2A - 2)) + i] = h_pair(g2[0], 2 * (u - (U_H2A - 2)) + i);
      if constexpr (u == 86 && i == 0) {
        const int row = w * 8 + (lane >> 3), c8 = lane & 7, m = (row >> 1) & 15;
        rda[0] = 4 * SLOT + row * 256 + (((2 * c8) ^ m) << 4); rda[1] = rda[0] ^ 16;
        out_off = (R2 * 64 + row) * 896 + n2 * 128 + c8 * 16;
        pin(rda[0]); pin(rda[1]); pin(out_off);
      }
      if constexpr (u >= 88 && 2 * (u - 88) + i >= 1 && 2 * (u - 88) + i <= 4) put_partial(IC<0>{}, IC<2 * (u - 88) + i - 1>{});
      if constexpr (i == 0 && nd == 2) issueA<SA.cum[v]>(c);
      if constexpr (i == (nbar ? 0 : 1) && nd >= 1) issueA<SA.cum[v + 1] - 1>(c);
      if constexpr (i == 0 && u + 1 == U_CHK2) check(IC<SA.base[v + 1] - SA.base[U0 + U_POLL2] - 1 + i>{}, 2, need_h);
      if constexpr (i == 0 && u + 1 == U_CHK3) check(IC<SA.base[v + 1] - SA.base[U0 + U_POLL3] - 1 + i>{}, 2, !need_h);
      SCHED();
    });
  };
  if (isv) static_for<0, SA.nhead>([&](auto V) { iter(V, IC<1>{}); });
  else static_for<0, SA.nhead>([&](auto V) { iter(V, IC<0>{}); });
  static_for<SA.nhead, NV>([&](auto V) { iter(V, IC<0>{}); });
  static_for<0, 4>([&](auto A) { put_partial(IC<1>{}, A); });
  asm volatile("s_waitcnt lgkmcnt(0)\n\ts_barrier" ::: "memory");
  f32x4 ps[8];
#pragma unroll
  for (int k = 0; k < 8; ++k) ps[k] = *(LAS f32x4*)(sm + rda[k & 1] + (k >> 1) * 16384);
  SCHED();
  const f32x4 s0 = (ps[0] + ps[2]) + (ps[4] + ps[6]), s1 = (ps[1] + ps[3]) + (ps[5] + ps[7]);
  const u32x4 o = {pk(s0[0], s0[1]), pk(s0[2], s0[3]), pk(s1[0], s1[1]), pk(s1[2], s1[3])};
  __builtin_amdgcn_raw_buffer_store_b128(o, mk(out, 2048 * 896), out_off, 0, SC1);
}

constexpr int PRO_B = 12, GB_TOUCH = 18, GB_POLL = 25;
constexpr int cumB(int g) {
  int cc = PRO_B;
  for (int j = 0; j <= g; ++j) cc = imin(imin(cc + 2, 6 * ((j + 1) / 4) + 18), 42);
  return cc;
}
static_assert(cumB(GB_TOUCH - 1) == 42 && GB_POLL > 23, "both sit behind the last ring piece; the poll behind the last counted barrier");
template <int N> DI void issueB(const Ctx& c) {
  constexpr int kc = N / 6, j = N % 6;
  constexpr u32 dst = (N % 18) * SLOT;
  if constexpr (j < 2) dmam<dst, 128>(c.m0w, c.vx + j * 896, c.rx, c.kc[kc]);
  else dmam<dst, 64 * 2688>(c.m0w, c.vkv + (j - 2) * 128, c.rqkv, c.kc[kc]);
}

DI void type_b(Ctx& c, int w, int lane, int mt, int hf, const void* gw) {
  const int r = lane & 31, hi = lane >> 5, blk = (lane >> 4) & 1, q = (lane & 15) >> 2, p = lane & 3;
  const int sp = ((p & 1) << 1) | (p >> 1);
  const int rg = w & 3, cg = w >> 2, T = mt * 4 + rg;
  LAS char* sm = c.sm;
  if (w == 0) dma<18 * SLOT>(c.m0w, ((c.seq >> (4 * (lane >> 3))) & 7) * 128 + (lane & 7) * 16, mk(gw, 896), 0);
  static_for<3, PRO_B>([&](auto N) { issueB<N>(c); });
  if (w == 0) put_tag(0, c.rws, lane == 0 ? (14 + hf) * 8 : OOB, WS_FLAG + 8192 + mt * 256);
  const int R = 32 * (rg & 1) + r, xm = (R >> 1) & 7;
  int xa[4], aq[4];
#pragma unroll
  for (int s = 0; s < 4; ++s) xa[s] = (rg >> 1) * SLOT + R * 128 + (((2 * s + hi) ^ xm) << 4);
  const int h0 = 8 * hf + 4 * cg;
#pragma unroll
  for (int i = 0; i < 4; ++i) {
    const int col = 28 * (h0 + i) - 4 * (i & 1) - 192 * hf + 16 * blk + 4 * sp;
    aq[i] = (2 + (col >> 6)) * SLOT + (8 * hi + q) * 128 + (((col & 63) * 2) ^ ((q & 2) << 5));
  }
  const bool first = (mt == 0) && !(rg & 1);
  const int Tp = first ? T : ((rg & 1) ? T - 1 : T - 3);
  const u32 lane16 = lane * 16, lo16 = hi ? OOB : lane16;
  const u32 ob = WS_A2 + T * A2_T + (1792 + 28 * h0) * 64;
  const u32 omg_off = (hf == 0 && cg == 0 && hi == 0) ? r * 4 : OOB;
  const u32 poll_off = WS_FLAG + ((lane & 16) && mt > 0 ? mt - 1 : mt) * 256 + (lane & 15) * 8;
  const bool poll_need = (lane & 15) < 14;
  u32x4 xf[2], gf[2], wf[2][4];
  u32x4 kf[4][2], kp[2], vf[4][2], vp[4];
  f32x16 qa[4] = {};
  u32x2 ev, pollv;
  u32 tw;
  const int sr = (r & 19) | ((r & 4) << 1) | ((r & 8) >> 1);
  const u32 kp_off = (28 * (r >> 3) - 4 * ((r >> 3) & 1)) * 64 + (hi * 32 + 24 + (r & 7)) * 16;
  const u32 v_off = hi * 7168 + sr * 16;
  auto load_kv = [&](auto IT) {
    constexpr int it = IT;
    if constexpr (it < 2) kp[it] = ld128_sc1(kp_off, c.rws, WS_K + Tp * KV_T + 28 * h0 * 64 + it * 1024);
    else if constexpr (it < 10) {
      constexpr int i = (it - 2) / 2, k = it & 1;
      kf[i][k] = ld128_sc1(lane16, c.rws, WS_K + T * KV_T + (28 * (h0 + i) - 4 * (i & 1)) * 64 + k * 1024);
    } else {
      constexpr int i = (it - 10) / 3, k = (it - 10) % 3;
      const int wh = 28 * (h0 + i) - 4 * (i & 1);
      if constexpr (k == 0) vp[i] = ld128_sc1(v_off, c.rws, WS_V + Tp * KV_T + 14336 + wh * 16);
      else vf[i][k - 1] = ld128_sc1(v_off, c.rws, WS_V + T * KV_T + (k - 1) * 14336 + wh * 16);
    }
  };
  auto read_x = [&](auto G) {
    constexpr int g = G;
    if constexpr (g < 28) {
      xf[g & 1] = *(LAS u32x4*)(sm + xa[g % 4] + ((6 * (g / 4)) % 18) * SLOT);
      gf[g & 1] = *(LAS u32x4*)(sm + 18 * SLOT + g * 32 + hi * 16);
    }
  };
  auto read_w = [&](auto G, auto I) {
    constexpr int g = G;
    if constexpr (g < 28) wf[g & 1][I] = trfrag(sm + aq[I] + ((6 * (g / 4)) % 18) * SLOT + (g % 4) * 2048);
  };
  sync_vm<PRO_B - 6>();
  pin(c.pf);
  read_x(IC<0>{});
  static_for<0, 4>([&](auto I) { read_w(IC<0>{}, I); });
  SCHED();
  static_for<0, 28>([&](auto G) {
    constexpr int g = G, b = g & 1;
    if constexpr (g % 4 == 3 && g < 27) { asm volatile("s_waitcnt lgkmcnt(0)" ::: "memory"); sync_vm<cumB(g - 1) - 6 * (g / 4 + 2) + (g > GB_TOUCH)>(); }
    if constexpr (g == GB_POLL) { ev = ld64_sc1(w == 0 ? WS_E : OOB, c.rws, 0); pollv = ld64_sc1(w == 0 ? poll_off : OOB, c.rws, 0); }
    if constexpr (g == GB_TOUCH) {
      const int gi = (mt * 2 + hf) * 8 + w;
      tw = touch(((gi < 196 ? gi : gi - 196) * 64 + lane) * 128, gi < 196 ? c.rwd : c.rwo);
    }
    SCHED();
    static_for<0, 4>([&](auto I) {
      constexpr int i = I;
      mfma_at(qa[i], wf[b][i], xf[b]);
      SCHED();
      if constexpr (i == 0) read_x(IC<g + 1>{});
      read_w(IC<g + 1>{}, I);
      if constexpr (i < 2) {
        wf[b][2][2 * i] = r == 28 ? gf[b][2 * i] : wf[b][2][2 * i];
        wf[b][2][2 * i + 1] = r == 28 ? gf[b][2 * i + 1] : wf[b][2][2 * i + 1];
        pin(wf[b][2]);
      }
      if constexpr (i >= 2 && cumB(g - 1) + i - 2 < cumB(g)) issueB<cumB(g - 1) + i - 2>(c);
      SCHED();
    });
  });
  wait_vm<0>();
  pin(pollv); pin(ev); pin(tw);
  const u64 tag = tag_of(ev);
  if (w == 0 && !match(pollv, poll_need, tag)) spin(c.ws, poll_off, poll_need, tag);
  asm volatile("s_barrier" ::: "memory");
  static_for<0, 6>(load_kv);
  SCHED();
  const float z = rbf(__uint_as_float(__builtin_amdgcn_permlane32_swap(__float_as_uint(qa[2][12]), __float_as_uint(qa[2][12]), false, false)[1]));
  const float gate = sigmoidf(z);
  const u32 omg = __float_as_uint(sigmoidf(-z));
  int ab[4];
  u32 msk[5][2];
#pragma unroll
  for (int b = 0; b < 4; ++b) ab[b] = (r >> 3) - ((r & 7) < 4 * hi + b ? 1 : 0);
#pragma unroll
  for (int a = 0; a < 5; ++a)
#pragma unroll
    for (int e = 0; e < 2; ++e) msk[a][e] = (ab[2 * e] == a - 1 ? 0xFFFFu : 0u) | (ab[2 * e + 1] == a - 1 ? 0xFFFF0000u : 0u);
  u32 p01[2], p23[2];
  u32x4 qf[4][2];
  static_for<0, 4>([&](auto I) {
    constexpr int i = I;
    qf[i][0] = pk8(qa[i], 0); qf[i][1] = pk8(qa[i], 1);
    if (i & 1) { qf[i][0][0] = hi ? qf[i][0][0] : 0u; qf[i][0][1] = hi ? qf[i][0][1] : 0u; }
    else       { qf[i][1][2] = hi ? 0u : qf[i][1][2]; qf[i][1][3] = hi ? 0u : qf[i][1][3]; }
    pin(qf[i][0]); pin(qf[i][1]);
  });
#pragma unroll
  for (int a = 0; a < 5; ++a) { pin(msk[a][0]); pin(msk[a][1]); }
  __builtin_amdgcn_raw_buffer_store_b32(omg, c.rws, omg_off, WS_OMG + T * 128, SC1);
  asm volatile("" ::: "memory");
  f32x16 so[2], sv[2], oa[2];
  auto scores = [&](auto J) {
    static_for<0, 2>([&](auto E) {
      constexpr int i = 2 * J + E;
      pin(kf[i][0]); pin(kf[i][1]);
      so[E] = mfma(kf[i][0], qf[i][0], f32x16{}); sv[E] = mfma(kp[0], qf[i][0], f32x16{});
      mfma_at(so[E], kf[i][1], qf[i][1]); mfma_at(sv[E], kp[1], qf[i][1]);
    });
  };
  auto softmax = [&](auto J) {
    static_for<0, 2>([&](auto E) {
      constexpr int i = 2 * J + E;
      float v[4];
#pragma unroll
      for (int b = 0; b < 4; ++b) {
        float t = first ? -1e30f : sv[E][4 * i + b];
#pragma unroll
        for (int a = 0; a < 4; ++a) t = ab[b] == a ? so[E][4 * a + b] : t;
        v[b] = rbf(rbf(t) * 0.18898223650461362f);
      }
      const float mx = xhalf_max(fmaxf(fmaxf(v[0], v[1]), fmaxf(v[2], v[3])));
#pragma unroll
      for (int b = 0; b < 4; ++b) v[b] = __builtin_amdgcn_exp2f((v[b] - mx) * 1.4426950408889634f);
      const float sc = gate * __builtin_amdgcn_rcpf(xhalf_sum((v[0] + v[1]) + (v[2] + v[3])));
      p01[E] = pk(v[0] * sc, v[1] * sc); p23[E] = pk(v[2] * sc, v[3] * sc);
    });
    pin(p01[0]); pin(p23[0]); pin(p01[1]); pin(p23[1]);
  };
  auto pv = [&](auto J) {
    static_for<0, 2>([&](auto E) {
      constexpr int i = 2 * J + E;
      pin(vp[i]); pin(vf[i][0]); pin(vf[i][1]);
      oa[E] = mfma(vp[i], u32x4{0u, 0u, p01[E] & msk[0][0], p23[E] & msk[0][1]}, f32x16{});
      oa[E] = mfma(vf[i][0], u32x4{p01[E] & msk[1][0], p23[E] & msk[1][1], p01[E] & msk[2][0], p23[E] & msk[2][1]}, oa[E]);
      mfma_at(oa[E], vf[i][1], u32x4{p01[E] & msk[3][0], p23[E] & msk[3][1], p01[E] & msk[4][0], p23[E] & msk[4][1]});
    });
  };
  auto store_o = [&](auto J) {
    u32x4 op[2][2] = {{pk8(oa[0], 0), pk8(oa[0], 1)}, {pk8(oa[1], 0), pk8(oa[1], 1)}};
#pragma unroll
    for (int e = 0; e < 2; ++e) { auto t = __builtin_amdgcn_permlane32_swap(op[0][1][e], op[1][0][e], false, false); op[0][1][e] = t[0]; op[1][0][e] = t[1]; }
    const u32 o2 = ob + J * 3584;
    st128(op[0][0], c.rws, lane16, o2);
    st128(op[0][1], c.rws, lo16, o2 + 1024);
    st128(op[1][0], c.rws, lane16, o2 + 1536);
    st128(op[1][1], c.rws, lane16, o2 + 2560);
  };
  static_for<10, 16>(load_kv);
  wait_vm<7>();
  pin(kp[0]); pin(kp[1]);
  scores(IC<0>{});
  static_for<6, 10>(load_kv);
  softmax(IC<0>{});
  static_for<16, 22>(load_kv);
  wait_vm<6>();
  pv(IC<0>{});
  scores(IC<1>{});
  store_o(IC<0>{});
  softmax(IC<1>{});
  wait_vm<4>();
  pv(IC<1>{});
  store_o(IC<1>{});
  sync_vm<0>();
  if (w == 0) put_tag(tag, c.rws, lane == 0 ? (14 + hf) * 8 : OOB, WS_FLAG + 8192 + mt * 256);
  if (mt == 0 && hf == 0 && w == 0) {
#pragma unroll 1
    for (int i = 0; i < 4; ++i) spin(c.ws, WS_FLAG + 8192 + (i * 4 + (lane >> 4)) * 256 + (lane & 15) * 8, true, tag);
    put_tag(tag, c.rws, lane == 0 ? 0 : OOB, WS_E);
  }
}

__global__ __launch_bounds__(512, 2) void fused_kernel(const void* __restrict__ x, const void* __restrict__ wg, const void* __restrict__ wu,
                                                       const void* __restrict__ wd, const void* __restrict__ wo, const void* __restrict__ wqkv,
                                                       const void* __restrict__ gw, void* __restrict__ out, void* __restrict__ ws) {
  __shared__ __attribute__((aligned(1024))) char smem[163840];
  const int lane = threadIdx.x & 63, w = __builtin_amdgcn_readfirstlane(threadIdx.x >> 6);
  Ctx c;
  {
    u64 pc;
    asm volatile("s_getpc_b64 %0" : "=s"(pc));
    c.pf = touch((w == 6 && lane < 7) ? ((blockIdx.x >> 3) * 7 + lane) * 128 : OOB, mk((const void*)(pc & ~255ull), CODE_BYTES));
    SCHED();
  }
  const int b = blockIdx.x, xc = b & 7, ix = b >> 3;
  const int mq = xc >> 1, mt = mq * 4 + (ix & 3), rl = ix >> 2, rh = xc & 1;
  const bool isa = rl < 7;
  const int role = rh * 7 + rl;
  c.sm = (LAS char*)smem;
  c.m0w = (u32)(size_t)c.sm + w * 1024;
  {
    constexpr u64 mod7 = 0x1065432106543210ull;
    c.seq = (u32)(mod7 >> (4 * ((mod7 >> (4 * (2 * mq + 4 * rh + 2 * (rl & 1) + (ix & 1)))) & 7)));
    c.seq2 = (u32)(mod7 >> (4 * xc));
#pragma unroll
    for (int j = 0; j < 7; ++j) { u32 k = (c.seq >> (4 * j)) & 7; asm volatile("" : "+s"(k)); c.kc[j] = k; }
  }
  const int r8 = lane >> 3, c8 = lane & 7, R = 8 * w + r8;
  c.vx = (mt * 128 + 2 * R) * 896 + ((c8 ^ ((R >> 1) & 7)) << 4);
  const int sw = (c8 ^ ((r8 & 2) << 1)) << 4;
  const int isai = (rl - 7) >> 31;
  c.vkv = R * 2688 + ((896 + role * 128) & isai) + ((rh * 384) & ~isai) + sw;
  pin(c.vx); pin(c.vkv);
  SCHED();
  asm volatile("" : "+s"(x), "+s"(wg), "+s"(wu), "+s"(wd), "+s"(wo), "+s"(wqkv), "+s"(gw), "+s"(out), "+s"(ws));
  c.rx = mk(x, 2048 * 896); c.rqkv = mk(wqkv, 448 * 2688); c.rwg = mk(wg, 448 * 3584); c.rwu = mk(wu, 448 * 3584);
  c.rwd = mk(wd, 1792 * 896); c.rwo = mk(wo, 448 * 896); c.rws = mk(ws, WS_END);
  c.ws = (const char*)ws;
  static_for<0, 3>([&](auto N) { issueA<N>(c); });
  if (__builtin_expect(isa, 1)) {
    c.vg = R * 3584 + role * 128 + sw;
    c.vu = R * 3584 + role * 128 + (sw ^ 32);
    c.vw2 = R * 896 + rl * 128 + sw;
    type_a(c, w, lane, mt, role, xc * 4 + (ix & 3), rl, out);
  } else {
    type_b(c, w, lane, mt, rh, gw);
  }
}

extern "C" void kernel_launch(void* const* d_in, const int* in_sizes, int n_in, void* d_out, int out_size, void* d_ws, size_t ws_size,
                              hipStream_t stream) {
  fused_kernel<<<256, 512, 0, stream>>>(d_in[0], d_in[4], d_in[5], d_in[6], d_in[2], d_in[1], d_in[7], d_out, d_ws);
}
```
